```python
import torch

def _softmax(x, axis=-1):
    return torch.softmax(x, dim=axis)
def _gelu(x):
    return torch.nn.functional.gelu(x, approximate="tanh")
def _silu(x):
    return torch.nn.functional.silu(x)
def _sigmoid(x):
    return torch.sigmoid(x)
def _layernorm(x, axis=-1, eps=1e-5):
    m = x.mean(dim=axis, keepdim=True)
    v = x.var(dim=axis, keepdim=True, unbiased=False)
    return (x - m) / torch.sqrt(v + eps)
def _scatter_add(tgt, idx, src, axis=0):
    return tgt.clone().index_add_(axis, idx, src)
def _segment_sum(src, seg, n):
    out = torch.zeros((n,) + src.shape[1:], dtype=src.dtype)
    return out.index_add_(0, seg, src)
def _pad(x, pw):
    flat = tuple(v for pair in reversed(pw) for v in pair)
    return torch.nn.functional.pad(x, flat)

kernel_name = 'standalone__gqa_softmax__near_ridge'

def setup_inputs():

    _w = (lambda a: (lambda t: t.to(torch.bfloat16) if t.is_floating_point() else t)(torch.as_tensor(a)))
    B, Hq, Hkv, Sq, Sk, D = 8, 32, 32, 736, 736, 128
    return {
        "q": _w((lambda *s: torch.randn(*s, dtype=torch.bfloat16) * 0.8)(B, Hq, Sq, D)),
        "k": _w((lambda *s: torch.randn(*s, dtype=torch.bfloat16) * 0.8)(B, Hkv, Sk, D)),
        "v": _w((lambda *s: torch.randn(*s, dtype=torch.bfloat16) * 0.05)(B, Hkv, Sk, D)),
    }

def reference(q, k, v):
    B, Hq, Sq, D = q.shape
    Hkv = k.shape[1]
    rep = Hq // Hkv
    k = (lambda x, n, axis: torch.repeat_interleave(x, n, dim=axis))(k, rep, axis=1)
    v = (lambda x, n, axis: torch.repeat_interleave(x, n, dim=axis))(v, rep, axis=1)
    scale = D ** -0.5
    scores = torch.einsum("bhqd,bhkd->bhqk", q, k) * scale
    p = _softmax(scores, axis=-1)
    return torch.einsum("bhqk,bhkd->bhqd", p, v)
```

```cpp
#include <hip/hip_runtime.h>
#include <hip/hip_bf16.h>

typedef float    f32x16 __attribute__((ext_vector_type(16)));
typedef __bf16   bf16x8 __attribute__((ext_vector_type(8)));
typedef __bf16   bf16x2 __attribute__((ext_vector_type(2)));
typedef float    f32x2  __attribute__((ext_vector_type(2)));
typedef unsigned u32x4  __attribute__((ext_vector_type(4)));
typedef unsigned u32x2  __attribute__((ext_vector_type(2)));

constexpr int S = 736, D = 128, NH = 256, NT = S / 32;

__device__ __forceinline__ unsigned cvtpk(float lo, float hi) {
  f32x2 v = {lo, hi}; bf16x2 b = __builtin_convertvector(v, bf16x2); return __builtin_bit_cast(unsigned, b);
}

__device__ __forceinline__ void safe_block(const unsigned short* __restrict__ Q, const unsigned short* __restrict__ K,
                                           const unsigned short* __restrict__ V, unsigned short* __restrict__ O,
                                           int head, int qb, int lane) {
  const int r = lane & 31, h = lane >> 5;
  const float c = 0.12751743082f;
  const unsigned short* q = Q + ((size_t)head * S + qb * 32 + r) * D + 8 * h;
  const unsigned short* k = K + (size_t)head * S * D;
  const unsigned short* v = V + (size_t)head * S * D;
  bf16x8 qf[8];
#pragma unroll
  for (int s = 0; s < 8; ++s) qf[s] = *(const bf16x8*)(q + 16 * s);
  f32x16 o[4] = {};
  float m = -INFINITY, l = 0.f;
  for (int t = 0; t < NT; ++t) {
    f32x16 sc = {};
#pragma unroll
    for (int s = 0; s < 8; ++s) {
      bf16x8 kf = *(const bf16x8*)(k + (size_t)(t * 32 + r) * D + 16 * s + 8 * h);
      sc = __builtin_amdgcn_mfma_f32_32x32x16_bf16(kf, qf[s], sc, 0, 0, 0);
    }
    float mx = sc[0];
#pragma unroll
    for (int i = 1; i < 16; ++i) mx = fmaxf(mx, sc[i]);
    mx = fmaxf(mx, __shfl_xor(mx, 32));
    const float mn = fmaxf(m, mx);
    const float al = __builtin_amdgcn_exp2f((m - mn) * c);
    m = mn;
    float ps = 0.f;
#pragma unroll
    for (int i = 0; i < 16; ++i) { sc[i] = __builtin_amdgcn_exp2f((sc[i] - mn) * c); ps += sc[i]; }
    l = l * al + ps;
#pragma unroll
    for (int db = 0; db < 4; ++db)
#pragma unroll
      for (int i = 0; i < 16; ++i) o[db][i] *= al;
#pragma unroll
    for (int ks = 0; ks < 2; ++ks) {
      u32x4 pw;
#pragma unroll
      for (int i = 0; i < 4; ++i) pw[i] = cvtpk(sc[8 * ks + 2 * i], sc[8 * ks + 2 * i + 1]);
      const bf16x8 pf = __builtin_bit_cast(bf16x8, pw);
#pragma unroll
      for (int db = 0; db < 4; ++db) {
        u32x4 vw;
#pragma unroll
        for (int i = 0; i < 4; ++i) {
          const int j0 = 2 * i, j1 = 2 * i + 1;
          const int k0 = t * 32 + 16 * ks + 8 * (j0 >> 2) + 4 * h + (j0 & 3);
          const int k1 = t * 32 + 16 * ks + 8 * (j1 >> 2) + 4 * h + (j1 & 3);
          vw[i] = (unsigned)v[(size_t)k0 * D + 32 * db + r] | ((unsigned)v[(size_t)k1 * D + 32 * db + r] << 16);
        }
        o[db] = __builtin_amdgcn_mfma_f32_32x32x16_bf16(__builtin_bit_cast(bf16x8, vw), pf, o[db], 0, 0, 0);
      }
    }
  }
  l += __shfl_xor(l, 32);
  const float il = 1.f / l;
  unsigned short* op = O + ((size_t)head * S + qb * 32 + r) * D;
#pragma unroll
  for (int db = 0; db < 4; ++db)
#pragma unroll
    for (int g = 0; g < 4; ++g) {
      u32x2 w = {cvtpk(o[db][4 * g] * il, o[db][4 * g + 1] * il), cvtpk(o[db][4 * g + 2] * il, o[db][4 * g + 3] * il)};
      *(u32x2*)(op + 32 * db + 8 * g + 4 * h) = w;
    }
}

__global__ __launch_bounds__(64) void attn_base(const unsigned short* __restrict__ Q, const unsigned short* __restrict__ K,
                                                const unsigned short* __restrict__ V, unsigned short* __restrict__ O) {
  safe_block(Q, K, V, O, blockIdx.x / NT, blockIdx.x % NT, threadIdx.x);
}

extern "C" void kernel_launch(void* const* d_in, const int* in_sizes, int n_in,
                              void* d_out, int out_size, void* d_ws, size_t ws_size,
                              hipStream_t stream) {
  attn_base<<<NH * NT, 64, 0, stream>>>((const unsigned short*)d_in[0], (const unsigned short*)d_in[1],
                                        (const unsigned short*)d_in[2], (unsigned short*)d_out);
}
```

```cpp
#include <hip/hip_runtime.h>
#include <hip/hip_bf16.h>

typedef float    f32x4  __attribute__((ext_vector_type(4)));
typedef float    f32x2  __attribute__((ext_vector_type(2)));
typedef __bf16   bf16x8 __attribute__((ext_vector_type(8)));
typedef __bf16   bf16x2 __attribute__((ext_vector_type(2)));
typedef unsigned u32x4  __attribute__((ext_vector_type(4)));
typedef unsigned u32x2  __attribute__((ext_vector_type(2)));

constexpr int S = 736, D = 128, NT = S / 32;

__device__ __forceinline__ unsigned cvtpk(float lo, float hi) {
  f32x2 v = {lo, hi}; bf16x2 b = __builtin_convertvector(v, bf16x2); return __builtin_bit_cast(unsigned, b);
}

__device__ __forceinline__ void safe_tile(const unsigned short* q, const unsigned short* k, const unsigned short* v,
                                          unsigned short* o, int lane) {
  const int r = lane & 15, g = lane >> 4;
  const float c = 0.12751743082f;
  bf16x8 qf[4];
#pragma unroll
  for (int s = 0; s < 4; ++s) qf[s] = *(const bf16x8*)(q + r * D + 32 * s + 8 * g);
  f32x4 acc[8] = {};
  float m = -INFINITY, l = 0.f;
#pragma unroll 1
  for (int t = 0; t < NT; ++t) {
    f32x4 sc[2] = {};
#pragma unroll
    for (int kt = 0; kt < 2; ++kt)
#pragma unroll
      for (int s = 0; s < 4; ++s) {
        const bf16x8 kf = *(const bf16x8*)(k + (t * 32 + 16 * kt + r) * D + 32 * s + 8 * g);
        sc[kt] = __builtin_amdgcn_mfma_f32_16x16x32_bf16(kf, qf[s], sc[kt], 0, 0, 0);
      }
    float mx = fmaxf(fmaxf(fmaxf(sc[0][0], sc[0][1]), fmaxf(sc[0][2], sc[0][3])), fmaxf(fmaxf(sc[1][0], sc[1][1]), fmaxf(sc[1][2], sc[1][3])));
    mx = fmaxf(mx, __shfl_xor(mx, 16));
    mx = fmaxf(mx, __shfl_xor(mx, 32));
    const float mn = fmaxf(m, mx);
    const float ms = mn == -INFINITY ? 0.f : mn;
    const float al = __builtin_amdgcn_exp2f((m - ms) * c);
    m = mn;
    float p[8], ps = 0.f;
#pragma unroll
    for (int e = 0; e < 8; ++e) { p[e] = __builtin_amdgcn_exp2f((sc[e >> 2][e & 3] - ms) * c); ps += p[e]; }
    l = l * al + ps;
    const u32x4 pw = {cvtpk(p[0], p[1]), cvtpk(p[2], p[3]), cvtpk(p[4], p[5]), cvtpk(p[6], p[7])};
#pragma unroll
    for (int n = 0; n < 8; ++n) {
      u32x4 vw;
#pragma unroll
      for (int i = 0; i < 4; ++i) {
        const int k0 = t * 32 + 16 * (i >> 1) + 4 * g + 2 * (i & 1);
        vw[i] = (unsigned)v[k0 * D + 16 * n + r] | ((unsigned)v[(k0 + 1) * D + 16 * n + r] << 16);
      }
      acc[n] = __builtin_amdgcn_mfma_f32_16x16x32_bf16(__builtin_bit_cast(bf16x8, vw), __builtin_bit_cast(bf16x8, pw), acc[n] * al, 0, 0, 0);
    }
  }
  l += __shfl_xor(l, 16);
  l += __shfl_xor(l, 32);
  const float il = 1.f / l;
#pragma unroll
  for (int n = 0; n < 8; ++n) {
    const u32x2 w = {cvtpk(acc[n][0] * il, acc[n][1] * il), cvtpk(acc[n][2] * il, acc[n][3] * il)};
    *(u32x2*)(o + r * D + 16 * n + 4 * g) = w;
  }
}

__global__ __launch_bounds__(256, 1) void attn_fwd(const unsigned short* Q_, const unsigned short* K_,
                                                   const unsigned short* V_, unsigned short* O_) {
  __shared__ __attribute__((aligned(1024))) char lds[163840];
  const unsigned w = __builtin_amdgcn_readfirstlane(threadIdx.x >> 6);
  const unsigned b = blockIdx.x;
  unsigned long long ka = (unsigned long long)__builtin_amdgcn_kernarg_segment_ptr();
  unsigned flags;
  asm volatile("" :: "s"((unsigned)(size_t)lds) : "memory");
  asm volatile(
"s_load_dwordx8 s[84:91], %1, 0x0\n"
"v_mbcnt_lo_u32_b32 v249, -1, 0\n"
"v_mbcnt_hi_u32_b32 v249, -1, v249\n"
"s_mov_b32 s65, %2\n"
"s_bfe_u32 s70, %3, 0x10003\n"
"s_getpc_b64 s[96:97]\n"
"Lpf%=:\n"
"s_lshl_b32 s59, s70, 2\n"
"s_add_u32 s59, s59, s65\n"
"s_lshl_b32 s59, s59, 13\n"
"s_mov_b32 s60, Lend%=-Lpf%=\n"
"v_lshl_add_u32 v0, v249, 7, s59\n"
"v_min_u32 v0, s60, v0\n"
"global_load_dword v255, v0, s[96:97]\n"
"s_bfe_u32 s59, %3, 0x30003\n"
"s_lshl_b32 s59, s59, 13\n"
"v_lshl_add_u32 v162, v249, 7, s59\n"
"v_min_u32 v162, s60, v162\n"
"s_and_b32 s59, %3, 7\n"
"s_lshr_b32 s60, %3, 4\n"
"s_lshl3_add_u32 s59, s60, s59\n"
"s_mul_i32 s72, s59, 376832\n"
"s_mov_b32 s73, 0\n"
"s_mul_i32 s70, s70, 94208\n"
"s_mov_b32 s38, 376832\n"
"s_mov_b32 s39, 131072\n"
"s_mov_b32 s42, 376832\n"
"s_mov_b32 s43, 131072\n"
"s_mov_b32 s46, 376832\n"
"s_mov_b32 s47, 131072\n"
"s_add_u32 s50, s70, 94208\n"
"s_mov_b32 s51, 131072\n"
"v_bfe_u32 v0, v249, 2, 3\n"
"v_lshlrev_b32 v0, 8, v0\n"
"v_lshrrev_b32 v1, 5, v249\n"
"v_lshl_add_u32 v0, v1, 6, v0\n"
"v_and_b32 v1, 3, v249\n"
"v_bfe_u32 v2, v249, 4, 1\n"
"v_lshlrev_b32 v2, 1, v2\n"
"v_xor_b32 v3, v1, v2\n"
"v_lshl_add_u32 v252, v3, 4, v0\n"
"v_xor_b32 v253, 48, v252\n"
"s_lshr_b32 s59, s65, 1\n"
"s_lshl_b32 s59, s59, 12\n"
"s_and_b32 s60, s65, 1\n"
"s_lshl_b32 s66, s60, 10\n"
"s_add_u32 s66, s66, s59\n"
"s_lshl_b32 s67, s60, 7\n"
"s_add_u32 s67, s67, s59\n"
"s_mul_i32 s61, s65, 24576\n"
"s_add_u32 s62, s61, 65536\n"
"s_add_u32 s61, s61, s70\n"
"s_waitcnt lgkmcnt(0)\n"
"s_add_u32 s44, s84, s72\n"
"s_addc_u32 s45, s85, s73\n"
"s_and_b32 s45, s45, 65535\n"
"s_add_u32 m0, s62, 0\n"
"s_add_u32 s59, s61, 0\n"
"buffer_load_dwordx4 v252, s[44:47], s59 offen nt lds\n"
"buffer_load_dwordx4 v253, s[44:47], s59 offen offset:2048 nt lds\n"
"s_add_u32 m0, s62, 1024\n"
"s_add_u32 s59, s61, 128\n"
"buffer_load_dwordx4 v252, s[44:47], s59 offen nt lds\n"
"buffer_load_dwordx4 v253, s[44:47], s59 offen offset:2048 nt lds\n"
"s_add_u32 s36, s86, s72\n"
"s_addc_u32 s37, s87, s73\n"
"s_and_b32 s37, s37, 65535\n"
"s_add_u32 m0, s66, 0\n"
"s_add_u32 s59, s67, 0\n"
"buffer_load_dwordx4 v252, s[36:39], s59 offen nt lds\n"
"buffer_load_dwordx4 v253, s[36:39], s59 offen offset:2048 nt lds\n"
"s_add_u32 m0, s66, 8192\n"
"s_add_u32 s59, s67, 8192\n"
"buffer_load_dwordx4 v252, s[36:39], s59 offen nt lds\n"
"buffer_load_dwordx4 v253, s[36:39], s59 offen offset:2048 nt lds\n"
"s_add_u32 s40, s88, s72\n"
"s_addc_u32 s41, s89, s73\n"
"s_and_b32 s41, s41, 65535\n"
"s_add_u32 m0, s66, 32768\n"
"s_add_u32 s59, s67, 0\n"
"buffer_load_dwordx4 v252, s[40:43], s59 offen nt lds\n"
"buffer_load_dwordx4 v252, s[40:43], s59 offen offset:2048 nt lds\n"
"s_add_u32 m0, s66, 40960\n"
"s_add_u32 s59, s67, 8192\n"
"buffer_load_dwordx4 v252, s[40:43], s59 offen nt lds\n"
"buffer_load_dwordx4 v252, s[40:43], s59 offen offset:2048 nt lds\n"
"s_add_u32 s48, s90, s72\n"
"s_addc_u32 s49, s91, s73\n"
"s_and_b32 s49, s49, 65535\n"
"v_bfe_u32 v0, v249, 2, 2\n"
"v_lshlrev_b32 v0, 1, v0\n"
"v_mov_b32 v1, 120\n"
"v_bfe_u32 v0, v1, v0, 2\n"
"v_lshrrev_b32 v1, 4, v249\n"
"v_xor_b32 v0, v0, v1\n"
"v_lshlrev_b32 v0, 4, v0\n"
"v_and_b32 v2, 7, v249\n"
"v_lshl_add_u32 v0, v2, 6, v0\n"
"v_bfe_u32 v2, v249, 3, 1\n"
"v_lshl_add_u32 v246, v2, 11, v0\n"
"v_add_u32 v254, s62, v246\n"
"v_and_b32 v0, 3, v249\n"
"v_lshlrev_b32 v0, 3, v0\n"
"v_bfe_u32 v2, v249, 2, 3\n"
"v_lshl_add_u32 v0, v2, 6, v0\n"
"v_lshrrev_b32 v2, 5, v249\n"
"v_lshl_add_u32 v0, v2, 11, v0\n"
"v_and_b32 v2, 1, v1\n"
"v_lshl_add_u32 v247, v2, 5, v0\n"
"v_xor_b32 v248, 32, v247\n"
"v_mov_b32 v250, 1.0\n"
"v_mov_b32 v251, 671088640\n"
"s_mov_b32 s71, 3258187776\n"
"v_mov_b32 v216, s71\n"
"v_mov_b32 v217, s71\n"
"v_mov_b32 v218, s71\n"
"v_mov_b32 v219, s71\n"
"v_mov_b32 v220, s71\n"
"v_mov_b32 v221, s71\n"
"v_mov_b32 v222, s71\n"
"v_mov_b32 v223, s71\n"
"v_mov_b32 v224, s71\n"
"v_mov_b32 v225, s71\n"
"v_mov_b32 v226, s71\n"
"v_mov_b32 v227, s71\n"
"v_mov_b32 v228, s71\n"
"v_mov_b32 v229, s71\n"
"v_mov_b32 v230, s71\n"
"v_mov_b32 v231, s71\n"
"v_mov_b32 v232, s71\n"
"v_mov_b32 v233, s71\n"
"v_mov_b32 v234, s71\n"
"v_mov_b32 v235, s71\n"
"v_mov_b32 v236, s71\n"
"v_mov_b32 v237, s71\n"
"v_mov_b32 v238, s71\n"
"v_mov_b32 v239, s71\n"
"s_mov_b32 s52, 1040356334\n"
"s_mov_b32 s53, 1040356334\n"
"s_mov_b32 s64, 4294901760\n"
"s_mov_b32 s55, 0\n"
"s_mov_b32 s54, 0\n"
"s_mov_b32 s56, 40960\n"
"s_mov_b32 s58, 69632\n"
"s_mov_b32 s69, 4096\n"
"s_add_u32 s57, s70, 192512\n"
"s_mov_b32 s68, s61\n"
"v_and_b32 v0, 1, v249\n"
"v_lshlrev_b32 v0, 4, v0\n"
"v_mov_b32 v1, 15875\n"
"v_mov_b32 v2, 47576\n"
"v_lshlrev_b32 v1, v0, v1\n"
"v_lshlrev_b32 v2, v0, v2\n"
"v_accvgpr_write_b32 a224, 0\n"
"v_accvgpr_write_b32 a225, 0\n"
"v_accvgpr_write_b32 a226, 0\n"
"v_accvgpr_write_b32 a227, 0\n"
"v_accvgpr_write_b32 a228, 0\n"
"v_accvgpr_write_b32 a229, 0\n"
"v_accvgpr_write_b32 a230, 0\n"
"v_accvgpr_write_b32 a231, 0\n"
"v_accvgpr_write_b32 a232, 0\n"
"v_accvgpr_write_b32 a233, 0\n"
"v_accvgpr_write_b32 a234, 0\n"
"v_accvgpr_write_b32 a235, 0\n"
"v_accvgpr_write_b32 a236, 0\n"
"v_accvgpr_write_b32 a237, 0\n"
"v_accvgpr_write_b32 a238, 0\n"
"v_accvgpr_write_b32 a239, 0\n"
"s_mov_b32 exec_lo, 3\n"
"s_mov_b32 exec_hi, 48\n"
"v_accvgpr_write_b32 a224, v1\n"
"v_accvgpr_write_b32 a228, v2\n"
"s_mov_b32 exec_lo, 12\n"
"s_mov_b32 exec_hi, 192\n"
"v_accvgpr_write_b32 a225, v1\n"
"v_accvgpr_write_b32 a229, v2\n"
"s_mov_b32 exec_lo, 768\n"
"s_mov_b32 exec_hi, 12288\n"
"v_accvgpr_write_b32 a226, v1\n"
"v_accvgpr_write_b32 a230, v2\n"
"s_mov_b32 exec_lo, 3072\n"
"s_mov_b32 exec_hi, 49152\n"
"v_accvgpr_write_b32 a227, v1\n"
"v_accvgpr_write_b32 a231, v2\n"
"s_mov_b32 exec_lo, 196608\n"
"s_mov_b32 exec_hi, 3145728\n"
"v_accvgpr_write_b32 a232, v1\n"
"v_accvgpr_write_b32 a236, v2\n"
"s_mov_b32 exec_lo, 786432\n"
"s_mov_b32 exec_hi, 12582912\n"
"v_accvgpr_write_b32 a233, v1\n"
"v_accvgpr_write_b32 a237, v2\n"
"s_mov_b32 exec_lo, 50331648\n"
"s_mov_b32 exec_hi, 805306368\n"
"v_accvgpr_write_b32 a234, v1\n"
"v_accvgpr_write_b32 a238, v2\n"
"s_mov_b32 exec_lo, 201326592\n"
"s_mov_b32 exec_hi, 3221225472\n"
"v_accvgpr_write_b32 a235, v1\n"
"v_accvgpr_write_b32 a239, v2\n"
"s_mov_b64 exec, -1\n"
"s_mov_b64 exec, 0\n"
"s_cmp_eq_u32 s65, 0\n"
"s_cbranch_scc1 Lhop0%=\n"
"s_cmp_eq_u32 s65, 1\n"
"s_cbranch_scc1 Lhop1%=\n"
"s_cmp_eq_u32 s65, 2\n"
"s_cbranch_scc1 Lhop2%=\n"
"s_branch Lhop3%=\n"
"Lhopret%=:\n"
"s_mov_b64 exec, -1\n"
"s_add_u32 m0, s62, 4096\n"
"s_add_u32 s59, s61, 4096\n"
"buffer_load_dwordx4 v252, s[44:47], s59 offen nt lds\n"
"buffer_load_dwordx4 v253, s[44:47], s59 offen offset:2048 nt lds\n"
"s_add_u32 m0, s62, 5120\n"
"s_add_u32 s59, s61, 4224\n"
"buffer_load_dwordx4 v252, s[44:47], s59 offen nt lds\n"
"buffer_load_dwordx4 v253, s[44:47], s59 offen offset:2048 nt lds\n"
"s_add_u32 m0, s62, 8192\n"
"s_add_u32 s59, s61, 8192\n"
"buffer_load_dwordx4 v252, s[44:47], s59 offen nt lds\n"
"buffer_load_dwordx4 v253, s[44:47], s59 offen offset:2048 nt lds\n"
"s_add_u32 m0, s62, 9216\n"
"s_add_u32 s59, s61, 8320\n"
"buffer_load_dwordx4 v252, s[44:47], s59 offen nt lds\n"
"buffer_load_dwordx4 v253, s[44:47], s59 offen offset:2048 nt lds\n"
"s_cmp_eq_u32 s65, 3\n"
"s_cbranch_scc1 LroleB%=\n"
"v_and_b32 v32, 48, v249\n"
"s_waitcnt vmcnt(16)\n"
"ds_read_b128 a[8:11], v254 offset:1024\n"
"ds_read_b128 a[12:15], v254 offset:1536\n"
"ds_read_b128 v[8:11], v254 offset:0\n"
"ds_read_b128 v[12:15], v254 offset:512\n"
"s_waitcnt lgkmcnt(3)\n"
"v_mfma_f32_32x32x16_bf16 v[16:31], a[224:227], a[8:11], 0\n"
"s_waitcnt lgkmcnt(1)\n"
"v_lshlrev_b32 v0, 16, v8\n"
"v_and_b32 v1, s64, v8\n"
"v_lshlrev_b32 v2, 16, v9\n"
"v_and_b32 v3, s64, v9\n"
"v_mfma_f32_32x32x16_bf16 v[16:31], a[228:231], a[8:11], v[16:31]\n"
"v_lshlrev_b32 v4, 16, v10\n"
"v_and_b32 v5, s64, v10\n"
"v_lshlrev_b32 v6, 16, v11\n"
"v_and_b32 v7, s64, v11\n"
"v_mfma_f32_32x32x16_bf16 v[16:31], a[232:235], a[12:15], v[16:31]\n"
"s_waitcnt lgkmcnt(0)\n"
"v_lshlrev_b32 v8, 16, v12\n"
"v_and_b32 v9, s64, v12\n"
"v_lshlrev_b32 v10, 16, v13\n"
"v_and_b32 v11, s64, v13\n"
"v_mfma_f32_32x32x16_bf16 v[16:31], a[236:239], a[12:15], v[16:31]\n"
"v_lshlrev_b32 v12, 16, v14\n"
"v_and_b32 v13, s64, v14\n"
"v_lshlrev_b32 v14, 16, v15\n"
"v_and_b32 v15, s64, v15\n"
"v_pk_mul_f32 v[0:1], v[0:1], s[52:53]\n"
"v_pk_mul_f32 v[2:3], v[2:3], s[52:53]\n"
"v_pk_mul_f32 v[4:5], v[4:5], s[52:53]\n"
"v_pk_mul_f32 v[6:7], v[6:7], s[52:53]\n"
"v_pk_mul_f32 v[8:9], v[8:9], s[52:53]\n"
"v_pk_mul_f32 v[10:11], v[10:11], s[52:53]\n"
"v_pk_mul_f32 v[12:13], v[12:13], s[52:53]\n"
"v_pk_mul_f32 v[14:15], v[14:15], s[52:53]\n"
"v_cvt_pk_bf16_f32 v0, v0, v1\n"
"v_cvt_pk_bf16_f32 v2, v2, v3\n"
"v_cvt_pk_bf16_f32 v4, v4, v5\n"
"v_cvt_pk_bf16_f32 v6, v6, v7\n"
"v_accvgpr_write_b32 a0, v0\n"
"v_accvgpr_write_b32 a1, v2\n"
"v_accvgpr_write_b32 a2, v4\n"
"v_accvgpr_write_b32 a3, v6\n"
"v_cvt_pk_bf16_f32 v8, v8, v9\n"
"v_cvt_pk_bf16_f32 v10, v10, v11\n"
"v_cvt_pk_bf16_f32 v12, v12, v13\n"
"v_cvt_pk_bf16_f32 v14, v14, v15\n"
"v_accvgpr_write_b32 a4, v8\n"
"v_accvgpr_write_b32 a5, v10\n"
"v_accvgpr_write_b32 a6, v12\n"
"v_accvgpr_write_b32 a7, v14\n"
"v_cvt_pk_bf16_f32 v16, v16, v17\n"
"v_cvt_pk_bf16_f32 v18, v18, v19\n"
"v_cvt_pk_bf16_f32 v20, v20, v21\n"
"v_cvt_pk_bf16_f32 v22, v22, v23\n"
"v_accvgpr_write_b32 a8, v16\n"
"v_accvgpr_write_b32 a9, v18\n"
"v_accvgpr_write_b32 a10, v20\n"
"v_accvgpr_write_b32 a11, v22\n"
"v_cvt_pk_bf16_f32 v24, v24, v25\n"
"v_cvt_pk_bf16_f32 v26, v26, v27\n"
"v_cvt_pk_bf16_f32 v28, v28, v29\n"
"v_cvt_pk_bf16_f32 v30, v30, v31\n"
"v_accvgpr_write_b32 a12, v24\n"
"v_accvgpr_write_b32 a13, v26\n"
"v_accvgpr_write_b32 a14, v28\n"
"v_accvgpr_write_b32 a15, v30\n"
"s_waitcnt vmcnt(12)\n"
"s_barrier\n"
"ds_read_b128 v[64:67], v32 offset:0\n"
"ds_read_b128 v[68:71], v32 offset:512\n"
"ds_read_b128 v[72:75], v32 offset:1024\n"
"ds_read_b128 v[76:79], v32 offset:1536\n"
"ds_read_b128 a[96:99], v246 offset:0\n"
"ds_read_b128 a[100:103], v246 offset:512\n"
"ds_read_b128 a[104:107], v246 offset:1024\n"
"ds_read_b128 a[108:111], v246 offset:1536\n"
"ds_read_b128 a[112:115], v246 offset:4096\n"
"ds_read_b128 a[116:119], v246 offset:4608\n"
"ds_read_b128 a[120:123], v246 offset:5120\n"
"ds_read_b128 a[124:127], v246 offset:5632\n"
"ds_read_b128 a[128:131], v246 offset:8192\n"
"ds_read_b128 a[132:135], v246 offset:8704\n"
"ds_read_b128 a[136:139], v246 offset:9216\n"
"ds_read_b128 a[140:143], v246 offset:9728\n"
"ds_read_b128 a[144:147], v246 offset:12288\n"
"ds_read_b128 a[148:151], v246 offset:12800\n"
"ds_read_b128 a[152:155], v246 offset:13312\n"
"ds_read_b128 a[156:159], v246 offset:13824\n"
"s_waitcnt lgkmcnt(15)\n"
"v_xor_b32 v64, 2147516416, v64\n"
"v_xor_b32 v65, 2147516416, v65\n"
"v_xor_b32 v66, 2147516416, v66\n"
"v_xor_b32 v67, 2147516416, v67\n"
"v_accvgpr_write_b32 a240, v64\n"
"v_accvgpr_write_b32 a241, v65\n"
"v_accvgpr_write_b32 a242, v66\n"
"v_accvgpr_write_b32 a243, v67\n"
"v_xor_b32 v68, 2147516416, v68\n"
"v_xor_b32 v69, 2147516416, v69\n"
"v_xor_b32 v70, 2147516416, v70\n"
"v_xor_b32 v71, 2147516416, v71\n"
"v_mfma_f32_16x16x32_bf16 v[216:219], a[240:243], a[0:3], v[216:219]\n"
"v_accvgpr_write_b32 a244, v68\n"
"v_accvgpr_write_b32 a245, v69\n"
"v_accvgpr_write_b32 a246, v70\n"
"v_accvgpr_write_b32 a247, v71\n"
"v_xor_b32 v72, 2147516416, v72\n"
"v_xor_b32 v73, 2147516416, v73\n"
"v_xor_b32 v74, 2147516416, v74\n"
"v_xor_b32 v75, 2147516416, v75\n"
"v_mfma_f32_16x16x32_bf16 v[216:219], a[244:247], a[4:7], v[216:219]\n"
"v_accvgpr_write_b32 a248, v72\n"
"v_accvgpr_write_b32 a249, v73\n"
"v_accvgpr_write_b32 a250, v74\n"
"v_accvgpr_write_b32 a251, v75\n"
"v_xor_b32 v76, 2147516416, v76\n"
"v_xor_b32 v77, 2147516416, v77\n"
"v_xor_b32 v78, 2147516416, v78\n"
"v_xor_b32 v79, 2147516416, v79\n"
"v_mfma_f32_16x16x32_bf16 v[216:219], a[248:251], a[8:11], v[216:219]\n"
"v_accvgpr_write_b32 a252, v76\n"
"v_accvgpr_write_b32 a253, v77\n"
"v_accvgpr_write_b32 a254, v78\n"
"v_accvgpr_write_b32 a255, v79\n"
"s_waitcnt vmcnt(8)\n"
"s_barrier\n"
"v_mfma_f32_16x16x32_bf16 v[216:219], a[252:255], a[12:15], v[216:219]\n"
"v_mfma_f32_16x16x32_bf16 v[192:195], a[96:99], a[0:3], v[216:219]\n"
"s_waitcnt lgkmcnt(0)\n"
"ds_read_b64_tr_b16 a[160:161], v247 offset:32768\n"
"ds_read_b64_tr_b16 a[162:163], v247 offset:36864\n"
"v_mfma_f32_16x16x32_bf16 v[192:195], a[100:103], a[4:7], v[192:195]\n"
"ds_read_b64_tr_b16 a[164:165], v248 offset:32768\n"
"ds_read_b64_tr_b16 a[166:167], v248 offset:36864\n"
"v_mfma_f32_16x16x32_bf16 v[192:195], a[104:107], a[8:11], v[192:195]\n"
"ds_read_b64_tr_b16 a[168:169], v247 offset:33280\n"
"ds_read_b64_tr_b16 a[170:171], v247 offset:37376\n"
"v_mfma_f32_16x16x32_bf16 v[192:195], a[108:111], a[12:15], v[192:195]\n"
"ds_read_b64_tr_b16 a[172:173], v248 offset:33280\n"
"ds_read_b64_tr_b16 a[174:175], v248 offset:37376\n"
"v_mfma_f32_16x16x32_bf16 v[196:199], a[112:115], a[0:3], v[216:219]\n"
"ds_read_b64_tr_b16 a[176:177], v247 offset:33792\n"
"ds_read_b64_tr_b16 a[178:179], v247 offset:37888\n"
"v_mfma_f32_16x16x32_bf16 v[196:199], a[116:119], a[4:7], v[196:199]\n"
"ds_read_b64_tr_b16 a[180:181], v248 offset:33792\n"
"ds_read_b64_tr_b16 a[182:183], v248 offset:37888\n"
"v_mfma_f32_16x16x32_bf16 v[196:199], a[120:123], a[8:11], v[196:199]\n"
"ds_read_b64_tr_b16 a[184:185], v247 offset:34304\n"
"ds_read_b64_tr_b16 a[186:187], v247 offset:38400\n"
"v_mfma_f32_16x16x32_bf16 v[196:199], a[124:127], a[12:15], v[196:199]\n"
"ds_read_b64_tr_b16 a[188:189], v248 offset:34304\n"
"ds_read_b64_tr_b16 a[190:191], v248 offset:38400\n"
"v_exp_f32 v192, v192\n"
"ds_read_b64_tr_b16 a[192:193], v247 offset:40960\n"
"ds_read_b64_tr_b16 a[194:195], v247 offset:45056\n"
"ds_read_b64_tr_b16 a[196:197], v248 offset:40960\n"
"ds_read_b64_tr_b16 a[198:199], v248 offset:45056\n"
"ds_read_b64_tr_b16 a[200:201], v247 offset:41472\n"
"ds_read_b64_tr_b16 a[202:203], v247 offset:45568\n"
"ds_read_b64_tr_b16 a[204:205], v248 offset:41472\n"
"ds_read_b64_tr_b16 a[206:207], v248 offset:45568\n"
"v_mov_b32 v240, v192\n"
"s_waitcnt lgkmcnt(15)\n"
"v_mfma_f32_16x16x32_bf16 v[0:3], a[160:163], a[160:163], 0\n"
"v_mfma_f32_16x16x32_bf16 v[0:3], a[164:167], a[164:167], v[0:3]\n"
"v_exp_f32 v193, v193\n"
"s_add_u32 m0, s62, 12288\n"
"s_add_u32 s59, s61, 12288\n"
"buffer_load_dwordx4 v252, s[44:47], s59 offen nt lds\n"
"buffer_load_dwordx4 v253, s[44:47], s59 offen offset:2048 nt lds\n"
"s_add_u32 m0, s62, 13312\n"
"s_add_u32 s59, s61, 12416\n"
"buffer_load_dwordx4 v252, s[44:47], s59 offen nt lds\n"
"buffer_load_dwordx4 v253, s[44:47], s59 offen offset:2048 nt lds\n"
"v_add_f32 v240, v240, v193\n"
"v_cvt_pk_bf16_f32 v208, v192, v193\n"
"v_exp_f32 v194, v194\n"
"s_nop 0\n"
"v_add_f32 v240, v240, v194\n"
"v_readlane_b32 s98, v0, 0\n"
"v_readlane_b32 s72, v1, 21\n"
"v_readlane_b32 s73, v2, 42\n"
"v_readlane_b32 s60, v3, 63\n"
"v_exp_f32 v195, v195\n"
"s_nop 0\n"
"v_add_f32 v240, v240, v195\n"
"v_cvt_pk_bf16_f32 v209, v194, v195\n"
"s_min_u32 s98, s98, s72\n"
"s_min_u32 s73, s73, s60\n"
"s_min_u32 s98, s98, s73\n"
"s_lshr_b32 s98, s98, 23\n"
"s_add_u32 s72, s98, 45\n"
"s_lshr_b32 s72, s72, 1\n"
"s_lshl_b32 s72, s72, 23\n"
"s_or_b32 s72, s72, 4194304\n"
"s_sub_u32 s98, s98, 33\n"
"s_mov_b32 s73, 671088640\n"
"s_cmp_lt_u32 s98, 200\n"
"s_cselect_b32 s98, s72, 0\n"
"s_cselect_b32 s63, 1568669696, s73\n"
"v_exp_f32 v196, v196\n"
"v_mfma_f32_16x16x32_bf16 v[200:203], a[128:131], a[0:3], v[216:219]\n"
"ds_read_b64_tr_b16 a[208:209], v247 offset:41984\n"
"v_add_f32 v240, v240, v196\n"
"v_mfma_f32_16x16x32_bf16 v[200:203], a[132:135], a[4:7], v[200:203]\n"
"ds_read_b64_tr_b16 a[210:211], v247 offset:46080\n"
"v_exp_f32 v197, v197\n"
"v_mfma_f32_16x16x32_bf16 v[200:203], a[136:139], a[8:11], v[200:203]\n"
"ds_read_b64_tr_b16 a[212:213], v248 offset:41984\n"
"v_add_f32 v240, v240, v197\n"
"v_cvt_pk_bf16_f32 v210, v196, v197\n"
"v_mfma_f32_16x16x32_bf16 v[200:203], a[140:143], a[12:15], v[200:203]\n"
"ds_read_b64_tr_b16 a[214:215], v248 offset:46080\n"
"v_exp_f32 v198, v198\n"
"v_mfma_f32_16x16x32_bf16 v[204:207], a[144:147], a[0:3], v[216:219]\n"
"ds_read_b64_tr_b16 a[216:217], v247 offset:42496\n"
"v_mov_b32 v28, s98\n"
"v_mov_b32 v29, s98\n"
"v_add_f32 v240, v240, v198\n"
"v_mfma_f32_16x16x32_bf16 v[204:207], a[148:151], a[4:7], v[204:207]\n"
"ds_read_b64_tr_b16 a[218:219], v247 offset:46592\n"
"v_mov_b32 v30, s98\n"
"v_mov_b32 v31, s98\n"
"v_exp_f32 v199, v199\n"
"v_mfma_f32_16x16x32_bf16 v[204:207], a[152:155], a[8:11], v[204:207]\n"
"ds_read_b64_tr_b16 a[220:221], v248 offset:42496\n"
"v_add_f32 v240, v240, v199\n"
"v_cvt_pk_bf16_f32 v211, v198, v199\n"
"v_mfma_f32_16x16x32_bf16 v[204:207], a[156:159], a[12:15], v[204:207]\n"
"s_waitcnt vmcnt(8)\n"
"ds_read_b64_tr_b16 a[222:223], v248 offset:46592\n"
"v_exp_f32 v200, v200\n"
"v_mfma_f32_16x16x32_bf16 v[0:3], a[160:163], v[208:211], v[28:31]\n"
"ds_read_b128 a[24:27], v254 offset:5120\n"
"v_add_f32 v240, v240, v200\n"
"v_mfma_f32_16x16x32_bf16 v[4:7], a[164:167], v[208:211], v[28:31]\n"
"ds_read_b128 a[28:31], v254 offset:5632\n"
"v_exp_f32 v201, v201\n"
"v_mfma_f32_16x16x32_bf16 v[8:11], a[168:171], v[208:211], v[28:31]\n"
"ds_read_b128 v[40:43], v254 offset:4096\n"
"v_add_f32 v240, v240, v201\n"
"v_cvt_pk_bf16_f32 v212, v200, v201\n"
"v_mfma_f32_16x16x32_bf16 v[12:15], a[172:175], v[208:211], v[28:31]\n"
"s_waitcnt lgkmcnt(6)\n"
"ds_read_b128 v[44:47], v254 offset:4608\n"
"v_exp_f32 v202, v202\n"
"v_mfma_f32_16x16x32_bf16 v[16:19], a[176:179], v[208:211], v[28:31]\n"
"v_add_f32 v240, v240, v202\n"
"v_mfma_f32_16x16x32_bf16 v[20:23], a[180:183], v[208:211], v[28:31]\n"
"v_exp_f32 v203, v203\n"
"v_mfma_f32_16x16x32_bf16 v[24:27], a[184:187], v[208:211], v[28:31]\n"
"v_add_f32 v240, v240, v203\n"
"v_cvt_pk_bf16_f32 v213, v202, v203\n"
"v_mfma_f32_16x16x32_bf16 v[28:31], a[188:191], v[208:211], v[28:31]\n"
"s_waitcnt lgkmcnt(3)\n"
"s_add_u32 m0, s62, 16384\n"
"v_exp_f32 v204, v204\n"
"v_mfma_f32_32x32x16_bf16 v[48:63], a[224:227], a[24:27], 0\n"
"s_waitcnt lgkmcnt(1)\n"
"v_lshlrev_b32 v32, 16, v40\n"
"v_and_b32 v33, s64, v40\n"
"v_lshlrev_b32 v34, 16, v41\n"
"v_and_b32 v35, s64, v41\n"
"v_mfma_f32_32x32x16_bf16 v[48:63], a[228:231], a[24:27], v[48:63]\n"
"v_lshlrev_b32 v36, 16, v42\n"
"v_and_b32 v37, s64, v42\n"
"v_lshlrev_b32 v38, 16, v43\n"
"v_and_b32 v39, s64, v43\n"
"v_mfma_f32_32x32x16_bf16 v[48:63], a[232:235], a[28:31], v[48:63]\n"
"s_waitcnt lgkmcnt(0)\n"
"v_lshlrev_b32 v40, 16, v44\n"
"v_and_b32 v41, s64, v44\n"
"v_lshlrev_b32 v42, 16, v45\n"
"v_and_b32 v43, s64, v45\n"
"v_mfma_f32_32x32x16_bf16 v[48:63], a[236:239], a[28:31], v[48:63]\n"
"v_lshlrev_b32 v44, 16, v46\n"
"v_and_b32 v45, s64, v46\n"
"v_lshlrev_b32 v46, 16, v47\n"
"v_and_b32 v47, s64, v47\n"
"v_pk_mul_f32 v[32:33], v[32:33], s[52:53]\n"
"v_pk_mul_f32 v[34:35], v[34:35], s[52:53]\n"
"v_pk_mul_f32 v[36:37], v[36:37], s[52:53]\n"
"v_pk_mul_f32 v[38:39], v[38:39], s[52:53]\n"
"v_pk_mul_f32 v[40:41], v[40:41], s[52:53]\n"
"v_pk_mul_f32 v[42:43], v[42:43], s[52:53]\n"
"v_pk_mul_f32 v[44:45], v[44:45], s[52:53]\n"
"v_pk_mul_f32 v[46:47], v[46:47], s[52:53]\n"
"v_cvt_pk_bf16_f32 v32, v32, v33\n"
"v_cvt_pk_bf16_f32 v34, v34, v35\n"
"v_cvt_pk_bf16_f32 v36, v36, v37\n"
"v_cvt_pk_bf16_f32 v38, v38, v39\n"
"v_accvgpr_write_b32 a16, v32\n"
"v_accvgpr_write_b32 a17, v34\n"
"v_accvgpr_write_b32 a18, v36\n"
"v_accvgpr_write_b32 a19, v38\n"
"v_cvt_pk_bf16_f32 v40, v40, v41\n"
"v_cvt_pk_bf16_f32 v42, v42, v43\n"
"v_mfma_f32_16x16x32_bf16 v[220:223], a[240:243], a[16:19], v[220:223]\n"
"v_cvt_pk_bf16_f32 v44, v44, v45\n"
"v_cvt_pk_bf16_f32 v46, v46, v47\n"
"v_accvgpr_write_b32 a20, v40\n"
"v_accvgpr_write_b32 a21, v42\n"
"v_accvgpr_write_b32 a22, v44\n"
"v_accvgpr_write_b32 a23, v46\n"
"v_cvt_pk_bf16_f32 v48, v48, v49\n"
"v_cvt_pk_bf16_f32 v50, v50, v51\n"
"v_mfma_f32_16x16x32_bf16 v[220:223], a[244:247], a[20:23], v[220:223]\n"
"v_cvt_pk_bf16_f32 v52, v52, v53\n"
"v_cvt_pk_bf16_f32 v54, v54, v55\n"
"v_accvgpr_write_b32 a24, v48\n"
"v_accvgpr_write_b32 a25, v50\n"
"v_accvgpr_write_b32 a26, v52\n"
"v_accvgpr_write_b32 a27, v54\n"
"v_cvt_pk_bf16_f32 v56, v56, v57\n"
"v_cvt_pk_bf16_f32 v58, v58, v59\n"
"v_mfma_f32_16x16x32_bf16 v[220:223], a[248:251], a[24:27], v[220:223]\n"
"v_cvt_pk_bf16_f32 v60, v60, v61\n"
"v_cvt_pk_bf16_f32 v62, v62, v63\n"
"v_accvgpr_write_b32 a28, v56\n"
"v_accvgpr_write_b32 a29, v58\n"
"v_accvgpr_write_b32 a30, v60\n"
"v_accvgpr_write_b32 a31, v62\n"
"s_nop 1\n"
"v_mfma_f32_16x16x32_bf16 v[220:223], a[252:255], a[28:31], v[220:223]\n"
"v_mfma_f32_16x16x32_bf16 v[192:195], a[96:99], a[16:19], v[220:223]\n"
"s_add_u32 s59, s61, 16384\n"
"buffer_load_dwordx4 v252, s[44:47], s59 offen nt lds\n"
"v_add_f32 v240, v240, v204\n"
"v_mfma_f32_16x16x32_bf16 v[192:195], a[100:103], a[20:23], v[192:195]\n"
"v_exp_f32 v205, v205\n"
"v_mfma_f32_16x16x32_bf16 v[192:195], a[104:107], a[24:27], v[192:195]\n"
"v_add_f32 v240, v240, v205\n"
"v_cvt_pk_bf16_f32 v214, v204, v205\n"
"v_mfma_f32_16x16x32_bf16 v[192:195], a[108:111], a[28:31], v[192:195]\n"
"s_add_u32 m0, s62, 16384\n"
"v_exp_f32 v206, v206\n"
"v_mfma_f32_16x16x32_bf16 v[196:199], a[112:115], a[16:19], v[220:223]\n"
"s_add_u32 s59, s61, 16384\n"
"buffer_load_dwordx4 v253, s[44:47], s59 offen offset:2048 nt lds\n"
"v_add_f32 v240, v240, v206\n"
"v_mfma_f32_16x16x32_bf16 v[196:199], a[116:119], a[20:23], v[196:199]\n"
"v_exp_f32 v207, v207\n"
"v_mfma_f32_16x16x32_bf16 v[196:199], a[120:123], a[24:27], v[196:199]\n"
"v_add_f32 v240, v240, v207\n"
"v_cvt_pk_bf16_f32 v215, v206, v207\n"
"v_mfma_f32_16x16x32_bf16 v[196:199], a[124:127], a[28:31], v[196:199]\n"
"s_add_u32 m0, s62, 17408\n"
"v_exp_f32 v192, v192\n"
"v_mfma_f32_16x16x32_bf16 v[0:3], a[192:195], v[212:215], v[0:3]\n"
"s_add_u32 s59, s61, 16512\n"
"buffer_load_dwordx4 v252, s[44:47], s59 offen nt lds\n"
"v_mov_b32 v60, s98\n"
"v_mov_b32 v241, v192\n"
"v_mfma_f32_16x16x32_bf16 v[4:7], a[196:199], v[212:215], v[4:7]\n"
"v_exp_f32 v193, v193\n"
"v_mfma_f32_16x16x32_bf16 v[8:11], a[200:203], v[212:215], v[8:11]\n"
"v_add_f32 v241, v241, v193\n"
"v_cvt_pk_bf16_f32 v208, v192, v193\n"
"v_mfma_f32_16x16x32_bf16 v[12:15], a[204:207], v[212:215], v[12:15]\n"
"s_add_u32 m0, s62, 17408\n"
"v_exp_f32 v194, v194\n"
"v_mfma_f32_16x16x32_bf16 v[16:19], a[208:211], v[212:215], v[16:19]\n"
"s_add_u32 s59, s61, 16512\n"
"buffer_load_dwordx4 v253, s[44:47], s59 offen offset:2048 nt lds\n"
"v_mov_b32 v61, s98\n"
"v_add_f32 v241, v241, v194\n"
"v_mfma_f32_16x16x32_bf16 v[20:23], a[212:215], v[212:215], v[20:23]\n"
"v_exp_f32 v195, v195\n"
"v_mfma_f32_16x16x32_bf16 v[24:27], a[216:219], v[212:215], v[24:27]\n"
"v_add_f32 v241, v241, v195\n"
"v_cvt_pk_bf16_f32 v209, v194, v195\n"
"v_mfma_f32_16x16x32_bf16 v[28:31], a[220:223], v[212:215], v[28:31]\n"
"v_exp_f32 v196, v196\n"
"v_mfma_f32_16x16x32_bf16 v[200:203], a[128:131], a[16:19], v[220:223]\n"
"v_mov_b32 v62, s98\n"
"v_add_f32 v241, v241, v196\n"
"v_mfma_f32_16x16x32_bf16 v[200:203], a[132:135], a[20:23], v[200:203]\n"
"v_exp_f32 v197, v197\n"
"v_mfma_f32_16x16x32_bf16 v[200:203], a[136:139], a[24:27], v[200:203]\n"
"v_add_f32 v241, v241, v197\n"
"v_cvt_pk_bf16_f32 v210, v196, v197\n"
"v_mfma_f32_16x16x32_bf16 v[200:203], a[140:143], a[28:31], v[200:203]\n"
"v_exp_f32 v198, v198\n"
"v_mfma_f32_16x16x32_bf16 v[204:207], a[144:147], a[16:19], v[220:223]\n"
"v_mov_b32 v63, s98\n"
"v_add_f32 v241, v241, v198\n"
"v_mfma_f32_16x16x32_bf16 v[204:207], a[148:151], a[20:23], v[204:207]\n"
"v_exp_f32 v199, v199\n"
"v_mfma_f32_16x16x32_bf16 v[204:207], a[152:155], a[24:27], v[204:207]\n"
"v_add_f32 v241, v241, v199\n"
"v_cvt_pk_bf16_f32 v211, v198, v199\n"
"v_mfma_f32_16x16x32_bf16 v[204:207], a[156:159], a[28:31], v[204:207]\n"
"s_waitcnt vmcnt(8)\n"
"v_exp_f32 v200, v200\n"
"v_mfma_f32_16x16x32_bf16 v[32:35], a[160:163], v[208:211], v[60:63]\n"
"ds_read_b128 a[40:43], v254 offset:9216\n"
"v_add_f32 v241, v241, v200\n"
"v_mfma_f32_16x16x32_bf16 v[36:39], a[164:167], v[208:211], v[60:63]\n"
"ds_read_b128 a[44:47], v254 offset:9728\n"
"v_exp_f32 v201, v201\n"
"v_mfma_f32_16x16x32_bf16 v[40:43], a[168:171], v[208:211], v[60:63]\n"
"ds_read_b128 v[72:75], v254 offset:8192\n"
"v_add_f32 v241, v241, v201\n"
"v_cvt_pk_bf16_f32 v212, v200, v201\n"
"v_mfma_f32_16x16x32_bf16 v[44:47], a[172:175], v[208:211], v[60:63]\n"
"ds_read_b128 v[76:79], v254 offset:8704\n"
"v_exp_f32 v202, v202\n"
"v_mfma_f32_16x16x32_bf16 v[48:51], a[176:179], v[208:211], v[60:63]\n"
"v_add_f32 v241, v241, v202\n"
"v_mfma_f32_16x16x32_bf16 v[52:55], a[180:183], v[208:211], v[60:63]\n"
"v_exp_f32 v203, v203\n"
"v_mfma_f32_16x16x32_bf16 v[56:59], a[184:187], v[208:211], v[60:63]\n"
"v_add_f32 v241, v241, v203\n"
"v_cvt_pk_bf16_f32 v213, v202, v203\n"
"v_mfma_f32_16x16x32_bf16 v[60:63], a[188:191], v[208:211], v[60:63]\n"
"s_waitcnt lgkmcnt(3)\n"
"s_add_u32 m0, s62, 20480\n"
"v_exp_f32 v204, v204\n"
"v_mfma_f32_32x32x16_bf16 v[80:95], a[224:227], a[40:43], 0\n"
"s_waitcnt lgkmcnt(1)\n"
"v_lshlrev_b32 v64, 16, v72\n"
"v_and_b32 v65, s64, v72\n"
"v_lshlrev_b32 v66, 16, v73\n"
"v_and_b32 v67, s64, v73\n"
"v_mfma_f32_32x32x16_bf16 v[80:95], a[228:231], a[40:43], v[80:95]\n"
"v_lshlrev_b32 v68, 16, v74\n"
"v_and_b32 v69, s64, v74\n"
"v_lshlrev_b32 v70, 16, v75\n"
"v_and_b32 v71, s64, v75\n"
"v_mfma_f32_32x32x16_bf16 v[80:95], a[232:235], a[44:47], v[80:95]\n"
"s_waitcnt lgkmcnt(0)\n"
"v_lshlrev_b32 v72, 16, v76\n"
"v_and_b32 v73, s64, v76\n"
"v_lshlrev_b32 v74, 16, v77\n"
"v_and_b32 v75, s64, v77\n"
"v_mfma_f32_32x32x16_bf16 v[80:95], a[236:239], a[44:47], v[80:95]\n"
"v_lshlrev_b32 v76, 16, v78\n"
"v_and_b32 v77, s64, v78\n"
"v_lshlrev_b32 v78, 16, v79\n"
"v_and_b32 v79, s64, v79\n"
"v_pk_mul_f32 v[64:65], v[64:65], s[52:53]\n"
"v_pk_mul_f32 v[66:67], v[66:67], s[52:53]\n"
"v_pk_mul_f32 v[68:69], v[68:69], s[52:53]\n"
"v_pk_mul_f32 v[70:71], v[70:71], s[52:53]\n"
"v_pk_mul_f32 v[72:73], v[72:73], s[52:53]\n"
"v_pk_mul_f32 v[74:75], v[74:75], s[52:53]\n"
"v_pk_mul_f32 v[76:77], v[76:77], s[52:53]\n"
"v_pk_mul_f32 v[78:79], v[78:79], s[52:53]\n"
"v_cvt_pk_bf16_f32 v64, v64, v65\n"
"v_cvt_pk_bf16_f32 v66, v66, v67\n"
"v_cvt_pk_bf16_f32 v68, v68, v69\n"
"v_cvt_pk_bf16_f32 v70, v70, v71\n"
"v_accvgpr_write_b32 a32, v64\n"
"v_accvgpr_write_b32 a33, v66\n"
"v_accvgpr_write_b32 a34, v68\n"
"v_accvgpr_write_b32 a35, v70\n"
"v_cvt_pk_bf16_f32 v72, v72, v73\n"
"v_cvt_pk_bf16_f32 v74, v74, v75\n"
"v_mfma_f32_16x16x32_bf16 v[224:227], a[240:243], a[32:35], v[224:227]\n"
"v_cvt_pk_bf16_f32 v76, v76, v77\n"
"v_cvt_pk_bf16_f32 v78, v78, v79\n"
"v_accvgpr_write_b32 a36, v72\n"
"v_accvgpr_write_b32 a37, v74\n"
"v_accvgpr_write_b32 a38, v76\n"
"v_accvgpr_write_b32 a39, v78\n"
"v_cvt_pk_bf16_f32 v80, v80, v81\n"
"v_cvt_pk_bf16_f32 v82, v82, v83\n"
"v_mfma_f32_16x16x32_bf16 v[224:227], a[244:247], a[36:39], v[224:227]\n"
"v_cvt_pk_bf16_f32 v84, v84, v85\n"
"v_cvt_pk_bf16_f32 v86, v86, v87\n"
"v_accvgpr_write_b32 a40, v80\n"
"v_accvgpr_write_b32 a41, v82\n"
"v_accvgpr_write_b32 a42, v84\n"
"v_accvgpr_write_b32 a43, v86\n"
"v_cvt_pk_bf16_f32 v88, v88, v89\n"
"v_cvt_pk_bf16_f32 v90, v90, v91\n"
"v_mfma_f32_16x16x32_bf16 v[224:227], a[248:251], a[40:43], v[224:227]\n"
"v_cvt_pk_bf16_f32 v92, v92, v93\n"
"v_cvt_pk_bf16_f32 v94, v94, v95\n"
"v_accvgpr_write_b32 a44, v88\n"
"v_accvgpr_write_b32 a45, v90\n"
"v_accvgpr_write_b32 a46, v92\n"
"v_accvgpr_write_b32 a47, v94\n"
"s_barrier\n"
"s_nop 0\n"
"v_mfma_f32_16x16x32_bf16 v[224:227], a[252:255], a[44:47], v[224:227]\n"
"v_mfma_f32_16x16x32_bf16 v[192:195], a[96:99], a[32:35], v[224:227]\n"
"s_add_u32 s59, s61, 20480\n"
"buffer_load_dwordx4 v252, s[44:47], s59 offen nt lds\n"
"v_add_f32 v241, v241, v204\n"
"v_mfma_f32_16x16x32_bf16 v[192:195], a[100:103], a[36:39], v[192:195]\n"
"v_exp_f32 v205, v205\n"
"v_mfma_f32_16x16x32_bf16 v[192:195], a[104:107], a[40:43], v[192:195]\n"
"v_add_f32 v241, v241, v205\n"
"v_cvt_pk_bf16_f32 v214, v204, v205\n"
"v_mfma_f32_16x16x32_bf16 v[192:195], a[108:111], a[44:47], v[192:195]\n"
"s_add_u32 m0, s62, 20480\n"
"v_exp_f32 v206, v206\n"
"v_mfma_f32_16x16x32_bf16 v[196:199], a[112:115], a[32:35], v[224:227]\n"
"s_add_u32 s59, s61, 20480\n"
"buffer_load_dwordx4 v253, s[44:47], s59 offen offset:2048 nt lds\n"
"v_add_f32 v241, v241, v206\n"
"v_mfma_f32_16x16x32_bf16 v[196:199], a[116:119], a[36:39], v[196:199]\n"
"v_exp_f32 v207, v207\n"
"v_mfma_f32_16x16x32_bf16 v[196:199], a[120:123], a[40:43], v[196:199]\n"
"v_add_f32 v241, v241, v207\n"
"v_cvt_pk_bf16_f32 v215, v206, v207\n"
"v_mfma_f32_16x16x32_bf16 v[196:199], a[124:127], a[44:47], v[196:199]\n"
"s_add_u32 m0, s62, 21504\n"
"v_exp_f32 v192, v192\n"
"v_mfma_f32_16x16x32_bf16 v[32:35], a[192:195], v[212:215], v[32:35]\n"
"s_add_u32 s59, s61, 20608\n"
"buffer_load_dwordx4 v252, s[44:47], s59 offen nt lds\n"
"v_mov_b32 v92, s98\n"
"v_mov_b32 v242, v192\n"
"v_mfma_f32_16x16x32_bf16 v[36:39], a[196:199], v[212:215], v[36:39]\n"
"v_exp_f32 v193, v193\n"
"v_mfma_f32_16x16x32_bf16 v[40:43], a[200:203], v[212:215], v[40:43]\n"
"v_add_f32 v242, v242, v193\n"
"v_cvt_pk_bf16_f32 v208, v192, v193\n"
"v_mfma_f32_16x16x32_bf16 v[44:47], a[204:207], v[212:215], v[44:47]\n"
"s_add_u32 m0, s62, 21504\n"
"v_exp_f32 v194, v194\n"
"v_mfma_f32_16x16x32_bf16 v[48:51], a[208:211], v[212:215], v[48:51]\n"
"s_add_u32 s59, s61, 20608\n"
"buffer_load_dwordx4 v253, s[44:47], s59 offen offset:2048 nt lds\n"
"v_mov_b32 v93, s98\n"
"v_add_f32 v242, v242, v194\n"
"v_mfma_f32_16x16x32_bf16 v[52:55], a[212:215], v[212:215], v[52:55]\n"
"v_exp_f32 v195, v195\n"
"v_mfma_f32_16x16x32_bf16 v[56:59], a[216:219], v[212:215], v[56:59]\n"
"v_add_f32 v242, v242, v195\n"
"v_cvt_pk_bf16_f32 v209, v194, v195\n"
"v_mfma_f32_16x16x32_bf16 v[60:63], a[220:223], v[212:215], v[60:63]\n"
"v_exp_f32 v196, v196\n"
"v_mfma_f32_16x16x32_bf16 v[200:203], a[128:131], a[32:35], v[224:227]\n"
"v_mov_b32 v94, s98\n"
"v_add_f32 v242, v242, v196\n"
"v_mfma_f32_16x16x32_bf16 v[200:203], a[132:135], a[36:39], v[200:203]\n"
"v_exp_f32 v197, v197\n"
"v_mfma_f32_16x16x32_bf16 v[200:203], a[136:139], a[40:43], v[200:203]\n"
"v_add_f32 v242, v242, v197\n"
"v_cvt_pk_bf16_f32 v210, v196, v197\n"
"v_mfma_f32_16x16x32_bf16 v[200:203], a[140:143], a[44:47], v[200:203]\n"
"v_exp_f32 v198, v198\n"
"v_mfma_f32_16x16x32_bf16 v[204:207], a[144:147], a[32:35], v[224:227]\n"
"v_mov_b32 v95, s98\n"
"v_add_f32 v242, v242, v198\n"
"v_mfma_f32_16x16x32_bf16 v[204:207], a[148:151], a[36:39], v[204:207]\n"
"v_exp_f32 v199, v199\n"
"v_mfma_f32_16x16x32_bf16 v[204:207], a[152:155], a[40:43], v[204:207]\n"
"v_add_f32 v242, v242, v199\n"
"v_cvt_pk_bf16_f32 v211, v198, v199\n"
"v_mfma_f32_16x16x32_bf16 v[204:207], a[156:159], a[44:47], v[204:207]\n"
"s_waitcnt vmcnt(8)\n"
"v_exp_f32 v200, v200\n"
"v_mfma_f32_16x16x32_bf16 v[64:67], a[160:163], v[208:211], v[92:95]\n"
"ds_read_b128 a[56:59], v254 offset:13312\n"
"v_add_f32 v242, v242, v200\n"
"v_mfma_f32_16x16x32_bf16 v[68:71], a[164:167], v[208:211], v[92:95]\n"
"ds_read_b128 a[60:63], v254 offset:13824\n"
"v_exp_f32 v201, v201\n"
"v_mfma_f32_16x16x32_bf16 v[72:75], a[168:171], v[208:211], v[92:95]\n"
"ds_read_b128 v[104:107], v254 offset:12288\n"
"v_add_f32 v242, v242, v201\n"
"v_cvt_pk_bf16_f32 v212, v200, v201\n"
"v_mfma_f32_16x16x32_bf16 v[76:79], a[172:175], v[208:211], v[92:95]\n"
"ds_read_b128 v[108:111], v254 offset:12800\n"
"v_exp_f32 v202, v202\n"
"v_mfma_f32_16x16x32_bf16 v[80:83], a[176:179], v[208:211], v[92:95]\n"
"v_add_f32 v242, v242, v202\n"
"v_mfma_f32_16x16x32_bf16 v[84:87], a[180:183], v[208:211], v[92:95]\n"
"v_exp_f32 v203, v203\n"
"v_mfma_f32_16x16x32_bf16 v[88:91], a[184:187], v[208:211], v[92:95]\n"
"v_add_f32 v242, v242, v203\n"
"v_cvt_pk_bf16_f32 v213, v202, v203\n"
"v_mfma_f32_16x16x32_bf16 v[92:95], a[188:191], v[208:211], v[92:95]\n"
"s_waitcnt lgkmcnt(3)\n"
"s_add_u32 m0, s66, 16384\n"
"v_exp_f32 v204, v204\n"
"v_mfma_f32_32x32x16_bf16 v[112:127], a[224:227], a[56:59], 0\n"
"s_waitcnt lgkmcnt(1)\n"
"v_lshlrev_b32 v96, 16, v104\n"
"v_and_b32 v97, s64, v104\n"
"v_lshlrev_b32 v98, 16, v105\n"
"v_and_b32 v99, s64, v105\n"
"v_mfma_f32_32x32x16_bf16 v[112:127], a[228:231], a[56:59], v[112:127]\n"
"v_lshlrev_b32 v100, 16, v106\n"
"v_and_b32 v101, s64, v106\n"
"v_lshlrev_b32 v102, 16, v107\n"
"v_and_b32 v103, s64, v107\n"
"v_mfma_f32_32x32x16_bf16 v[112:127], a[232:235], a[60:63], v[112:127]\n"
"s_waitcnt lgkmcnt(0)\n"
"v_lshlrev_b32 v104, 16, v108\n"
"v_and_b32 v105, s64, v108\n"
"v_lshlrev_b32 v106, 16, v109\n"
"v_and_b32 v107, s64, v109\n"
"v_mfma_f32_32x32x16_bf16 v[112:127], a[236:239], a[60:63], v[112:127]\n"
"v_lshlrev_b32 v108, 16, v110\n"
"v_and_b32 v109, s64, v110\n"
"v_lshlrev_b32 v110, 16, v111\n"
"v_and_b32 v111, s64, v111\n"
"v_pk_mul_f32 v[96:97], v[96:97], s[52:53]\n"
"v_pk_mul_f32 v[98:99], v[98:99], s[52:53]\n"
"v_pk_mul_f32 v[100:101], v[100:101], s[52:53]\n"
"v_pk_mul_f32 v[102:103], v[102:103], s[52:53]\n"
"v_pk_mul_f32 v[104:105], v[104:105], s[52:53]\n"
"v_pk_mul_f32 v[106:107], v[106:107], s[52:53]\n"
"v_pk_mul_f32 v[108:109], v[108:109], s[52:53]\n"
"v_pk_mul_f32 v[110:111], v[110:111], s[52:53]\n"
"v_cvt_pk_bf16_f32 v96, v96, v97\n"
"v_cvt_pk_bf16_f32 v98, v98, v99\n"
"v_cvt_pk_bf16_f32 v100, v100, v101\n"
"v_cvt_pk_bf16_f32 v102, v102, v103\n"
"v_accvgpr_write_b32 a48, v96\n"
"v_accvgpr_write_b32 a49, v98\n"
"v_accvgpr_write_b32 a50, v100\n"
"v_accvgpr_write_b32 a51, v102\n"
"v_cvt_pk_bf16_f32 v104, v104, v105\n"
"v_cvt_pk_bf16_f32 v106, v106, v107\n"
"v_mfma_f32_16x16x32_bf16 v[228:231], a[240:243], a[48:51], v[228:231]\n"
"v_cvt_pk_bf16_f32 v108, v108, v109\n"
"v_cvt_pk_bf16_f32 v110, v110, v111\n"
"v_accvgpr_write_b32 a52, v104\n"
"v_accvgpr_write_b32 a53, v106\n"
"v_accvgpr_write_b32 a54, v108\n"
"v_accvgpr_write_b32 a55, v110\n"
"v_cvt_pk_bf16_f32 v112, v112, v113\n"
"v_cvt_pk_bf16_f32 v114, v114, v115\n"
"v_mfma_f32_16x16x32_bf16 v[228:231], a[244:247], a[52:55], v[228:231]\n"
"v_cvt_pk_bf16_f32 v116, v116, v117\n"
"v_cvt_pk_bf16_f32 v118, v118, v119\n"
"v_accvgpr_write_b32 a56, v112\n"
"v_accvgpr_write_b32 a57, v114\n"
"v_accvgpr_write_b32 a58, v116\n"
"v_accvgpr_write_b32 a59, v118\n"
"v_cvt_pk_bf16_f32 v120, v120, v121\n"
"v_cvt_pk_bf16_f32 v122, v122, v123\n"
"v_mfma_f32_16x16x32_bf16 v[228:231], a[248:251], a[56:59], v[228:231]\n"
"v_cvt_pk_bf16_f32 v124, v124, v125\n"
"v_cvt_pk_bf16_f32 v126, v126, v127\n"
"v_accvgpr_write_b32 a60, v120\n"
"v_accvgpr_write_b32 a61, v122\n"
"v_accvgpr_write_b32 a62, v124\n"
"v_accvgpr_write_b32 a63, v126\n"
"s_nop 1\n"
"v_mfma_f32_16x16x32_bf16 v[228:231], a[252:255], a[60:63], v[228:231]\n"
"v_mfma_f32_16x16x32_bf16 v[192:195], a[96:99], a[48:51], v[228:231]\n"
"s_add_u32 s59, s67, 16384\n"
"buffer_load_dwordx4 v252, s[36:39], s59 offen nt lds\n"
"v_add_f32 v242, v242, v204\n"
"v_mfma_f32_16x16x32_bf16 v[192:195], a[100:103], a[52:55], v[192:195]\n"
"v_exp_f32 v205, v205\n"
"v_mfma_f32_16x16x32_bf16 v[192:195], a[104:107], a[56:59], v[192:195]\n"
"v_add_f32 v242, v242, v205\n"
"v_cvt_pk_bf16_f32 v214, v204, v205\n"
"v_mfma_f32_16x16x32_bf16 v[192:195], a[108:111], a[60:63], v[192:195]\n"
"s_add_u32 m0, s66, 16384\n"
"v_exp_f32 v206, v206\n"
"v_mfma_f32_16x16x32_bf16 v[196:199], a[112:115], a[48:51], v[228:231]\n"
"s_add_u32 s59, s67, 16384\n"
"buffer_load_dwordx4 v253, s[36:39], s59 offen offset:2048 nt lds\n"
"v_add_f32 v242, v242, v206\n"
"v_mfma_f32_16x16x32_bf16 v[196:199], a[116:119], a[52:55], v[196:199]\n"
"v_exp_f32 v207, v207\n"
"v_mfma_f32_16x16x32_bf16 v[196:199], a[120:123], a[56:59], v[196:199]\n"
"v_add_f32 v242, v242, v207\n"
"v_cvt_pk_bf16_f32 v215, v206, v207\n"
"v_mfma_f32_16x16x32_bf16 v[196:199], a[124:127], a[60:63], v[196:199]\n"
"s_add_u32 m0, s66, 49152\n"
"v_exp_f32 v192, v192\n"
"v_mfma_f32_16x16x32_bf16 v[64:67], a[192:195], v[212:215], v[64:67]\n"
"s_add_u32 s59, s67, 16384\n"
"buffer_load_dwordx4 v252, s[40:43], s59 offen nt lds\n"
"v_mov_b32 v124, s98\n"
"v_mov_b32 v243, v192\n"
"v_mfma_f32_16x16x32_bf16 v[68:71], a[196:199], v[212:215], v[68:71]\n"
"v_exp_f32 v193, v193\n"
"v_mfma_f32_16x16x32_bf16 v[72:75], a[200:203], v[212:215], v[72:75]\n"
"v_add_f32 v243, v243, v193\n"
"v_cvt_pk_bf16_f32 v208, v192, v193\n"
"v_mfma_f32_16x16x32_bf16 v[76:79], a[204:207], v[212:215], v[76:79]\n"
"s_add_u32 m0, s66, 49152\n"
"v_exp_f32 v194, v194\n"
"v_mfma_f32_16x16x32_bf16 v[80:83], a[208:211], v[212:215], v[80:83]\n"
"s_add_u32 s59, s67, 16384\n"
"buffer_load_dwordx4 v252, s[40:43], s59 offen offset:2048 nt lds\n"
"v_mov_b32 v125, s98\n"
"v_add_f32 v243, v243, v194\n"
"v_mfma_f32_16x16x32_bf16 v[84:87], a[212:215], v[212:215], v[84:87]\n"
"v_exp_f32 v195, v195\n"
"v_mfma_f32_16x16x32_bf16 v[88:91], a[216:219], v[212:215], v[88:91]\n"
"v_add_f32 v243, v243, v195\n"
"v_cvt_pk_bf16_f32 v209, v194, v195\n"
"v_mfma_f32_16x16x32_bf16 v[92:95], a[220:223], v[212:215], v[92:95]\n"
"s_add_u32 m0, s66, 24576\n"
"v_exp_f32 v196, v196\n"
"v_mfma_f32_16x16x32_bf16 v[200:203], a[128:131], a[48:51], v[228:231]\n"
"s_add_u32 s59, s67, 24576\n"
"buffer_load_dwordx4 v252, s[36:39], s59 offen nt lds\n"
"v_mov_b32 v126, s98\n"
"v_add_f32 v243, v243, v196\n"
"v_mfma_f32_16x16x32_bf16 v[200:203], a[132:135], a[52:55], v[200:203]\n"
"v_exp_f32 v197, v197\n"
"v_mfma_f32_16x16x32_bf16 v[200:203], a[136:139], a[56:59], v[200:203]\n"
"v_add_f32 v243, v243, v197\n"
"v_cvt_pk_bf16_f32 v210, v196, v197\n"
"v_mfma_f32_16x16x32_bf16 v[200:203], a[140:143], a[60:63], v[200:203]\n"
"s_add_u32 m0, s66, 24576\n"
"v_exp_f32 v198, v198\n"
"v_mfma_f32_16x16x32_bf16 v[204:207], a[144:147], a[48:51], v[228:231]\n"
"s_add_u32 s59, s67, 24576\n"
"buffer_load_dwordx4 v253, s[36:39], s59 offen offset:2048 nt lds\n"
"v_mov_b32 v127, s98\n"
"v_add_f32 v243, v243, v198\n"
"v_mfma_f32_16x16x32_bf16 v[204:207], a[148:151], a[52:55], v[204:207]\n"
"v_exp_f32 v199, v199\n"
"v_mfma_f32_16x16x32_bf16 v[204:207], a[152:155], a[56:59], v[204:207]\n"
"v_add_f32 v243, v243, v199\n"
"v_cvt_pk_bf16_f32 v211, v198, v199\n"
"v_mfma_f32_16x16x32_bf16 v[204:207], a[156:159], a[60:63], v[204:207]\n"
"s_waitcnt vmcnt(10)\n"
"v_exp_f32 v200, v200\n"
"v_mfma_f32_16x16x32_bf16 v[96:99], a[160:163], v[208:211], v[124:127]\n"
"ds_read_b128 a[72:75], v254 offset:17408\n"
"v_add_f32 v243, v243, v200\n"
"v_mfma_f32_16x16x32_bf16 v[100:103], a[164:167], v[208:211], v[124:127]\n"
"ds_read_b128 a[76:79], v254 offset:17920\n"
"v_exp_f32 v201, v201\n"
"v_mfma_f32_16x16x32_bf16 v[104:107], a[168:171], v[208:211], v[124:127]\n"
"ds_read_b128 v[136:139], v254 offset:16384\n"
"v_add_f32 v243, v243, v201\n"
"v_cvt_pk_bf16_f32 v212, v200, v201\n"
"v_mfma_f32_16x16x32_bf16 v[108:111], a[172:175], v[208:211], v[124:127]\n"
"ds_read_b128 v[140:143], v254 offset:16896\n"
"v_exp_f32 v202, v202\n"
"v_mfma_f32_16x16x32_bf16 v[112:115], a[176:179], v[208:211], v[124:127]\n"
"v_add_f32 v243, v243, v202\n"
"v_mfma_f32_16x16x32_bf16 v[116:119], a[180:183], v[208:211], v[124:127]\n"
"v_exp_f32 v203, v203\n"
"v_mfma_f32_16x16x32_bf16 v[120:123], a[184:187], v[208:211], v[124:127]\n"
"v_add_f32 v243, v243, v203\n"
"v_cvt_pk_bf16_f32 v213, v202, v203\n"
"v_mfma_f32_16x16x32_bf16 v[124:127], a[188:191], v[208:211], v[124:127]\n"
"s_waitcnt lgkmcnt(3)\n"
"v_exp_f32 v204, v204\n"
"v_mfma_f32_32x32x16_bf16 v[144:159], a[224:227], a[72:75], 0\n"
"s_waitcnt lgkmcnt(1)\n"
"v_lshlrev_b32 v128, 16, v136\n"
"v_and_b32 v129, s64, v136\n"
"v_lshlrev_b32 v130, 16, v137\n"
"v_and_b32 v131, s64, v137\n"
"v_mfma_f32_32x32x16_bf16 v[144:159], a[228:231], a[72:75], v[144:159]\n"
"v_lshlrev_b32 v132, 16, v138\n"
"v_and_b32 v133, s64, v138\n"
"v_lshlrev_b32 v134, 16, v139\n"
"v_and_b32 v135, s64, v139\n"
"v_mfma_f32_32x32x16_bf16 v[144:159], a[232:235], a[76:79], v[144:159]\n"
"s_waitcnt lgkmcnt(0)\n"
"v_lshlrev_b32 v136, 16, v140\n"
"v_and_b32 v137, s64, v140\n"
"v_lshlrev_b32 v138, 16, v141\n"
"v_and_b32 v139, s64, v141\n"
"v_mfma_f32_32x32x16_bf16 v[144:159], a[236:239], a[76:79], v[144:159]\n"
"v_lshlrev_b32 v140, 16, v142\n"
"v_and_b32 v141, s64, v142\n"
"v_lshlrev_b32 v142, 16, v143\n"
"v_and_b32 v143, s64, v143\n"
"v_pk_mul_f32 v[128:129], v[128:129], s[52:53]\n"
"v_pk_mul_f32 v[130:131], v[130:131], s[52:53]\n"
"v_pk_mul_f32 v[132:133], v[132:133], s[52:53]\n"
"v_pk_mul_f32 v[134:135], v[134:135], s[52:53]\n"
"v_pk_mul_f32 v[136:137], v[136:137], s[52:53]\n"
"v_pk_mul_f32 v[138:139], v[138:139], s[52:53]\n"
"v_pk_mul_f32 v[140:141], v[140:141], s[52:53]\n"
"v_pk_mul_f32 v[142:143], v[142:143], s[52:53]\n"
"v_cvt_pk_bf16_f32 v128, v128, v129\n"
"v_cvt_pk_bf16_f32 v130, v130, v131\n"
"v_cvt_pk_bf16_f32 v132, v132, v133\n"
"v_cvt_pk_bf16_f32 v134, v134, v135\n"
"v_accvgpr_write_b32 a64, v128\n"
"v_accvgpr_write_b32 a65, v130\n"
"v_accvgpr_write_b32 a66, v132\n"
"v_accvgpr_write_b32 a67, v134\n"
"v_cvt_pk_bf16_f32 v136, v136, v137\n"
"v_cvt_pk_bf16_f32 v138, v138, v139\n"
"v_mfma_f32_16x16x32_bf16 v[232:235], a[240:243], a[64:67], v[232:235]\n"
"v_cvt_pk_bf16_f32 v140, v140, v141\n"
"v_cvt_pk_bf16_f32 v142, v142, v143\n"
"v_accvgpr_write_b32 a68, v136\n"
"v_accvgpr_write_b32 a69, v138\n"
"v_accvgpr_write_b32 a70, v140\n"
"v_accvgpr_write_b32 a71, v142\n"
"v_cvt_pk_bf16_f32 v144, v144, v145\n"
"v_cvt_pk_bf16_f32 v146, v146, v147\n"
"v_mfma_f32_16x16x32_bf16 v[232:235], a[244:247], a[68:71], v[232:235]\n"
"v_cvt_pk_bf16_f32 v148, v148, v149\n"
"v_cvt_pk_bf16_f32 v150, v150, v151\n"
"v_accvgpr_write_b32 a72, v144\n"
"v_accvgpr_write_b32 a73, v146\n"
"v_accvgpr_write_b32 a74, v148\n"
"v_accvgpr_write_b32 a75, v150\n"
"v_cvt_pk_bf16_f32 v152, v152, v153\n"
"v_cvt_pk_bf16_f32 v154, v154, v155\n"
"v_mfma_f32_16x16x32_bf16 v[232:235], a[248:251], a[72:75], v[232:235]\n"
"v_cvt_pk_bf16_f32 v156, v156, v157\n"
"v_cvt_pk_bf16_f32 v158, v158, v159\n"
"v_accvgpr_write_b32 a76, v152\n"
"v_accvgpr_write_b32 a77, v154\n"
"v_accvgpr_write_b32 a78, v156\n"
"v_accvgpr_write_b32 a79, v158\n"
"v_lshlrev_b32 v255, 4, v249\n"
"v_and_b32 v129, 15, v249\n"
"v_mul_u32_u24 v252, 272, v129\n"
"v_lshrrev_b32 v129, 4, v249\n"
"v_lshl_add_u32 v252, v129, 4, v252\n"
"v_add_u32 v252, s62, v252\n"
"v_mul_u32_u24 v253, 272, v129\n"
"v_add_u32 v253, s62, v253\n"
"v_bfe_u32 v129, v249, 2, 2\n"
"v_lshl_add_u32 v253, v129, 6, v253\n"
"v_and_b32 v129, 1, v249\n"
"v_lshl_add_u32 v253, v129, 5, v253\n"
"v_bfe_u32 v129, v249, 1, 1\n"
"v_lshl_add_u32 v253, v129, 3, v253\n"
"v_and_b32 v249, 48, v249\n"
"v_mfma_f32_16x16x32_bf16 v[232:235], a[252:255], a[76:79], v[232:235]\n"
"v_mfma_f32_16x16x32_bf16 v[192:195], a[96:99], a[64:67], v[232:235]\n"
"v_add_f32 v243, v243, v204\n"
"v_mfma_f32_16x16x32_bf16 v[192:195], a[100:103], a[68:71], v[192:195]\n"
"v_exp_f32 v205, v205\n"
"v_mfma_f32_16x16x32_bf16 v[192:195], a[104:107], a[72:75], v[192:195]\n"
"v_add_f32 v243, v243, v205\n"
"v_cvt_pk_bf16_f32 v214, v204, v205\n"
"v_mfma_f32_16x16x32_bf16 v[192:195], a[108:111], a[76:79], v[192:195]\n"
"v_exp_f32 v206, v206\n"
"v_mfma_f32_16x16x32_bf16 v[196:199], a[112:115], a[64:67], v[232:235]\n"
"v_add_f32 v243, v243, v206\n"
"v_mfma_f32_16x16x32_bf16 v[196:199], a[116:119], a[68:71], v[196:199]\n"
"v_exp_f32 v207, v207\n"
"v_mfma_f32_16x16x32_bf16 v[196:199], a[120:123], a[72:75], v[196:199]\n"
"v_add_f32 v243, v243, v207\n"
"v_cvt_pk_bf16_f32 v215, v206, v207\n"
"v_mfma_f32_16x16x32_bf16 v[196:199], a[124:127], a[76:79], v[196:199]\n"
"v_exp_f32 v192, v192\n"
"v_mfma_f32_16x16x32_bf16 v[96:99], a[192:195], v[212:215], v[96:99]\n"
"v_mov_b32 v156, s98\n"
"v_mov_b32 v244, v192\n"
"v_mfma_f32_16x16x32_bf16 v[100:103], a[196:199], v[212:215], v[100:103]\n"
"v_exp_f32 v193, v193\n"
"v_mfma_f32_16x16x32_bf16 v[104:107], a[200:203], v[212:215], v[104:107]\n"
"v_add_f32 v244, v244, v193\n"
"v_cvt_pk_bf16_f32 v208, v192, v193\n"
"v_mfma_f32_16x16x32_bf16 v[108:111], a[204:207], v[212:215], v[108:111]\n"
"v_exp_f32 v194, v194\n"
"v_mfma_f32_16x16x32_bf16 v[112:115], a[208:211], v[212:215], v[112:115]\n"
"v_mov_b32 v157, s98\n"
"v_add_f32 v244, v244, v194\n"
"v_mfma_f32_16x16x32_bf16 v[116:119], a[212:215], v[212:215], v[116:119]\n"
"v_exp_f32 v195, v195\n"
"v_mfma_f32_16x16x32_bf16 v[120:123], a[216:219], v[212:215], v[120:123]\n"
"v_add_f32 v244, v244, v195\n"
"v_cvt_pk_bf16_f32 v209, v194, v195\n"
"v_mfma_f32_16x16x32_bf16 v[124:127], a[220:223], v[212:215], v[124:127]\n"
"v_exp_f32 v196, v196\n"
"v_mfma_f32_16x16x32_bf16 v[200:203], a[128:131], a[64:67], v[232:235]\n"
"v_mov_b32 v158, s98\n"
"v_add_f32 v244, v244, v196\n"
"v_mfma_f32_16x16x32_bf16 v[200:203], a[132:135], a[68:71], v[200:203]\n"
"v_exp_f32 v197, v197\n"
"v_mfma_f32_16x16x32_bf16 v[200:203], a[136:139], a[72:75], v[200:203]\n"
"v_add_f32 v244, v244, v197\n"
"v_cvt_pk_bf16_f32 v210, v196, v197\n"
"v_mfma_f32_16x16x32_bf16 v[200:203], a[140:143], a[76:79], v[200:203]\n"
"v_exp_f32 v198, v198\n"
"v_mfma_f32_16x16x32_bf16 v[204:207], a[144:147], a[64:67], v[232:235]\n"
"v_mov_b32 v159, s98\n"
"v_add_f32 v244, v244, v198\n"
"v_mfma_f32_16x16x32_bf16 v[204:207], a[148:151], a[68:71], v[204:207]\n"
"v_exp_f32 v199, v199\n"
"v_mfma_f32_16x16x32_bf16 v[204:207], a[152:155], a[72:75], v[204:207]\n"
"v_add_f32 v244, v244, v199\n"
"v_cvt_pk_bf16_f32 v211, v198, v199\n"
"v_mfma_f32_16x16x32_bf16 v[204:207], a[156:159], a[76:79], v[204:207]\n"
"s_waitcnt vmcnt(6)\n"
"v_exp_f32 v200, v200\n"
"v_mfma_f32_16x16x32_bf16 v[128:131], a[160:163], v[208:211], v[156:159]\n"
"ds_read_b128 a[88:91], v254 offset:21504\n"
"v_add_f32 v244, v244, v200\n"
"v_mfma_f32_16x16x32_bf16 v[132:135], a[164:167], v[208:211], v[156:159]\n"
"ds_read_b128 a[92:95], v254 offset:22016\n"
"v_exp_f32 v201, v201\n"
"v_mfma_f32_16x16x32_bf16 v[136:139], a[168:171], v[208:211], v[156:159]\n"
"ds_read_b128 v[168:171], v254 offset:20480\n"
"v_add_f32 v244, v244, v201\n"
"v_cvt_pk_bf16_f32 v212, v200, v201\n"
"v_mfma_f32_16x16x32_bf16 v[140:143], a[172:175], v[208:211], v[156:159]\n"
"ds_read_b128 v[172:175], v254 offset:20992\n"
"v_exp_f32 v202, v202\n"
"v_mfma_f32_16x16x32_bf16 v[144:147], a[176:179], v[208:211], v[156:159]\n"
"v_add_f32 v244, v244, v202\n"
"v_mfma_f32_16x16x32_bf16 v[148:151], a[180:183], v[208:211], v[156:159]\n"
"v_exp_f32 v203, v203\n"
"v_mfma_f32_16x16x32_bf16 v[152:155], a[184:187], v[208:211], v[156:159]\n"
"v_add_f32 v244, v244, v203\n"
"v_cvt_pk_bf16_f32 v213, v202, v203\n"
"v_mfma_f32_16x16x32_bf16 v[156:159], a[188:191], v[208:211], v[156:159]\n"
"s_waitcnt lgkmcnt(3)\n"
"v_exp_f32 v204, v204\n"
"v_mfma_f32_32x32x16_bf16 v[176:191], a[224:227], a[88:91], 0\n"
"s_waitcnt lgkmcnt(1)\n"
"v_lshlrev_b32 v160, 16, v168\n"
"v_and_b32 v161, s64, v168\n"
"v_lshlrev_b32 v162, 16, v169\n"
"v_and_b32 v163, s64, v169\n"
"v_mfma_f32_32x32x16_bf16 v[176:191], a[228:231], a[88:91], v[176:191]\n"
"v_lshlrev_b32 v164, 16, v170\n"
"v_and_b32 v165, s64, v170\n"
"v_lshlrev_b32 v166, 16, v171\n"
"v_and_b32 v167, s64, v171\n"
"v_mfma_f32_32x32x16_bf16 v[176:191], a[232:235], a[92:95], v[176:191]\n"
"s_waitcnt lgkmcnt(0)\n"
"v_lshlrev_b32 v168, 16, v172\n"
"v_and_b32 v169, s64, v172\n"
"v_lshlrev_b32 v170, 16, v173\n"
"v_and_b32 v171, s64, v173\n"
"v_mfma_f32_32x32x16_bf16 v[176:191], a[236:239], a[92:95], v[176:191]\n"
"v_lshlrev_b32 v172, 16, v174\n"
"v_and_b32 v173, s64, v174\n"
"v_lshlrev_b32 v174, 16, v175\n"
"v_and_b32 v175, s64, v175\n"
"v_pk_mul_f32 v[160:161], v[160:161], s[52:53]\n"
"v_pk_mul_f32 v[162:163], v[162:163], s[52:53]\n"
"v_pk_mul_f32 v[164:165], v[164:165], s[52:53]\n"
"v_pk_mul_f32 v[166:167], v[166:167], s[52:53]\n"
"v_pk_mul_f32 v[168:169], v[168:169], s[52:53]\n"
"v_pk_mul_f32 v[170:171], v[170:171], s[52:53]\n"
"v_pk_mul_f32 v[172:173], v[172:173], s[52:53]\n"
"v_pk_mul_f32 v[174:175], v[174:175], s[52:53]\n"
"v_cvt_pk_bf16_f32 v160, v160, v161\n"
"v_cvt_pk_bf16_f32 v162, v162, v163\n"
"v_cvt_pk_bf16_f32 v164, v164, v165\n"
"v_cvt_pk_bf16_f32 v166, v166, v167\n"
"v_accvgpr_write_b32 a80, v160\n"
"v_accvgpr_write_b32 a81, v162\n"
"v_accvgpr_write_b32 a82, v164\n"
"v_accvgpr_write_b32 a83, v166\n"
"v_cvt_pk_bf16_f32 v168, v168, v169\n"
"v_cvt_pk_bf16_f32 v170, v170, v171\n"
"v_mfma_f32_16x16x32_bf16 v[236:239], a[240:243], a[80:83], v[236:239]\n"
"v_cvt_pk_bf16_f32 v172, v172, v173\n"
"v_cvt_pk_bf16_f32 v174, v174, v175\n"
"v_accvgpr_write_b32 a84, v168\n"
"v_accvgpr_write_b32 a85, v170\n"
"v_accvgpr_write_b32 a86, v172\n"
"v_accvgpr_write_b32 a87, v174\n"
"v_cvt_pk_bf16_f32 v176, v176, v177\n"
"v_cvt_pk_bf16_f32 v178, v178, v179\n"
"v_mfma_f32_16x16x32_bf16 v[236:239], a[244:247], a[84:87], v[236:239]\n"
"v_cvt_pk_bf16_f32 v180, v180, v181\n"
"v_cvt_pk_bf16_f32 v182, v182, v183\n"
"v_accvgpr_write_b32 a88, v176\n"
"v_accvgpr_write_b32 a89, v178\n"
"v_accvgpr_write_b32 a90, v180\n"
"v_accvgpr_write_b32 a91, v182\n"
"v_cvt_pk_bf16_f32 v184, v184, v185\n"
"v_cvt_pk_bf16_f32 v186, v186, v187\n"
"v_mfma_f32_16x16x32_bf16 v[236:239], a[248:251], a[88:91], v[236:239]\n"
"v_cvt_pk_bf16_f32 v188, v188, v189\n"
"v_cvt_pk_bf16_f32 v190, v190, v191\n"
"v_accvgpr_write_b32 a92, v184\n"
"v_accvgpr_write_b32 a93, v186\n"
"v_accvgpr_write_b32 a94, v188\n"
"v_accvgpr_write_b32 a95, v190\n"
"s_waitcnt vmcnt(4)\n"
"s_barrier\n"
"v_mfma_f32_16x16x32_bf16 v[236:239], a[252:255], a[92:95], v[236:239]\n"
"v_mfma_f32_16x16x32_bf16 v[192:195], a[96:99], a[80:83], v[236:239]\n"
"ds_read_b128 a[96:99], v246 offset:16384\n"
"v_add_f32 v244, v244, v204\n"
"v_mfma_f32_16x16x32_bf16 v[192:195], a[100:103], a[84:87], v[192:195]\n"
"v_exp_f32 v205, v205\n"
"v_mfma_f32_16x16x32_bf16 v[192:195], a[104:107], a[88:91], v[192:195]\n"
"ds_read_b128 a[100:103], v246 offset:16896\n"
"v_add_f32 v244, v244, v205\n"
"v_cvt_pk_bf16_f32 v214, v204, v205\n"
"v_mfma_f32_16x16x32_bf16 v[192:195], a[108:111], a[92:95], v[192:195]\n"
"v_exp_f32 v206, v206\n"
"v_mfma_f32_16x16x32_bf16 v[196:199], a[112:115], a[80:83], v[236:239]\n"
"ds_read_b128 a[104:107], v246 offset:17408\n"
"v_add_f32 v244, v244, v206\n"
"v_mfma_f32_16x16x32_bf16 v[196:199], a[116:119], a[84:87], v[196:199]\n"
"v_exp_f32 v207, v207\n"
"v_mfma_f32_16x16x32_bf16 v[196:199], a[120:123], a[88:91], v[196:199]\n"
"ds_read_b128 a[108:111], v246 offset:17920\n"
"v_add_f32 v244, v244, v207\n"
"v_cvt_pk_bf16_f32 v215, v206, v207\n"
"v_mfma_f32_16x16x32_bf16 v[196:199], a[124:127], a[92:95], v[196:199]\n"
"v_exp_f32 v192, v192\n"
"v_mfma_f32_16x16x32_bf16 v[128:131], a[192:195], v[212:215], v[128:131]\n"
"ds_read_b128 a[112:115], v246 offset:20480\n"
"v_mov_b32 v188, s98\n"
"v_mov_b32 v245, v192\n"
"v_mfma_f32_16x16x32_bf16 v[132:135], a[196:199], v[212:215], v[132:135]\n"
"v_exp_f32 v193, v193\n"
"v_mfma_f32_16x16x32_bf16 v[136:139], a[200:203], v[212:215], v[136:139]\n"
"ds_read_b128 a[116:119], v246 offset:20992\n"
"v_add_f32 v245, v245, v193\n"
"v_cvt_pk_bf16_f32 v208, v192, v193\n"
"v_mfma_f32_16x16x32_bf16 v[140:143], a[204:207], v[212:215], v[140:143]\n"
"v_exp_f32 v194, v194\n"
"v_mfma_f32_16x16x32_bf16 v[144:147], a[208:211], v[212:215], v[144:147]\n"
"ds_read_b128 a[120:123], v246 offset:21504\n"
"v_mov_b32 v189, s98\n"
"v_add_f32 v245, v245, v194\n"
"v_mfma_f32_16x16x32_bf16 v[148:151], a[212:215], v[212:215], v[148:151]\n"
"v_exp_f32 v195, v195\n"
"v_mfma_f32_16x16x32_bf16 v[152:155], a[216:219], v[212:215], v[152:155]\n"
"ds_read_b128 a[124:127], v246 offset:22016\n"
"v_add_f32 v245, v245, v195\n"
"v_cvt_pk_bf16_f32 v209, v194, v195\n"
"v_mfma_f32_16x16x32_bf16 v[156:159], a[220:223], v[212:215], v[156:159]\n"
"v_exp_f32 v196, v196\n"
"v_mfma_f32_16x16x32_bf16 v[200:203], a[128:131], a[80:83], v[236:239]\n"
"v_mov_b32 v190, s98\n"
"v_add_f32 v245, v245, v196\n"
"v_mfma_f32_16x16x32_bf16 v[200:203], a[132:135], a[84:87], v[200:203]\n"
"v_exp_f32 v197, v197\n"
"v_mfma_f32_16x16x32_bf16 v[200:203], a[136:139], a[88:91], v[200:203]\n"
"v_add_f32 v245, v245, v197\n"
"v_cvt_pk_bf16_f32 v210, v196, v197\n"
"v_mfma_f32_16x16x32_bf16 v[200:203], a[140:143], a[92:95], v[200:203]\n"
"v_exp_f32 v198, v198\n"
"v_mfma_f32_16x16x32_bf16 v[204:207], a[144:147], a[80:83], v[236:239]\n"
"v_mov_b32 v191, s98\n"
"v_add_f32 v245, v245, v198\n"
"v_mfma_f32_16x16x32_bf16 v[204:207], a[148:151], a[84:87], v[204:207]\n"
"s_waitcnt vmcnt(0)\n"
"v_exp_f32 v199, v199\n"
"v_mfma_f32_16x16x32_bf16 v[204:207], a[152:155], a[88:91], v[204:207]\n"
"s_barrier\n"
"v_add_f32 v245, v245, v199\n"
"v_cvt_pk_bf16_f32 v211, v198, v199\n"
"v_mfma_f32_16x16x32_bf16 v[204:207], a[156:159], a[92:95], v[204:207]\n"
"v_exp_f32 v200, v200\n"
"v_mfma_f32_16x16x32_bf16 v[160:163], a[160:163], v[208:211], v[188:191]\n"
"ds_read_b64_tr_b16 a[160:161], v247 offset:49152\n"
"v_add_f32 v245, v245, v200\n"
"v_mfma_f32_16x16x32_bf16 v[164:167], a[164:167], v[208:211], v[188:191]\n"
"ds_read_b64_tr_b16 a[162:163], v247 offset:53248\n"
"v_exp_f32 v201, v201\n"
"v_mfma_f32_16x16x32_bf16 v[168:171], a[168:171], v[208:211], v[188:191]\n"
"ds_read_b64_tr_b16 a[164:165], v248 offset:49152\n"
"v_add_f32 v245, v245, v201\n"
"v_cvt_pk_bf16_f32 v212, v200, v201\n"
"v_mfma_f32_16x16x32_bf16 v[172:175], a[172:175], v[208:211], v[188:191]\n"
"ds_read_b64_tr_b16 a[166:167], v248 offset:53248\n"
"v_exp_f32 v202, v202\n"
"v_mfma_f32_16x16x32_bf16 v[176:179], a[176:179], v[208:211], v[188:191]\n"
"ds_read_b64_tr_b16 a[168:169], v247 offset:49664\n"
"v_add_f32 v245, v245, v202\n"
"v_mfma_f32_16x16x32_bf16 v[180:183], a[180:183], v[208:211], v[188:191]\n"
"ds_read_b64_tr_b16 a[170:171], v247 offset:53760\n"
"v_exp_f32 v203, v203\n"
"v_mfma_f32_16x16x32_bf16 v[184:187], a[184:187], v[208:211], v[188:191]\n"
"ds_read_b64_tr_b16 a[172:173], v248 offset:49664\n"
"v_add_f32 v245, v245, v203\n"
"v_cvt_pk_bf16_f32 v213, v202, v203\n"
"v_mfma_f32_16x16x32_bf16 v[188:191], a[188:191], v[208:211], v[188:191]\n"
"s_waitcnt lgkmcnt(7)\n"
"ds_read_b64_tr_b16 a[174:175], v248 offset:53760\n"
"v_exp_f32 v204, v204\n"
"v_mfma_f32_16x16x32_bf16 v[192:195], a[96:99], a[0:3], v[216:219]\n"
"ds_read_b64_tr_b16 a[176:177], v247 offset:50176\n"
"v_add_f32 v245, v245, v204\n"
"v_mfma_f32_16x16x32_bf16 v[192:195], a[100:103], a[4:7], v[192:195]\n"
"v_exp_f32 v205, v205\n"
"v_mfma_f32_16x16x32_bf16 v[192:195], a[104:107], a[8:11], v[192:195]\n"
"ds_read_b64_tr_b16 a[178:179], v247 offset:54272\n"
"v_add_f32 v245, v245, v205\n"
"v_cvt_pk_bf16_f32 v214, v204, v205\n"
"v_mfma_f32_16x16x32_bf16 v[192:195], a[108:111], a[12:15], v[192:195]\n"
"v_exp_f32 v206, v206\n"
"v_mfma_f32_16x16x32_bf16 v[196:199], a[112:115], a[0:3], v[216:219]\n"
"ds_read_b64_tr_b16 a[180:181], v248 offset:50176\n"
"v_add_f32 v245, v245, v206\n"
"v_mfma_f32_16x16x32_bf16 v[196:199], a[116:119], a[4:7], v[196:199]\n"
"v_exp_f32 v207, v207\n"
"v_mfma_f32_16x16x32_bf16 v[196:199], a[120:123], a[8:11], v[196:199]\n"
"ds_read_b64_tr_b16 a[182:183], v248 offset:54272\n"
"v_add_f32 v245, v245, v207\n"
"v_cvt_pk_bf16_f32 v215, v206, v207\n"
"v_mfma_f32_16x16x32_bf16 v[196:199], a[124:127], a[12:15], v[196:199]\n"
"v_exp_f32 v192, v192\n"
"v_mfma_f32_16x16x32_bf16 v[160:163], a[192:195], v[212:215], v[160:163]\n"
"ds_read_b64_tr_b16 a[184:185], v247 offset:50688\n"
"v_add_f32 v240, v240, v192\n"
"v_mfma_f32_16x16x32_bf16 v[164:167], a[196:199], v[212:215], v[164:167]\n"
"v_exp_f32 v193, v193\n"
"v_mfma_f32_16x16x32_bf16 v[168:171], a[200:203], v[212:215], v[168:171]\n"
"ds_read_b64_tr_b16 a[186:187], v247 offset:54784\n"
"v_add_f32 v240, v240, v193\n"
"v_cvt_pk_bf16_f32 v208, v192, v193\n"
"v_mfma_f32_16x16x32_bf16 v[172:175], a[204:207], v[212:215], v[172:175]\n"
"v_exp_f32 v194, v194\n"
"v_mfma_f32_16x16x32_bf16 v[176:179], a[208:211], v[212:215], v[176:179]\n"
"ds_read_b64_tr_b16 a[188:189], v248 offset:50688\n"
"v_add_f32 v240, v240, v194\n"
"v_mfma_f32_16x16x32_bf16 v[180:183], a[212:215], v[212:215], v[180:183]\n"
"v_exp_f32 v195, v195\n"
"v_mfma_f32_16x16x32_bf16 v[184:187], a[216:219], v[212:215], v[184:187]\n"
"ds_read_b64_tr_b16 a[190:191], v248 offset:54784\n"
"v_add_f32 v240, v240, v195\n"
"v_cvt_pk_bf16_f32 v209, v194, v195\n"
"v_mfma_f32_16x16x32_bf16 v[188:191], a[220:223], v[212:215], v[188:191]\n"
"LMA0%=:\n"
"v_exp_f32 v196, v196\n"
"v_mfma_f32_16x16x32_bf16 v[200:203], a[96:99], a[16:19], v[220:223]\n"
"ds_read_b128 a[128:131], v246 offset:24576\n"
"v_add_f32 v240, v240, v196\n"
"v_mfma_f32_16x16x32_bf16 v[200:203], a[100:103], a[20:23], v[200:203]\n"
"v_exp_f32 v197, v197\n"
"v_mfma_f32_16x16x32_bf16 v[200:203], a[104:107], a[24:27], v[200:203]\n"
"v_add_f32 v240, v240, v197\n"
"v_cvt_pk_bf16_f32 v210, v196, v197\n"
"v_mfma_f32_16x16x32_bf16 v[200:203], a[108:111], a[28:31], v[200:203]\n"
"v_exp_f32 v198, v198\n"
"v_mfma_f32_16x16x32_bf16 v[204:207], a[112:115], a[16:19], v[220:223]\n"
"ds_read_b128 a[132:135], v246 offset:25088\n"
"v_add_f32 v240, v240, v198\n"
"v_mfma_f32_16x16x32_bf16 v[204:207], a[116:119], a[20:23], v[204:207]\n"
"s_waitcnt lgkmcnt(2)\n"
"v_exp_f32 v199, v199\n"
"v_mfma_f32_16x16x32_bf16 v[204:207], a[120:123], a[24:27], v[204:207]\n"
"v_add_f32 v240, v240, v199\n"
"v_cvt_pk_bf16_f32 v211, v198, v199\n"
"v_mfma_f32_16x16x32_bf16 v[204:207], a[124:127], a[28:31], v[204:207]\n"
"s_barrier\n"
"v_exp_f32 v200, v200\n"
"v_mfma_f32_16x16x32_bf16 v[0:3], a[160:163], v[208:211], v[0:3]\n"
"ds_read_b128 a[136:139], v246 offset:25600\n"
"v_add_f32 v241, v241, v200\n"
"v_mfma_f32_16x16x32_bf16 v[4:7], a[164:167], v[208:211], v[4:7]\n"
"v_exp_f32 v201, v201\n"
"v_mfma_f32_16x16x32_bf16 v[8:11], a[168:171], v[208:211], v[8:11]\n"
"v_add_f32 v241, v241, v201\n"
"v_cvt_pk_bf16_f32 v212, v200, v201\n"
"v_mfma_f32_16x16x32_bf16 v[12:15], a[172:175], v[208:211], v[12:15]\n"
"v_exp_f32 v202, v202\n"
"v_mfma_f32_16x16x32_bf16 v[16:19], a[176:179], v[208:211], v[16:19]\n"
"ds_read_b128 a[140:143], v246 offset:26112\n"
"v_add_f32 v241, v241, v202\n"
"v_mfma_f32_16x16x32_bf16 v[20:23], a[180:183], v[208:211], v[20:23]\n"
"v_exp_f32 v203, v203\n"
"v_mfma_f32_16x16x32_bf16 v[24:27], a[184:187], v[208:211], v[24:27]\n"
"v_add_f32 v241, v241, v203\n"
"v_cvt_pk_bf16_f32 v213, v202, v203\n"
"v_mfma_f32_16x16x32_bf16 v[28:31], a[188:191], v[208:211], v[28:31]\n"
"v_exp_f32 v204, v204\n"
"v_mfma_f32_16x16x32_bf16 v[192:195], a[96:99], a[32:35], v[224:227]\n"
"ds_read_b128 a[144:147], v246 offset:28672\n"
"v_add_f32 v241, v241, v204\n"
"v_mfma_f32_16x16x32_bf16 v[192:195], a[100:103], a[36:39], v[192:195]\n"
"v_exp_f32 v205, v205\n"
"v_mfma_f32_16x16x32_bf16 v[192:195], a[104:107], a[40:43], v[192:195]\n"
"v_add_f32 v241, v241, v205\n"
"v_cvt_pk_bf16_f32 v214, v204, v205\n"
"v_mfma_f32_16x16x32_bf16 v[192:195], a[108:111], a[44:47], v[192:195]\n"
"v_exp_f32 v206, v206\n"
"v_mfma_f32_16x16x32_bf16 v[196:199], a[112:115], a[32:35], v[224:227]\n"
"ds_read_b128 a[148:151], v246 offset:29184\n"
"v_add_f32 v241, v241, v206\n"
"v_mfma_f32_16x16x32_bf16 v[196:199], a[116:119], a[36:39], v[196:199]\n"
"v_exp_f32 v207, v207\n"
"v_mfma_f32_16x16x32_bf16 v[196:199], a[120:123], a[40:43], v[196:199]\n"
"v_add_f32 v241, v241, v207\n"
"v_cvt_pk_bf16_f32 v215, v206, v207\n"
"v_mfma_f32_16x16x32_bf16 v[196:199], a[124:127], a[44:47], v[196:199]\n"
"v_exp_f32 v192, v192\n"
"v_mfma_f32_16x16x32_bf16 v[32:35], a[160:163], v[212:215], v[32:35]\n"
"ds_read_b128 a[152:155], v246 offset:29696\n"
"v_add_f32 v242, v242, v192\n"
"v_mfma_f32_16x16x32_bf16 v[36:39], a[164:167], v[212:215], v[36:39]\n"
"v_exp_f32 v193, v193\n"
"v_mfma_f32_16x16x32_bf16 v[40:43], a[168:171], v[212:215], v[40:43]\n"
"v_add_f32 v242, v242, v193\n"
"v_cvt_pk_bf16_f32 v208, v192, v193\n"
"v_mfma_f32_16x16x32_bf16 v[44:47], a[172:175], v[212:215], v[44:47]\n"
"v_exp_f32 v194, v194\n"
"v_mfma_f32_16x16x32_bf16 v[48:51], a[176:179], v[212:215], v[48:51]\n"
"ds_read_b128 a[156:159], v246 offset:30208\n"
"v_add_f32 v242, v242, v194\n"
"v_mfma_f32_16x16x32_bf16 v[52:55], a[180:183], v[212:215], v[52:55]\n"
"v_exp_f32 v195, v195\n"
"v_mfma_f32_16x16x32_bf16 v[56:59], a[184:187], v[212:215], v[56:59]\n"
"v_add_f32 v242, v242, v195\n"
"v_cvt_pk_bf16_f32 v209, v194, v195\n"
"v_mfma_f32_16x16x32_bf16 v[60:63], a[188:191], v[212:215], v[60:63]\n"
"v_exp_f32 v196, v196\n"
"v_mfma_f32_16x16x32_bf16 v[200:203], a[96:99], a[48:51], v[228:231]\n"
"ds_read_b64_tr_b16 a[192:193], v247 offset:57344\n"
"v_add_f32 v242, v242, v196\n"
"v_mfma_f32_16x16x32_bf16 v[200:203], a[100:103], a[52:55], v[200:203]\n"
"v_exp_f32 v197, v197\n"
"v_mfma_f32_16x16x32_bf16 v[200:203], a[104:107], a[56:59], v[200:203]\n"
"v_add_f32 v242, v242, v197\n"
"v_cvt_pk_bf16_f32 v210, v196, v197\n"
"v_mfma_f32_16x16x32_bf16 v[200:203], a[108:111], a[60:63], v[200:203]\n"
"v_exp_f32 v198, v198\n"
"v_mfma_f32_16x16x32_bf16 v[204:207], a[112:115], a[48:51], v[228:231]\n"
"ds_read_b64_tr_b16 a[194:195], v247 offset:61440\n"
"v_add_f32 v242, v242, v198\n"
"v_mfma_f32_16x16x32_bf16 v[204:207], a[116:119], a[52:55], v[204:207]\n"
"v_exp_f32 v199, v199\n"
"v_mfma_f32_16x16x32_bf16 v[204:207], a[120:123], a[56:59], v[204:207]\n"
"v_add_f32 v242, v242, v199\n"
"v_cvt_pk_bf16_f32 v211, v198, v199\n"
"v_mfma_f32_16x16x32_bf16 v[204:207], a[124:127], a[60:63], v[204:207]\n"
"v_exp_f32 v200, v200\n"
"v_mfma_f32_16x16x32_bf16 v[64:67], a[160:163], v[208:211], v[64:67]\n"
"ds_read_b64_tr_b16 a[196:197], v248 offset:57344\n"
"v_add_f32 v243, v243, v200\n"
"v_mfma_f32_16x16x32_bf16 v[68:71], a[164:167], v[208:211], v[68:71]\n"
"v_exp_f32 v201, v201\n"
"v_mfma_f32_16x16x32_bf16 v[72:75], a[168:171], v[208:211], v[72:75]\n"
"v_add_f32 v243, v243, v201\n"
"v_cvt_pk_bf16_f32 v212, v200, v201\n"
"v_mfma_f32_16x16x32_bf16 v[76:79], a[172:175], v[208:211], v[76:79]\n"
"v_exp_f32 v202, v202\n"
"v_mfma_f32_16x16x32_bf16 v[80:83], a[176:179], v[208:211], v[80:83]\n"
"ds_read_b64_tr_b16 a[198:199], v248 offset:61440\n"
"v_add_f32 v243, v243, v202\n"
"v_mfma_f32_16x16x32_bf16 v[84:87], a[180:183], v[208:211], v[84:87]\n"
"v_exp_f32 v203, v203\n"
"v_mfma_f32_16x16x32_bf16 v[88:91], a[184:187], v[208:211], v[88:91]\n"
"v_add_f32 v243, v243, v203\n"
"v_cvt_pk_bf16_f32 v213, v202, v203\n"
"v_mfma_f32_16x16x32_bf16 v[92:95], a[188:191], v[208:211], v[92:95]\n"
"v_exp_f32 v204, v204\n"
"v_mfma_f32_16x16x32_bf16 v[192:195], a[96:99], a[64:67], v[232:235]\n"
"ds_read_b64_tr_b16 a[200:201], v247 offset:57856\n"
"v_add_f32 v243, v243, v204\n"
"v_mfma_f32_16x16x32_bf16 v[192:195], a[100:103], a[68:71], v[192:195]\n"
"v_exp_f32 v205, v205\n"
"v_mfma_f32_16x16x32_bf16 v[192:195], a[104:107], a[72:75], v[192:195]\n"
"v_add_f32 v243, v243, v205\n"
"v_cvt_pk_bf16_f32 v214, v204, v205\n"
"v_mfma_f32_16x16x32_bf16 v[192:195], a[108:111], a[76:79], v[192:195]\n"
"v_exp_f32 v206, v206\n"
"v_mfma_f32_16x16x32_bf16 v[196:199], a[112:115], a[64:67], v[232:235]\n"
"ds_read_b64_tr_b16 a[202:203], v247 offset:61952\n"
"v_add_f32 v243, v243, v206\n"
"v_mfma_f32_16x16x32_bf16 v[196:199], a[116:119], a[68:71], v[196:199]\n"
"v_exp_f32 v207, v207\n"
"v_mfma_f32_16x16x32_bf16 v[196:199], a[120:123], a[72:75], v[196:199]\n"
"v_add_f32 v243, v243, v207\n"
"v_cvt_pk_bf16_f32 v215, v206, v207\n"
"v_mfma_f32_16x16x32_bf16 v[196:199], a[124:127], a[76:79], v[196:199]\n"
"v_exp_f32 v192, v192\n"
"v_mfma_f32_16x16x32_bf16 v[96:99], a[160:163], v[212:215], v[96:99]\n"
"ds_read_b64_tr_b16 a[204:205], v248 offset:57856\n"
"v_add_f32 v244, v244, v192\n"
"v_mfma_f32_16x16x32_bf16 v[100:103], a[164:167], v[212:215], v[100:103]\n"
"v_exp_f32 v193, v193\n"
"v_mfma_f32_16x16x32_bf16 v[104:107], a[168:171], v[212:215], v[104:107]\n"
"v_add_f32 v244, v244, v193\n"
"v_cvt_pk_bf16_f32 v208, v192, v193\n"
"v_mfma_f32_16x16x32_bf16 v[108:111], a[172:175], v[212:215], v[108:111]\n"
"v_exp_f32 v194, v194\n"
"v_mfma_f32_16x16x32_bf16 v[112:115], a[176:179], v[212:215], v[112:115]\n"
"ds_read_b64_tr_b16 a[206:207], v248 offset:61952\n"
"v_add_f32 v244, v244, v194\n"
"v_mfma_f32_16x16x32_bf16 v[116:119], a[180:183], v[212:215], v[116:119]\n"
"v_exp_f32 v195, v195\n"
"v_mfma_f32_16x16x32_bf16 v[120:123], a[184:187], v[212:215], v[120:123]\n"
"v_add_f32 v244, v244, v195\n"
"v_cvt_pk_bf16_f32 v209, v194, v195\n"
"v_mfma_f32_16x16x32_bf16 v[124:127], a[188:191], v[212:215], v[124:127]\n"
"v_exp_f32 v196, v196\n"
"v_mfma_f32_16x16x32_bf16 v[200:203], a[96:99], a[80:83], v[236:239]\n"
"ds_read_b64_tr_b16 a[208:209], v247 offset:58368\n"
"v_add_f32 v244, v244, v196\n"
"v_mfma_f32_16x16x32_bf16 v[200:203], a[100:103], a[84:87], v[200:203]\n"
"v_exp_f32 v197, v197\n"
"v_mfma_f32_16x16x32_bf16 v[200:203], a[104:107], a[88:91], v[200:203]\n"
"v_add_f32 v244, v244, v197\n"
"v_cvt_pk_bf16_f32 v210, v196, v197\n"
"v_mfma_f32_16x16x32_bf16 v[200:203], a[108:111], a[92:95], v[200:203]\n"
"v_exp_f32 v198, v198\n"
"v_mfma_f32_16x16x32_bf16 v[204:207], a[112:115], a[80:83], v[236:239]\n"
"ds_read_b64_tr_b16 a[210:211], v247 offset:62464\n"
"v_add_f32 v244, v244, v198\n"
"v_mfma_f32_16x16x32_bf16 v[204:207], a[116:119], a[84:87], v[204:207]\n"
"v_exp_f32 v199, v199\n"
"v_mfma_f32_16x16x32_bf16 v[204:207], a[120:123], a[88:91], v[204:207]\n"
"v_add_f32 v244, v244, v199\n"
"v_cvt_pk_bf16_f32 v211, v198, v199\n"
"v_mfma_f32_16x16x32_bf16 v[204:207], a[124:127], a[92:95], v[204:207]\n"
"v_exp_f32 v200, v200\n"
"v_mfma_f32_16x16x32_bf16 v[128:131], a[160:163], v[208:211], v[128:131]\n"
"ds_read_b64_tr_b16 a[212:213], v248 offset:58368\n"
"v_add_f32 v245, v245, v200\n"
"v_mfma_f32_16x16x32_bf16 v[132:135], a[164:167], v[208:211], v[132:135]\n"
"v_exp_f32 v201, v201\n"
"v_mfma_f32_16x16x32_bf16 v[136:139], a[168:171], v[208:211], v[136:139]\n"
"v_add_f32 v245, v245, v201\n"
"v_cvt_pk_bf16_f32 v212, v200, v201\n"
"v_mfma_f32_16x16x32_bf16 v[140:143], a[172:175], v[208:211], v[140:143]\n"
"v_exp_f32 v202, v202\n"
"v_mfma_f32_16x16x32_bf16 v[144:147], a[176:179], v[208:211], v[144:147]\n"
"ds_read_b64_tr_b16 a[214:215], v248 offset:62464\n"
"v_add_f32 v245, v245, v202\n"
"v_mfma_f32_16x16x32_bf16 v[148:151], a[180:183], v[208:211], v[148:151]\n"
"v_exp_f32 v203, v203\n"
"v_mfma_f32_16x16x32_bf16 v[152:155], a[184:187], v[208:211], v[152:155]\n"
"v_add_f32 v245, v245, v203\n"
"v_cvt_pk_bf16_f32 v213, v202, v203\n"
"v_mfma_f32_16x16x32_bf16 v[156:159], a[188:191], v[208:211], v[156:159]\n"
"s_waitcnt lgkmcnt(2)\n"
"v_exp_f32 v204, v204\n"
"v_mfma_f32_16x16x32_bf16 v[192:195], a[128:131], a[0:3], v[216:219]\n"
"ds_read_b64_tr_b16 a[216:217], v247 offset:58880\n"
"v_add_f32 v245, v245, v204\n"
"v_mfma_f32_16x16x32_bf16 v[192:195], a[132:135], a[4:7], v[192:195]\n"
"v_exp_f32 v205, v205\n"
"v_mfma_f32_16x16x32_bf16 v[192:195], a[136:139], a[8:11], v[192:195]\n"
"v_add_f32 v245, v245, v205\n"
"v_cvt_pk_bf16_f32 v214, v204, v205\n"
"v_mfma_f32_16x16x32_bf16 v[192:195], a[140:143], a[12:15], v[192:195]\n"
"v_exp_f32 v206, v206\n"
"v_mfma_f32_16x16x32_bf16 v[196:199], a[144:147], a[0:3], v[216:219]\n"
"ds_read_b64_tr_b16 a[218:219], v247 offset:62976\n"
"v_add_f32 v245, v245, v206\n"
"v_mfma_f32_16x16x32_bf16 v[196:199], a[148:151], a[4:7], v[196:199]\n"
"v_exp_f32 v207, v207\n"
"v_mfma_f32_16x16x32_bf16 v[196:199], a[152:155], a[8:11], v[196:199]\n"
"v_add_f32 v245, v245, v207\n"
"v_cvt_pk_bf16_f32 v215, v206, v207\n"
"v_mfma_f32_16x16x32_bf16 v[196:199], a[156:159], a[12:15], v[196:199]\n"
"v_exp_f32 v192, v192\n"
"v_mfma_f32_16x16x32_bf16 v[160:163], a[160:163], v[212:215], v[160:163]\n"
"ds_read_b64_tr_b16 a[220:221], v248 offset:58880\n"
"v_add_f32 v240, v240, v192\n"
"v_mfma_f32_16x16x32_bf16 v[164:167], a[164:167], v[212:215], v[164:167]\n"
"v_exp_f32 v193, v193\n"
"v_mfma_f32_16x16x32_bf16 v[168:171], a[168:171], v[212:215], v[168:171]\n"
"v_add_f32 v240, v240, v193\n"
"v_cvt_pk_bf16_f32 v208, v192, v193\n"
"v_mfma_f32_16x16x32_bf16 v[172:175], a[172:175], v[212:215], v[172:175]\n"
"v_exp_f32 v194, v194\n"
"v_mfma_f32_16x16x32_bf16 v[176:179], a[176:179], v[212:215], v[176:179]\n"
"ds_read_b64_tr_b16 a[222:223], v248 offset:62976\n"
"v_add_f32 v240, v240, v194\n"
"v_mfma_f32_16x16x32_bf16 v[180:183], a[180:183], v[212:215], v[180:183]\n"
"v_exp_f32 v195, v195\n"
"v_mfma_f32_16x16x32_bf16 v[184:187], a[184:187], v[212:215], v[184:187]\n"
"v_add_f32 v240, v240, v195\n"
"v_cvt_pk_bf16_f32 v209, v194, v195\n"
"v_mfma_f32_16x16x32_bf16 v[188:191], a[188:191], v[212:215], v[188:191]\n"
"LMA1%=:\n"
"v_exp_f32 v196, v196\n"
"v_mfma_f32_16x16x32_bf16 v[200:203], a[128:131], a[16:19], v[220:223]\n"
"ds_read_b128 a[96:99], v246 offset:0\n"
"v_add_f32 v240, v240, v196\n"
"v_mfma_f32_16x16x32_bf16 v[200:203], a[132:135], a[20:23], v[200:203]\n"
"v_exp_f32 v197, v197\n"
"v_mfma_f32_16x16x32_bf16 v[200:203], a[136:139], a[24:27], v[200:203]\n"
"v_add_f32 v240, v240, v197\n"
"v_cvt_pk_bf16_f32 v210, v196, v197\n"
"v_mfma_f32_16x16x32_bf16 v[200:203], a[140:143], a[28:31], v[200:203]\n"
"v_exp_f32 v198, v198\n"
"v_mfma_f32_16x16x32_bf16 v[204:207], a[144:147], a[16:19], v[220:223]\n"
"ds_read_b128 a[100:103], v246 offset:512\n"
"v_add_f32 v240, v240, v198\n"
"v_mfma_f32_16x16x32_bf16 v[204:207], a[148:151], a[20:23], v[204:207]\n"
"s_waitcnt lgkmcnt(2)\n"
"v_exp_f32 v199, v199\n"
"v_mfma_f32_16x16x32_bf16 v[204:207], a[152:155], a[24:27], v[204:207]\n"
"v_add_f32 v240, v240, v199\n"
"v_cvt_pk_bf16_f32 v211, v198, v199\n"
"v_mfma_f32_16x16x32_bf16 v[204:207], a[156:159], a[28:31], v[204:207]\n"
"s_barrier\n"
"v_exp_f32 v200, v200\n"
"v_mfma_f32_16x16x32_bf16 v[0:3], a[192:195], v[208:211], v[0:3]\n"
"ds_read_b128 a[104:107], v246 offset:1024\n"
"v_add_f32 v241, v241, v200\n"
"v_mfma_f32_16x16x32_bf16 v[4:7], a[196:199], v[208:211], v[4:7]\n"
"v_exp_f32 v201, v201\n"
"v_mfma_f32_16x16x32_bf16 v[8:11], a[200:203], v[208:211], v[8:11]\n"
"v_add_f32 v241, v241, v201\n"
"v_cvt_pk_bf16_f32 v212, v200, v201\n"
"v_mfma_f32_16x16x32_bf16 v[12:15], a[204:207], v[208:211], v[12:15]\n"
"v_exp_f32 v202, v202\n"
"v_mfma_f32_16x16x32_bf16 v[16:19], a[208:211], v[208:211], v[16:19]\n"
"ds_read_b128 a[108:111], v246 offset:1536\n"
"v_add_f32 v241, v241, v202\n"
"v_mfma_f32_16x16x32_bf16 v[20:23], a[212:215], v[208:211], v[20:23]\n"
"v_exp_f32 v203, v203\n"
"v_mfma_f32_16x16x32_bf16 v[24:27], a[216:219], v[208:211], v[24:27]\n"
"v_add_f32 v241, v241, v203\n"
"v_cvt_pk_bf16_f32 v213, v202, v203\n"
"v_mfma_f32_16x16x32_bf16 v[28:31], a[220:223], v[208:211], v[28:31]\n"
"v_exp_f32 v204, v204\n"
"v_mfma_f32_16x16x32_bf16 v[192:195], a[128:131], a[32:35], v[224:227]\n"
"ds_read_b128 a[112:115], v246 offset:4096\n"
"v_add_f32 v241, v241, v204\n"
"v_mfma_f32_16x16x32_bf16 v[192:195], a[132:135], a[36:39], v[192:195]\n"
"v_exp_f32 v205, v205\n"
"v_mfma_f32_16x16x32_bf16 v[192:195], a[136:139], a[40:43], v[192:195]\n"
"v_add_f32 v241, v241, v205\n"
"v_cvt_pk_bf16_f32 v214, v204, v205\n"
"v_mfma_f32_16x16x32_bf16 v[192:195], a[140:143], a[44:47], v[192:195]\n"
"v_exp_f32 v206, v206\n"
"v_mfma_f32_16x16x32_bf16 v[196:199], a[144:147], a[32:35], v[224:227]\n"
"ds_read_b128 a[116:119], v246 offset:4608\n"
"v_add_f32 v241, v241, v206\n"
"v_mfma_f32_16x16x32_bf16 v[196:199], a[148:151], a[36:39], v[196:199]\n"
"v_exp_f32 v207, v207\n"
"v_mfma_f32_16x16x32_bf16 v[196:199], a[152:155], a[40:43], v[196:199]\n"
"v_add_f32 v241, v241, v207\n"
"v_cvt_pk_bf16_f32 v215, v206, v207\n"
"v_mfma_f32_16x16x32_bf16 v[196:199], a[156:159], a[44:47], v[196:199]\n"
"v_exp_f32 v192, v192\n"
"v_mfma_f32_16x16x32_bf16 v[32:35], a[192:195], v[212:215], v[32:35]\n"
"ds_read_b128 a[120:123], v246 offset:5120\n"
"v_add_f32 v242, v242, v192\n"
"v_mfma_f32_16x16x32_bf16 v[36:39], a[196:199], v[212:215], v[36:39]\n"
"v_exp_f32 v193, v193\n"
"v_mfma_f32_16x16x32_bf16 v[40:43], a[200:203], v[212:215], v[40:43]\n"
"v_add_f32 v242, v242, v193\n"
"v_cvt_pk_bf16_f32 v208, v192, v193\n"
"v_mfma_f32_16x16x32_bf16 v[44:47], a[204:207], v[212:215], v[44:47]\n"
"v_exp_f32 v194, v194\n"
"v_mfma_f32_16x16x32_bf16 v[48:51], a[208:211], v[212:215], v[48:51]\n"
"ds_read_b128 a[124:127], v246 offset:5632\n"
"v_add_f32 v242, v242, v194\n"
"v_mfma_f32_16x16x32_bf16 v[52:55], a[212:215], v[212:215], v[52:55]\n"
"v_exp_f32 v195, v195\n"
"v_mfma_f32_16x16x32_bf16 v[56:59], a[216:219], v[212:215], v[56:59]\n"
"v_add_f32 v242, v242, v195\n"
"v_cvt_pk_bf16_f32 v209, v194, v195\n"
"v_mfma_f32_16x16x32_bf16 v[60:63], a[220:223], v[212:215], v[60:63]\n"
"v_exp_f32 v196, v196\n"
"v_mfma_f32_16x16x32_bf16 v[200:203], a[128:131], a[48:51], v[228:231]\n"
"ds_read_b64_tr_b16 a[160:161], v247 offset:32768\n"
"v_add_f32 v242, v242, v196\n"
"v_mfma_f32_16x16x32_bf16 v[200:203], a[132:135], a[52:55], v[200:203]\n"
"v_exp_f32 v197, v197\n"
"v_mfma_f32_16x16x32_bf16 v[200:203], a[136:139], a[56:59], v[200:203]\n"
"v_add_f32 v242, v242, v197\n"
"v_cvt_pk_bf16_f32 v210, v196, v197\n"
"v_mfma_f32_16x16x32_bf16 v[200:203], a[140:143], a[60:63], v[200:203]\n"
"v_exp_f32 v198, v198\n"
"v_mfma_f32_16x16x32_bf16 v[204:207], a[144:147], a[48:51], v[228:231]\n"
"ds_read_b64_tr_b16 a[162:163], v247 offset:36864\n"
"v_add_f32 v242, v242, v198\n"
"v_mfma_f32_16x16x32_bf16 v[204:207], a[148:151], a[52:55], v[204:207]\n"
"v_exp_f32 v199, v199\n"
"v_mfma_f32_16x16x32_bf16 v[204:207], a[152:155], a[56:59], v[204:207]\n"
"v_add_f32 v242, v242, v199\n"
"v_cvt_pk_bf16_f32 v211, v198, v199\n"
"v_mfma_f32_16x16x32_bf16 v[204:207], a[156:159], a[60:63], v[204:207]\n"
"v_exp_f32 v200, v200\n"
"v_mfma_f32_16x16x32_bf16 v[64:67], a[192:195], v[208:211], v[64:67]\n"
"ds_read_b64_tr_b16 a[164:165], v248 offset:32768\n"
"v_add_f32 v243, v243, v200\n"
"v_mfma_f32_16x16x32_bf16 v[68:71], a[196:199], v[208:211], v[68:71]\n"
"v_exp_f32 v201, v201\n"
"v_mfma_f32_16x16x32_bf16 v[72:75], a[200:203], v[208:211], v[72:75]\n"
"v_add_f32 v243, v243, v201\n"
"v_cvt_pk_bf16_f32 v212, v200, v201\n"
"v_mfma_f32_16x16x32_bf16 v[76:79], a[204:207], v[208:211], v[76:79]\n"
"v_exp_f32 v202, v202\n"
"v_mfma_f32_16x16x32_bf16 v[80:83], a[208:211], v[208:211], v[80:83]\n"
"ds_read_b64_tr_b16 a[166:167], v248 offset:36864\n"
"v_add_f32 v243, v243, v202\n"
"v_mfma_f32_16x16x32_bf16 v[84:87], a[212:215], v[208:211], v[84:87]\n"
"v_exp_f32 v203, v203\n"
"v_mfma_f32_16x16x32_bf16 v[88:91], a[216:219], v[208:211], v[88:91]\n"
"v_add_f32 v243, v243, v203\n"
"v_cvt_pk_bf16_f32 v213, v202, v203\n"
"v_mfma_f32_16x16x32_bf16 v[92:95], a[220:223], v[208:211], v[92:95]\n"
"v_exp_f32 v204, v204\n"
"v_mfma_f32_16x16x32_bf16 v[192:195], a[128:131], a[64:67], v[232:235]\n"
"ds_read_b64_tr_b16 a[168:169], v247 offset:33280\n"
"v_add_f32 v243, v243, v204\n"
"v_mfma_f32_16x16x32_bf16 v[192:195], a[132:135], a[68:71], v[192:195]\n"
"v_exp_f32 v205, v205\n"
"v_mfma_f32_16x16x32_bf16 v[192:195], a[136:139], a[72:75], v[192:195]\n"
"v_add_f32 v243, v243, v205\n"
"v_cvt_pk_bf16_f32 v214, v204, v205\n"
"v_mfma_f32_16x16x32_bf16 v[192:195], a[140:143], a[76:79], v[192:195]\n"
"v_exp_f32 v206, v206\n"
"v_mfma_f32_16x16x32_bf16 v[196:199], a[144:147], a[64:67], v[232:235]\n"
"ds_read_b64_tr_b16 a[170:171], v247 offset:37376\n"
"v_add_f32 v243, v243, v206\n"
"v_mfma_f32_16x16x32_bf16 v[196:199], a[148:151], a[68:71], v[196:199]\n"
"v_exp_f32 v207, v207\n"
"v_mfma_f32_16x16x32_bf16 v[196:199], a[152:155], a[72:75], v[196:199]\n"
"v_add_f32 v243, v243, v207\n"
"v_cvt_pk_bf16_f32 v215, v206, v207\n"
"v_mfma_f32_16x16x32_bf16 v[196:199], a[156:159], a[76:79], v[196:199]\n"
"v_exp_f32 v192, v192\n"
"v_mfma_f32_16x16x32_bf16 v[96:99], a[192:195], v[212:215], v[96:99]\n"
"ds_read_b64_tr_b16 a[172:173], v248 offset:33280\n"
"v_add_f32 v244, v244, v192\n"
"v_mfma_f32_16x16x32_bf16 v[100:103], a[196:199], v[212:215], v[100:103]\n"
"v_exp_f32 v193, v193\n"
"v_mfma_f32_16x16x32_bf16 v[104:107], a[200:203], v[212:215], v[104:107]\n"
"v_add_f32 v244, v244, v193\n"
"v_cvt_pk_bf16_f32 v208, v192, v193\n"
"v_mfma_f32_16x16x32_bf16 v[108:111], a[204:207], v[212:215], v[108:111]\n"
"v_exp_f32 v194, v194\n"
"v_mfma_f32_16x16x32_bf16 v[112:115], a[208:211], v[212:215], v[112:115]\n"
"ds_read_b64_tr_b16 a[174:175], v248 offset:37376\n"
"v_add_f32 v244, v244, v194\n"
"v_mfma_f32_16x16x32_bf16 v[116:119], a[212:215], v[212:215], v[116:119]\n"
"v_exp_f32 v195, v195\n"
"v_mfma_f32_16x16x32_bf16 v[120:123], a[216:219], v[212:215], v[120:123]\n"
"v_add_f32 v244, v244, v195\n"
"v_cvt_pk_bf16_f32 v209, v194, v195\n"
"v_mfma_f32_16x16x32_bf16 v[124:127], a[220:223], v[212:215], v[124:127]\n"
"v_exp_f32 v196, v196\n"
"v_mfma_f32_16x16x32_bf16 v[200:203], a[128:131], a[80:83], v[236:239]\n"
"ds_read_b64_tr_b16 a[176:177], v247 offset:33792\n"
"v_add_f32 v244, v244, v196\n"
"v_mfma_f32_16x16x32_bf16 v[200:203], a[132:135], a[84:87], v[200:203]\n"
"v_exp_f32 v197, v197\n"
"v_mfma_f32_16x16x32_bf16 v[200:203], a[136:139], a[88:91], v[200:203]\n"
"v_add_f32 v244, v244, v197\n"
"v_cvt_pk_bf16_f32 v210, v196, v197\n"
"v_mfma_f32_16x16x32_bf16 v[200:203], a[140:143], a[92:95], v[200:203]\n"
"v_exp_f32 v198, v198\n"
"v_mfma_f32_16x16x32_bf16 v[204:207], a[144:147], a[80:83], v[236:239]\n"
"ds_read_b64_tr_b16 a[178:179], v247 offset:37888\n"
"v_add_f32 v244, v244, v198\n"
"v_mfma_f32_16x16x32_bf16 v[204:207], a[148:151], a[84:87], v[204:207]\n"
"v_exp_f32 v199, v199\n"
"v_mfma_f32_16x16x32_bf16 v[204:207], a[152:155], a[88:91], v[204:207]\n"
"v_add_f32 v244, v244, v199\n"
"v_cvt_pk_bf16_f32 v211, v198, v199\n"
"v_mfma_f32_16x16x32_bf16 v[204:207], a[156:159], a[92:95], v[204:207]\n"
"v_exp_f32 v200, v200\n"
"v_mfma_f32_16x16x32_bf16 v[128:131], a[192:195], v[208:211], v[128:131]\n"
"ds_read_b64_tr_b16 a[180:181], v248 offset:33792\n"
"v_add_f32 v245, v245, v200\n"
"v_mfma_f32_16x16x32_bf16 v[132:135], a[196:199], v[208:211], v[132:135]\n"
"v_exp_f32 v201, v201\n"
"v_mfma_f32_16x16x32_bf16 v[136:139], a[200:203], v[208:211], v[136:139]\n"
"v_add_f32 v245, v245, v201\n"
"v_cvt_pk_bf16_f32 v212, v200, v201\n"
"v_mfma_f32_16x16x32_bf16 v[140:143], a[204:207], v[208:211], v[140:143]\n"
"v_exp_f32 v202, v202\n"
"v_mfma_f32_16x16x32_bf16 v[144:147], a[208:211], v[208:211], v[144:147]\n"
"ds_read_b64_tr_b16 a[182:183], v248 offset:37888\n"
"v_add_f32 v245, v245, v202\n"
"v_mfma_f32_16x16x32_bf16 v[148:151], a[212:215], v[208:211], v[148:151]\n"
"v_exp_f32 v203, v203\n"
"v_mfma_f32_16x16x32_bf16 v[152:155], a[216:219], v[208:211], v[152:155]\n"
"v_add_f32 v245, v245, v203\n"
"v_cvt_pk_bf16_f32 v213, v202, v203\n"
"v_mfma_f32_16x16x32_bf16 v[156:159], a[220:223], v[208:211], v[156:159]\n"
"s_waitcnt lgkmcnt(2)\n"
"v_exp_f32 v204, v204\n"
"v_mfma_f32_16x16x32_bf16 v[192:195], a[96:99], a[0:3], v[216:219]\n"
"ds_read_b64_tr_b16 a[184:185], v247 offset:34304\n"
"v_add_f32 v245, v245, v204\n"
"v_mfma_f32_16x16x32_bf16 v[192:195], a[100:103], a[4:7], v[192:195]\n"
"v_exp_f32 v205, v205\n"
"v_mfma_f32_16x16x32_bf16 v[192:195], a[104:107], a[8:11], v[192:195]\n"
"v_add_f32 v245, v245, v205\n"
"v_cvt_pk_bf16_f32 v214, v204, v205\n"
"v_mfma_f32_16x16x32_bf16 v[192:195], a[108:111], a[12:15], v[192:195]\n"
"v_exp_f32 v206, v206\n"
"v_mfma_f32_16x16x32_bf16 v[196:199], a[112:115], a[0:3], v[216:219]\n"
"ds_read_b64_tr_b16 a[186:187], v247 offset:38400\n"
"v_add_f32 v245, v245, v206\n"
"v_mfma_f32_16x16x32_bf16 v[196:199], a[116:119], a[4:7], v[196:199]\n"
"v_exp_f32 v207, v207\n"
"v_mfma_f32_16x16x32_bf16 v[196:199], a[120:123], a[8:11], v[196:199]\n"
"v_add_f32 v245, v245, v207\n"
"v_cvt_pk_bf16_f32 v215, v206, v207\n"
"v_mfma_f32_16x16x32_bf16 v[196:199], a[124:127], a[12:15], v[196:199]\n"
"v_exp_f32 v192, v192\n"
"v_mfma_f32_16x16x32_bf16 v[160:163], a[192:195], v[212:215], v[160:163]\n"
"ds_read_b64_tr_b16 a[188:189], v248 offset:34304\n"
"v_add_f32 v240, v240, v192\n"
"v_mfma_f32_16x16x32_bf16 v[164:167], a[196:199], v[212:215], v[164:167]\n"
"v_exp_f32 v193, v193\n"
"v_mfma_f32_16x16x32_bf16 v[168:171], a[200:203], v[212:215], v[168:171]\n"
"v_add_f32 v240, v240, v193\n"
"v_cvt_pk_bf16_f32 v208, v192, v193\n"
"v_mfma_f32_16x16x32_bf16 v[172:175], a[204:207], v[212:215], v[172:175]\n"
"v_exp_f32 v194, v194\n"
"v_mfma_f32_16x16x32_bf16 v[176:179], a[208:211], v[212:215], v[176:179]\n"
"ds_read_b64_tr_b16 a[190:191], v248 offset:38400\n"
"v_add_f32 v240, v240, v194\n"
"v_mfma_f32_16x16x32_bf16 v[180:183], a[212:215], v[212:215], v[180:183]\n"
"v_exp_f32 v195, v195\n"
"v_mfma_f32_16x16x32_bf16 v[184:187], a[216:219], v[212:215], v[184:187]\n"
"v_add_f32 v240, v240, v195\n"
"v_cvt_pk_bf16_f32 v209, v194, v195\n"
"v_mfma_f32_16x16x32_bf16 v[188:191], a[220:223], v[212:215], v[188:191]\n"
"LMA2%=:\n"
"v_exp_f32 v196, v196\n"
"v_mfma_f32_16x16x32_bf16 v[200:203], a[96:99], a[16:19], v[220:223]\n"
"ds_read_b128 a[128:131], v246 offset:8192\n"
"v_add_f32 v240, v240, v196\n"
"v_mfma_f32_16x16x32_bf16 v[200:203], a[100:103], a[20:23], v[200:203]\n"
"s_add_u32 s54, s54, 1\n"
"v_exp_f32 v197, v197\n"
"v_mfma_f32_16x16x32_bf16 v[200:203], a[104:107], a[24:27], v[200:203]\n"
"v_add_f32 v240, v240, v197\n"
"v_cvt_pk_bf16_f32 v210, v196, v197\n"
"v_mfma_f32_16x16x32_bf16 v[200:203], a[108:111], a[28:31], v[200:203]\n"
"v_exp_f32 v198, v198\n"
"v_mfma_f32_16x16x32_bf16 v[204:207], a[112:115], a[16:19], v[220:223]\n"
"ds_read_b128 a[132:135], v246 offset:8704\n"
"v_add_f32 v240, v240, v198\n"
"v_mfma_f32_16x16x32_bf16 v[204:207], a[116:119], a[20:23], v[204:207]\n"
"s_waitcnt lgkmcnt(2)\n"
"v_exp_f32 v199, v199\n"
"v_mfma_f32_16x16x32_bf16 v[204:207], a[120:123], a[24:27], v[204:207]\n"
"v_add_f32 v240, v240, v199\n"
"v_cvt_pk_bf16_f32 v211, v198, v199\n"
"v_mfma_f32_16x16x32_bf16 v[204:207], a[124:127], a[28:31], v[204:207]\n"
"s_barrier\n"
"v_exp_f32 v200, v200\n"
"v_mfma_f32_16x16x32_bf16 v[0:3], a[160:163], v[208:211], v[0:3]\n"
"ds_read_b128 a[136:139], v246 offset:9216\n"
"v_add_f32 v241, v241, v200\n"
"v_mfma_f32_16x16x32_bf16 v[4:7], a[164:167], v[208:211], v[4:7]\n"
"v_exp_f32 v201, v201\n"
"v_mfma_f32_16x16x32_bf16 v[8:11], a[168:171], v[208:211], v[8:11]\n"
"v_add_f32 v241, v241, v201\n"
"v_cvt_pk_bf16_f32 v212, v200, v201\n"
"v_mfma_f32_16x16x32_bf16 v[12:15], a[172:175], v[208:211], v[12:15]\n"
"v_exp_f32 v202, v202\n"
"v_mfma_f32_16x16x32_bf16 v[16:19], a[176:179], v[208:211], v[16:19]\n"
"ds_read_b128 a[140:143], v246 offset:9728\n"
"v_add_f32 v241, v241, v202\n"
"v_mfma_f32_16x16x32_bf16 v[20:23], a[180:183], v[208:211], v[20:23]\n"
"v_exp_f32 v203, v203\n"
"v_mfma_f32_16x16x32_bf16 v[24:27], a[184:187], v[208:211], v[24:27]\n"
"v_add_f32 v241, v241, v203\n"
"v_cvt_pk_bf16_f32 v213, v202, v203\n"
"v_mfma_f32_16x16x32_bf16 v[28:31], a[188:191], v[208:211], v[28:31]\n"
"v_exp_f32 v204, v204\n"
"v_mfma_f32_16x16x32_bf16 v[192:195], a[96:99], a[32:35], v[224:227]\n"
"ds_read_b128 a[144:147], v246 offset:12288\n"
"v_add_f32 v241, v241, v204\n"
"v_mfma_f32_16x16x32_bf16 v[192:195], a[100:103], a[36:39], v[192:195]\n"
"v_exp_f32 v205, v205\n"
"v_mfma_f32_16x16x32_bf16 v[192:195], a[104:107], a[40:43], v[192:195]\n"
"v_add_f32 v241, v241, v205\n"
"v_cvt_pk_bf16_f32 v214, v204, v205\n"
"v_mfma_f32_16x16x32_bf16 v[192:195], a[108:111], a[44:47], v[192:195]\n"
"Lhop0%=:\n"
"s_cbranch_execz Lhop4%=\n"
"v_exp_f32 v206, v206\n"
"v_mfma_f32_16x16x32_bf16 v[196:199], a[112:115], a[32:35], v[224:227]\n"
"ds_read_b128 a[148:151], v246 offset:12800\n"
"v_add_f32 v241, v241, v206\n"
"v_mfma_f32_16x16x32_bf16 v[196:199], a[116:119], a[36:39], v[196:199]\n"
"v_exp_f32 v207, v207\n"
"v_mfma_f32_16x16x32_bf16 v[196:199], a[120:123], a[40:43], v[196:199]\n"
"v_add_f32 v241, v241, v207\n"
"v_cvt_pk_bf16_f32 v215, v206, v207\n"
"v_mfma_f32_16x16x32_bf16 v[196:199], a[124:127], a[44:47], v[196:199]\n"
"v_exp_f32 v192, v192\n"
"v_mfma_f32_16x16x32_bf16 v[32:35], a[160:163], v[212:215], v[32:35]\n"
"ds_read_b128 a[152:155], v246 offset:13312\n"
"v_add_f32 v242, v242, v192\n"
"v_mfma_f32_16x16x32_bf16 v[36:39], a[164:167], v[212:215], v[36:39]\n"
"v_exp_f32 v193, v193\n"
"v_mfma_f32_16x16x32_bf16 v[40:43], a[168:171], v[212:215], v[40:43]\n"
"v_add_f32 v242, v242, v193\n"
"v_cvt_pk_bf16_f32 v208, v192, v193\n"
"v_mfma_f32_16x16x32_bf16 v[44:47], a[172:175], v[212:215], v[44:47]\n"
"v_exp_f32 v194, v194\n"
"v_mfma_f32_16x16x32_bf16 v[48:51], a[176:179], v[212:215], v[48:51]\n"
"ds_read_b128 a[156:159], v246 offset:13824\n"
"v_add_f32 v242, v242, v194\n"
"v_mfma_f32_16x16x32_bf16 v[52:55], a[180:183], v[212:215], v[52:55]\n"
"v_exp_f32 v195, v195\n"
"v_mfma_f32_16x16x32_bf16 v[56:59], a[184:187], v[212:215], v[56:59]\n"
"v_add_f32 v242, v242, v195\n"
"v_cvt_pk_bf16_f32 v209, v194, v195\n"
"v_mfma_f32_16x16x32_bf16 v[60:63], a[188:191], v[212:215], v[60:63]\n"
"v_exp_f32 v196, v196\n"
"v_mfma_f32_16x16x32_bf16 v[200:203], a[96:99], a[48:51], v[228:231]\n"
"ds_read_b64_tr_b16 a[192:193], v247 offset:40960\n"
"v_add_f32 v242, v242, v196\n"
"v_mfma_f32_16x16x32_bf16 v[200:203], a[100:103], a[52:55], v[200:203]\n"
"v_exp_f32 v197, v197\n"
"v_mfma_f32_16x16x32_bf16 v[200:203], a[104:107], a[56:59], v[200:203]\n"
"v_add_f32 v242, v242, v197\n"
"v_cvt_pk_bf16_f32 v210, v196, v197\n"
"v_mfma_f32_16x16x32_bf16 v[200:203], a[108:111], a[60:63], v[200:203]\n"
"v_exp_f32 v198, v198\n"
"v_mfma_f32_16x16x32_bf16 v[204:207], a[112:115], a[48:51], v[228:231]\n"
"ds_read_b64_tr_b16 a[194:195], v247 offset:45056\n"
"v_add_f32 v242, v242, v198\n"
"v_mfma_f32_16x16x32_bf16 v[204:207], a[116:119], a[52:55], v[204:207]\n"
"v_exp_f32 v199, v199\n"
"v_mfma_f32_16x16x32_bf16 v[204:207], a[120:123], a[56:59], v[204:207]\n"
"v_add_f32 v242, v242, v199\n"
"v_cvt_pk_bf16_f32 v211, v198, v199\n"
"v_mfma_f32_16x16x32_bf16 v[204:207], a[124:127], a[60:63], v[204:207]\n"
"v_exp_f32 v200, v200\n"
"v_mfma_f32_16x16x32_bf16 v[64:67], a[160:163], v[208:211], v[64:67]\n"
"ds_read_b64_tr_b16 a[196:197], v248 offset:40960\n"
"v_add_f32 v243, v243, v200\n"
"v_mfma_f32_16x16x32_bf16 v[68:71], a[164:167], v[208:211], v[68:71]\n"
"v_exp_f32 v201, v201\n"
"v_mfma_f32_16x16x32_bf16 v[72:75], a[168:171], v[208:211], v[72:75]\n"
"v_add_f32 v243, v243, v201\n"
"v_cvt_pk_bf16_f32 v212, v200, v201\n"
"v_mfma_f32_16x16x32_bf16 v[76:79], a[172:175], v[208:211], v[76:79]\n"
"v_exp_f32 v202, v202\n"
"v_mfma_f32_16x16x32_bf16 v[80:83], a[176:179], v[208:211], v[80:83]\n"
"ds_read_b64_tr_b16 a[198:199], v248 offset:45056\n"
"v_add_f32 v243, v243, v202\n"
"v_mfma_f32_16x16x32_bf16 v[84:87], a[180:183], v[208:211], v[84:87]\n"
"v_exp_f32 v203, v203\n"
"v_mfma_f32_16x16x32_bf16 v[88:91], a[184:187], v[208:211], v[88:91]\n"
"v_add_f32 v243, v243, v203\n"
"v_cvt_pk_bf16_f32 v213, v202, v203\n"
"v_mfma_f32_16x16x32_bf16 v[92:95], a[188:191], v[208:211], v[92:95]\n"
"v_exp_f32 v204, v204\n"
"v_mfma_f32_16x16x32_bf16 v[192:195], a[96:99], a[64:67], v[232:235]\n"
"ds_read_b64_tr_b16 a[200:201], v247 offset:41472\n"
"v_add_f32 v243, v243, v204\n"
"v_mfma_f32_16x16x32_bf16 v[192:195], a[100:103], a[68:71], v[192:195]\n"
"v_exp_f32 v205, v205\n"
"v_mfma_f32_16x16x32_bf16 v[192:195], a[104:107], a[72:75], v[192:195]\n"
"v_add_f32 v243, v243, v205\n"
"v_cvt_pk_bf16_f32 v214, v204, v205\n"
"v_mfma_f32_16x16x32_bf16 v[192:195], a[108:111], a[76:79], v[192:195]\n"
"v_exp_f32 v206, v206\n"
"v_mfma_f32_16x16x32_bf16 v[196:199], a[112:115], a[64:67], v[232:235]\n"
"ds_read_b64_tr_b16 a[202:203], v247 offset:45568\n"
"v_add_f32 v243, v243, v206\n"
"v_mfma_f32_16x16x32_bf16 v[196:199], a[116:119], a[68:71], v[196:199]\n"
"v_exp_f32 v207, v207\n"
"v_mfma_f32_16x16x32_bf16 v[196:199], a[120:123], a[72:75], v[196:199]\n"
"v_add_f32 v243, v243, v207\n"
"v_cvt_pk_bf16_f32 v215, v206, v207\n"
"v_mfma_f32_16x16x32_bf16 v[196:199], a[124:127], a[76:79], v[196:199]\n"
"v_exp_f32 v192, v192\n"
"v_mfma_f32_16x16x32_bf16 v[96:99], a[160:163], v[212:215], v[96:99]\n"
"ds_read_b64_tr_b16 a[204:205], v248 offset:41472\n"
"v_add_f32 v244, v244, v192\n"
"v_mfma_f32_16x16x32_bf16 v[100:103], a[164:167], v[212:215], v[100:103]\n"
"v_exp_f32 v193, v193\n"
"v_mfma_f32_16x16x32_bf16 v[104:107], a[168:171], v[212:215], v[104:107]\n"
"v_add_f32 v244, v244, v193\n"
"v_cvt_pk_bf16_f32 v208, v192, v193\n"
"v_mfma_f32_16x16x32_bf16 v[108:111], a[172:175], v[212:215], v[108:111]\n"
"v_exp_f32 v194, v194\n"
"v_mfma_f32_16x16x32_bf16 v[112:115], a[176:179], v[212:215], v[112:115]\n"
"ds_read_b64_tr_b16 a[206:207], v248 offset:45568\n"
"v_add_f32 v244, v244, v194\n"
"v_mfma_f32_16x16x32_bf16 v[116:119], a[180:183], v[212:215], v[116:119]\n"
"v_exp_f32 v195, v195\n"
"v_mfma_f32_16x16x32_bf16 v[120:123], a[184:187], v[212:215], v[120:123]\n"
"v_add_f32 v244, v244, v195\n"
"v_cvt_pk_bf16_f32 v209, v194, v195\n"
"v_mfma_f32_16x16x32_bf16 v[124:127], a[188:191], v[212:215], v[124:127]\n"
"v_exp_f32 v196, v196\n"
"v_mfma_f32_16x16x32_bf16 v[200:203], a[96:99], a[80:83], v[236:239]\n"
"ds_read_b64_tr_b16 a[208:209], v247 offset:41984\n"
"v_add_f32 v244, v244, v196\n"
"v_mfma_f32_16x16x32_bf16 v[200:203], a[100:103], a[84:87], v[200:203]\n"
"v_exp_f32 v197, v197\n"
"v_mfma_f32_16x16x32_bf16 v[200:203], a[104:107], a[88:91], v[200:203]\n"
"v_add_f32 v244, v244, v197\n"
"v_cvt_pk_bf16_f32 v210, v196, v197\n"
"v_mfma_f32_16x16x32_bf16 v[200:203], a[108:111], a[92:95], v[200:203]\n"
"v_exp_f32 v198, v198\n"
"v_mfma_f32_16x16x32_bf16 v[204:207], a[112:115], a[80:83], v[236:239]\n"
"ds_read_b64_tr_b16 a[210:211], v247 offset:46080\n"
"v_add_f32 v244, v244, v198\n"
"v_mfma_f32_16x16x32_bf16 v[204:207], a[116:119], a[84:87], v[204:207]\n"
"v_exp_f32 v199, v199\n"
"v_mfma_f32_16x16x32_bf16 v[204:207], a[120:123], a[88:91], v[204:207]\n"
"v_add_f32 v244, v244, v199\n"
"v_cvt_pk_bf16_f32 v211, v198, v199\n"
"v_mfma_f32_16x16x32_bf16 v[204:207], a[124:127], a[92:95], v[204:207]\n"
"v_exp_f32 v200, v200\n"
"v_mfma_f32_16x16x32_bf16 v[128:131], a[160:163], v[208:211], v[128:131]\n"
"ds_read_b64_tr_b16 a[212:213], v248 offset:41984\n"
"v_add_f32 v245, v245, v200\n"
"v_mfma_f32_16x16x32_bf16 v[132:135], a[164:167], v[208:211], v[132:135]\n"
"v_exp_f32 v201, v201\n"
"v_mfma_f32_16x16x32_bf16 v[136:139], a[168:171], v[208:211], v[136:139]\n"
"v_add_f32 v245, v245, v201\n"
"v_cvt_pk_bf16_f32 v212, v200, v201\n"
"v_mfma_f32_16x16x32_bf16 v[140:143], a[172:175], v[208:211], v[140:143]\n"
"v_exp_f32 v202, v202\n"
"v_mfma_f32_16x16x32_bf16 v[144:147], a[176:179], v[208:211], v[144:147]\n"
"ds_read_b64_tr_b16 a[214:215], v248 offset:46080\n"
"v_add_f32 v245, v245, v202\n"
"v_mfma_f32_16x16x32_bf16 v[148:151], a[180:183], v[208:211], v[148:151]\n"
"v_exp_f32 v203, v203\n"
"v_mfma_f32_16x16x32_bf16 v[152:155], a[184:187], v[208:211], v[152:155]\n"
"v_add_f32 v245, v245, v203\n"
"v_cvt_pk_bf16_f32 v213, v202, v203\n"
"v_mfma_f32_16x16x32_bf16 v[156:159], a[188:191], v[208:211], v[156:159]\n"
"s_waitcnt lgkmcnt(2)\n"
"v_exp_f32 v204, v204\n"
"v_mfma_f32_16x16x32_bf16 v[192:195], a[128:131], a[0:3], v[216:219]\n"
"ds_read_b64_tr_b16 a[216:217], v247 offset:42496\n"
"v_add_f32 v245, v245, v204\n"
"v_mfma_f32_16x16x32_bf16 v[192:195], a[132:135], a[4:7], v[192:195]\n"
"v_exp_f32 v205, v205\n"
"v_mfma_f32_16x16x32_bf16 v[192:195], a[136:139], a[8:11], v[192:195]\n"
"v_add_f32 v245, v245, v205\n"
"v_cvt_pk_bf16_f32 v214, v204, v205\n"
"v_mfma_f32_16x16x32_bf16 v[192:195], a[140:143], a[12:15], v[192:195]\n"
"v_exp_f32 v206, v206\n"
"v_mfma_f32_16x16x32_bf16 v[196:199], a[144:147], a[0:3], v[216:219]\n"
"ds_read_b64_tr_b16 a[218:219], v247 offset:46592\n"
"v_add_f32 v245, v245, v206\n"
"v_mfma_f32_16x16x32_bf16 v[196:199], a[148:151], a[4:7], v[196:199]\n"
"v_exp_f32 v207, v207\n"
"v_mfma_f32_16x16x32_bf16 v[196:199], a[152:155], a[8:11], v[196:199]\n"
"v_add_f32 v245, v245, v207\n"
"v_cvt_pk_bf16_f32 v215, v206, v207\n"
"v_mfma_f32_16x16x32_bf16 v[196:199], a[156:159], a[12:15], v[196:199]\n"
"v_exp_f32 v192, v192\n"
"v_mfma_f32_16x16x32_bf16 v[160:163], a[160:163], v[212:215], v[160:163]\n"
"ds_read_b64_tr_b16 a[220:221], v248 offset:42496\n"
"v_add_f32 v240, v240, v192\n"
"v_mfma_f32_16x16x32_bf16 v[164:167], a[164:167], v[212:215], v[164:167]\n"
"v_exp_f32 v193, v193\n"
"v_mfma_f32_16x16x32_bf16 v[168:171], a[168:171], v[212:215], v[168:171]\n"
"v_add_f32 v240, v240, v193\n"
"v_cvt_pk_bf16_f32 v208, v192, v193\n"
"v_mfma_f32_16x16x32_bf16 v[172:175], a[172:175], v[212:215], v[172:175]\n"
"v_exp_f32 v194, v194\n"
"v_mfma_f32_16x16x32_bf16 v[176:179], a[176:179], v[212:215], v[176:179]\n"
"ds_read_b64_tr_b16 a[222:223], v248 offset:46592\n"
"v_add_f32 v240, v240, v194\n"
"v_mfma_f32_16x16x32_bf16 v[180:183], a[180:183], v[212:215], v[180:183]\n"
"v_exp_f32 v195, v195\n"
"v_mfma_f32_16x16x32_bf16 v[184:187], a[184:187], v[212:215], v[184:187]\n"
"v_add_f32 v240, v240, v195\n"
"v_cvt_pk_bf16_f32 v209, v194, v195\n"
"v_mfma_f32_16x16x32_bf16 v[188:191], a[188:191], v[212:215], v[188:191]\n"
"LMA3%=:\n"
"v_exp_f32 v196, v196\n"
"v_mfma_f32_16x16x32_bf16 v[200:203], a[128:131], a[16:19], v[220:223]\n"
"ds_read_b128 a[96:99], v246 offset:16384\n"
"v_add_f32 v240, v240, v196\n"
"v_mfma_f32_16x16x32_bf16 v[200:203], a[132:135], a[20:23], v[200:203]\n"
"v_exp_f32 v197, v197\n"
"v_mfma_f32_16x16x32_bf16 v[200:203], a[136:139], a[24:27], v[200:203]\n"
"v_add_f32 v240, v240, v197\n"
"v_cvt_pk_bf16_f32 v210, v196, v197\n"
"v_mfma_f32_16x16x32_bf16 v[200:203], a[140:143], a[28:31], v[200:203]\n"
"v_exp_f32 v198, v198\n"
"v_mfma_f32_16x16x32_bf16 v[204:207], a[144:147], a[16:19], v[220:223]\n"
"ds_read_b128 a[100:103], v246 offset:16896\n"
"v_add_f32 v240, v240, v198\n"
"v_mfma_f32_16x16x32_bf16 v[204:207], a[148:151], a[20:23], v[204:207]\n"
"s_waitcnt lgkmcnt(2)\n"
"v_exp_f32 v199, v199\n"
"v_mfma_f32_16x16x32_bf16 v[204:207], a[152:155], a[24:27], v[204:207]\n"
"v_add_f32 v240, v240, v199\n"
"v_cvt_pk_bf16_f32 v211, v198, v199\n"
"v_mfma_f32_16x16x32_bf16 v[204:207], a[156:159], a[28:31], v[204:207]\n"
"s_barrier\n"
"v_exp_f32 v200, v200\n"
"v_mfma_f32_16x16x32_bf16 v[0:3], a[192:195], v[208:211], v[0:3]\n"
"ds_read_b128 a[104:107], v246 offset:17408\n"
"v_add_f32 v241, v241, v200\n"
"v_mfma_f32_16x16x32_bf16 v[4:7], a[196:199], v[208:211], v[4:7]\n"
"v_exp_f32 v201, v201\n"
"v_mfma_f32_16x16x32_bf16 v[8:11], a[200:203], v[208:211], v[8:11]\n"
"v_add_f32 v241, v241, v201\n"
"v_cvt_pk_bf16_f32 v212, v200, v201\n"
"v_mfma_f32_16x16x32_bf16 v[12:15], a[204:207], v[208:211], v[12:15]\n"
"v_exp_f32 v202, v202\n"
"v_mfma_f32_16x16x32_bf16 v[16:19], a[208:211], v[208:211], v[16:19]\n"
"ds_read_b128 a[108:111], v246 offset:17920\n"
"v_add_f32 v241, v241, v202\n"
"v_mfma_f32_16x16x32_bf16 v[20:23], a[212:215], v[208:211], v[20:23]\n"
"v_exp_f32 v203, v203\n"
"v_mfma_f32_16x16x32_bf16 v[24:27], a[216:219], v[208:211], v[24:27]\n"
"v_add_f32 v241, v241, v203\n"
"v_cvt_pk_bf16_f32 v213, v202, v203\n"
"v_mfma_f32_16x16x32_bf16 v[28:31], a[220:223], v[208:211], v[28:31]\n"
"v_exp_f32 v204, v204\n"
"v_mfma_f32_16x16x32_bf16 v[192:195], a[128:131], a[32:35], v[224:227]\n"
"ds_read_b128 a[112:115], v246 offset:20480\n"
"v_add_f32 v241, v241, v204\n"
"v_mfma_f32_16x16x32_bf16 v[192:195], a[132:135], a[36:39], v[192:195]\n"
"v_exp_f32 v205, v205\n"
"v_mfma_f32_16x16x32_bf16 v[192:195], a[136:139], a[40:43], v[192:195]\n"
"v_add_f32 v241, v241, v205\n"
"v_cvt_pk_bf16_f32 v214, v204, v205\n"
"v_mfma_f32_16x16x32_bf16 v[192:195], a[140:143], a[44:47], v[192:195]\n"
"v_exp_f32 v206, v206\n"
"v_mfma_f32_16x16x32_bf16 v[196:199], a[144:147], a[32:35], v[224:227]\n"
"ds_read_b128 a[116:119], v246 offset:20992\n"
"v_add_f32 v241, v241, v206\n"
"v_mfma_f32_16x16x32_bf16 v[196:199], a[148:151], a[36:39], v[196:199]\n"
"v_exp_f32 v207, v207\n"
"v_mfma_f32_16x16x32_bf16 v[196:199], a[152:155], a[40:43], v[196:199]\n"
"v_add_f32 v241, v241, v207\n"
"v_cvt_pk_bf16_f32 v215, v206, v207\n"
"v_mfma_f32_16x16x32_bf16 v[196:199], a[156:159], a[44:47], v[196:199]\n"
"v_exp_f32 v192, v192\n"
"v_mfma_f32_16x16x32_bf16 v[32:35], a[192:195], v[212:215], v[32:35]\n"
"ds_read_b128 a[120:123], v246 offset:21504\n"
"v_add_f32 v242, v242, v192\n"
"v_mfma_f32_16x16x32_bf16 v[36:39], a[196:199], v[212:215], v[36:39]\n"
"v_exp_f32 v193, v193\n"
"v_mfma_f32_16x16x32_bf16 v[40:43], a[200:203], v[212:215], v[40:43]\n"
"v_add_f32 v242, v242, v193\n"
"v_cvt_pk_bf16_f32 v208, v192, v193\n"
"v_mfma_f32_16x16x32_bf16 v[44:47], a[204:207], v[212:215], v[44:47]\n"
"v_exp_f32 v194, v194\n"
"v_mfma_f32_16x16x32_bf16 v[48:51], a[208:211], v[212:215], v[48:51]\n"
"ds_read_b128 a[124:127], v246 offset:22016\n"
"v_add_f32 v242, v242, v194\n"
"v_mfma_f32_16x16x32_bf16 v[52:55], a[212:215], v[212:215], v[52:55]\n"
"v_exp_f32 v195, v195\n"
"v_mfma_f32_16x16x32_bf16 v[56:59], a[216:219], v[212:215], v[56:59]\n"
"v_add_f32 v242, v242, v195\n"
"v_cvt_pk_bf16_f32 v209, v194, v195\n"
"v_mfma_f32_16x16x32_bf16 v[60:63], a[220:223], v[212:215], v[60:63]\n"
"v_exp_f32 v196, v196\n"
"v_mfma_f32_16x16x32_bf16 v[200:203], a[128:131], a[48:51], v[228:231]\n"
"ds_read_b64_tr_b16 a[160:161], v247 offset:49152\n"
"v_add_f32 v242, v242, v196\n"
"v_mfma_f32_16x16x32_bf16 v[200:203], a[132:135], a[52:55], v[200:203]\n"
"v_exp_f32 v197, v197\n"
"v_mfma_f32_16x16x32_bf16 v[200:203], a[136:139], a[56:59], v[200:203]\n"
"v_add_f32 v242, v242, v197\n"
"v_cvt_pk_bf16_f32 v210, v196, v197\n"
"v_mfma_f32_16x16x32_bf16 v[200:203], a[140:143], a[60:63], v[200:203]\n"
"v_exp_f32 v198, v198\n"
"v_mfma_f32_16x16x32_bf16 v[204:207], a[144:147], a[48:51], v[228:231]\n"
"ds_read_b64_tr_b16 a[162:163], v247 offset:53248\n"
"v_add_f32 v242, v242, v198\n"
"v_mfma_f32_16x16x32_bf16 v[204:207], a[148:151], a[52:55], v[204:207]\n"
"v_exp_f32 v199, v199\n"
"v_mfma_f32_16x16x32_bf16 v[204:207], a[152:155], a[56:59], v[204:207]\n"
"v_add_f32 v242, v242, v199\n"
"v_cvt_pk_bf16_f32 v211, v198, v199\n"
"v_mfma_f32_16x16x32_bf16 v[204:207], a[156:159], a[60:63], v[204:207]\n"
"v_exp_f32 v200, v200\n"
"v_mfma_f32_16x16x32_bf16 v[64:67], a[192:195], v[208:211], v[64:67]\n"
"ds_read_b64_tr_b16 a[164:165], v248 offset:49152\n"
"v_add_f32 v243, v243, v200\n"
"v_mfma_f32_16x16x32_bf16 v[68:71], a[196:199], v[208:211], v[68:71]\n"
"v_exp_f32 v201, v201\n"
"v_mfma_f32_16x16x32_bf16 v[72:75], a[200:203], v[208:211], v[72:75]\n"
"v_add_f32 v243, v243, v201\n"
"v_cvt_pk_bf16_f32 v212, v200, v201\n"
"v_mfma_f32_16x16x32_bf16 v[76:79], a[204:207], v[208:211], v[76:79]\n"
"v_exp_f32 v202, v202\n"
"v_mfma_f32_16x16x32_bf16 v[80:83], a[208:211], v[208:211], v[80:83]\n"
"ds_read_b64_tr_b16 a[166:167], v248 offset:53248\n"
"v_add_f32 v243, v243, v202\n"
"v_mfma_f32_16x16x32_bf16 v[84:87], a[212:215], v[208:211], v[84:87]\n"
"v_exp_f32 v203, v203\n"
"v_mfma_f32_16x16x32_bf16 v[88:91], a[216:219], v[208:211], v[88:91]\n"
"v_add_f32 v243, v243, v203\n"
"v_cvt_pk_bf16_f32 v213, v202, v203\n"
"v_mfma_f32_16x16x32_bf16 v[92:95], a[220:223], v[208:211], v[92:95]\n"
"v_exp_f32 v204, v204\n"
"v_mfma_f32_16x16x32_bf16 v[192:195], a[128:131], a[64:67], v[232:235]\n"
"ds_read_b64_tr_b16 a[168:169], v247 offset:49664\n"
"v_add_f32 v243, v243, v204\n"
"v_mfma_f32_16x16x32_bf16 v[192:195], a[132:135], a[68:71], v[192:195]\n"
"v_exp_f32 v205, v205\n"
"v_mfma_f32_16x16x32_bf16 v[192:195], a[136:139], a[72:75], v[192:195]\n"
"v_add_f32 v243, v243, v205\n"
"v_cvt_pk_bf16_f32 v214, v204, v205\n"
"v_mfma_f32_16x16x32_bf16 v[192:195], a[140:143], a[76:79], v[192:195]\n"
"v_exp_f32 v206, v206\n"
"v_mfma_f32_16x16x32_bf16 v[196:199], a[144:147], a[64:67], v[232:235]\n"
"ds_read_b64_tr_b16 a[170:171], v247 offset:53760\n"
"v_add_f32 v243, v243, v206\n"
"v_mfma_f32_16x16x32_bf16 v[196:199], a[148:151], a[68:71], v[196:199]\n"
"v_exp_f32 v207, v207\n"
"v_mfma_f32_16x16x32_bf16 v[196:199], a[152:155], a[72:75], v[196:199]\n"
"v_add_f32 v243, v243, v207\n"
"v_cvt_pk_bf16_f32 v215, v206, v207\n"
"v_mfma_f32_16x16x32_bf16 v[196:199], a[156:159], a[76:79], v[196:199]\n"
"v_exp_f32 v192, v192\n"
"v_mfma_f32_16x16x32_bf16 v[96:99], a[192:195], v[212:215], v[96:99]\n"
"ds_read_b64_tr_b16 a[172:173], v248 offset:49664\n"
"v_add_f32 v244, v244, v192\n"
"v_mfma_f32_16x16x32_bf16 v[100:103], a[196:199], v[212:215], v[100:103]\n"
"v_exp_f32 v193, v193\n"
"v_mfma_f32_16x16x32_bf16 v[104:107], a[200:203], v[212:215], v[104:107]\n"
"v_add_f32 v244, v244, v193\n"
"v_cvt_pk_bf16_f32 v208, v192, v193\n"
"v_mfma_f32_16x16x32_bf16 v[108:111], a[204:207], v[212:215], v[108:111]\n"
"v_exp_f32 v194, v194\n"
"v_mfma_f32_16x16x32_bf16 v[112:115], a[208:211], v[212:215], v[112:115]\n"
"ds_read_b64_tr_b16 a[174:175], v248 offset:53760\n"
"v_add_f32 v244, v244, v194\n"
"v_mfma_f32_16x16x32_bf16 v[116:119], a[212:215], v[212:215], v[116:119]\n"
"v_exp_f32 v195, v195\n"
"v_mfma_f32_16x16x32_bf16 v[120:123], a[216:219], v[212:215], v[120:123]\n"
"v_add_f32 v244, v244, v195\n"
"v_cvt_pk_bf16_f32 v209, v194, v195\n"
"v_mfma_f32_16x16x32_bf16 v[124:127], a[220:223], v[212:215], v[124:127]\n"
"v_exp_f32 v196, v196\n"
"v_mfma_f32_16x16x32_bf16 v[200:203], a[128:131], a[80:83], v[236:239]\n"
"ds_read_b64_tr_b16 a[176:177], v247 offset:50176\n"
"v_add_f32 v244, v244, v196\n"
"v_mfma_f32_16x16x32_bf16 v[200:203], a[132:135], a[84:87], v[200:203]\n"
"v_exp_f32 v197, v197\n"
"v_mfma_f32_16x16x32_bf16 v[200:203], a[136:139], a[88:91], v[200:203]\n"
"v_add_f32 v244, v244, v197\n"
"v_cvt_pk_bf16_f32 v210, v196, v197\n"
"v_mfma_f32_16x16x32_bf16 v[200:203], a[140:143], a[92:95], v[200:203]\n"
"v_exp_f32 v198, v198\n"
"v_mfma_f32_16x16x32_bf16 v[204:207], a[144:147], a[80:83], v[236:239]\n"
"ds_read_b64_tr_b16 a[178:179], v247 offset:54272\n"
"v_add_f32 v244, v244, v198\n"
"v_mfma_f32_16x16x32_bf16 v[204:207], a[148:151], a[84:87], v[204:207]\n"
"v_exp_f32 v199, v199\n"
"v_mfma_f32_16x16x32_bf16 v[204:207], a[152:155], a[88:91], v[204:207]\n"
"v_add_f32 v244, v244, v199\n"
"v_cvt_pk_bf16_f32 v211, v198, v199\n"
"v_mfma_f32_16x16x32_bf16 v[204:207], a[156:159], a[92:95], v[204:207]\n"
"v_exp_f32 v200, v200\n"
"v_mfma_f32_16x16x32_bf16 v[128:131], a[192:195], v[208:211], v[128:131]\n"
"ds_read_b64_tr_b16 a[180:181], v248 offset:50176\n"
"v_add_f32 v245, v245, v200\n"
"v_mfma_f32_16x16x32_bf16 v[132:135], a[196:199], v[208:211], v[132:135]\n"
"v_exp_f32 v201, v201\n"
"v_mfma_f32_16x16x32_bf16 v[136:139], a[200:203], v[208:211], v[136:139]\n"
"v_add_f32 v245, v245, v201\n"
"v_cvt_pk_bf16_f32 v212, v200, v201\n"
"v_mfma_f32_16x16x32_bf16 v[140:143], a[204:207], v[208:211], v[140:143]\n"
"v_exp_f32 v202, v202\n"
"v_mfma_f32_16x16x32_bf16 v[144:147], a[208:211], v[208:211], v[144:147]\n"
"ds_read_b64_tr_b16 a[182:183], v248 offset:54272\n"
"v_add_f32 v245, v245, v202\n"
"v_mfma_f32_16x16x32_bf16 v[148:151], a[212:215], v[208:211], v[148:151]\n"
"v_exp_f32 v203, v203\n"
"v_mfma_f32_16x16x32_bf16 v[152:155], a[216:219], v[208:211], v[152:155]\n"
"v_add_f32 v245, v245, v203\n"
"v_cvt_pk_bf16_f32 v213, v202, v203\n"
"v_mfma_f32_16x16x32_bf16 v[156:159], a[220:223], v[208:211], v[156:159]\n"
"s_waitcnt lgkmcnt(2)\n"
"v_exp_f32 v204, v204\n"
"v_mfma_f32_16x16x32_bf16 v[192:195], a[96:99], a[0:3], v[216:219]\n"
"ds_read_b64_tr_b16 a[184:185], v247 offset:50688\n"
"v_add_f32 v245, v245, v204\n"
"v_mfma_f32_16x16x32_bf16 v[192:195], a[100:103], a[4:7], v[192:195]\n"
"v_exp_f32 v205, v205\n"
"v_mfma_f32_16x16x32_bf16 v[192:195], a[104:107], a[8:11], v[192:195]\n"
"v_add_f32 v245, v245, v205\n"
"v_cvt_pk_bf16_f32 v214, v204, v205\n"
"v_mfma_f32_16x16x32_bf16 v[192:195], a[108:111], a[12:15], v[192:195]\n"
"v_exp_f32 v206, v206\n"
"v_mfma_f32_16x16x32_bf16 v[196:199], a[112:115], a[0:3], v[216:219]\n"
"ds_read_b64_tr_b16 a[186:187], v247 offset:54784\n"
"v_add_f32 v245, v245, v206\n"
"v_mfma_f32_16x16x32_bf16 v[196:199], a[116:119], a[4:7], v[196:199]\n"
"v_exp_f32 v207, v207\n"
"v_mfma_f32_16x16x32_bf16 v[196:199], a[120:123], a[8:11], v[196:199]\n"
"v_add_f32 v245, v245, v207\n"
"v_cvt_pk_bf16_f32 v215, v206, v207\n"
"v_mfma_f32_16x16x32_bf16 v[196:199], a[124:127], a[12:15], v[196:199]\n"
"v_exp_f32 v192, v192\n"
"v_mfma_f32_16x16x32_bf16 v[160:163], a[192:195], v[212:215], v[160:163]\n"
"ds_read_b64_tr_b16 a[188:189], v248 offset:50688\n"
"v_add_f32 v240, v240, v192\n"
"v_mfma_f32_16x16x32_bf16 v[164:167], a[196:199], v[212:215], v[164:167]\n"
"v_exp_f32 v193, v193\n"
"v_mfma_f32_16x16x32_bf16 v[168:171], a[200:203], v[212:215], v[168:171]\n"
"v_add_f32 v240, v240, v193\n"
"v_cvt_pk_bf16_f32 v208, v192, v193\n"
"v_mfma_f32_16x16x32_bf16 v[172:175], a[204:207], v[212:215], v[172:175]\n"
"v_exp_f32 v194, v194\n"
"v_mfma_f32_16x16x32_bf16 v[176:179], a[208:211], v[212:215], v[176:179]\n"
"s_cmp_eq_u32 s54, 5\n"
"ds_read_b64_tr_b16 a[190:191], v248 offset:54784\n"
"v_add_f32 v240, v240, v194\n"
"v_mfma_f32_16x16x32_bf16 v[180:183], a[212:215], v[212:215], v[180:183]\n"
"s_cselect_b64 vcc, -1, 0\n"
"v_exp_f32 v195, v195\n"
"v_mfma_f32_16x16x32_bf16 v[184:187], a[216:219], v[212:215], v[184:187]\n"
"s_cmp_lt_u32 s54, 5\n"
"v_add_f32 v240, v240, v195\n"
"v_cvt_pk_bf16_f32 v209, v194, v195\n"
"v_mfma_f32_16x16x32_bf16 v[188:191], a[220:223], v[212:215], v[188:191]\n"
"s_cbranch_scc1 LMA0%=\n"
"s_cbranch_vccnz LSEAMA%=\n"
"v_exp_f32 v196, v196\n"
"v_mfma_f32_16x16x32_bf16 v[200:203], a[96:99], a[16:19], v[220:223]\n"
"ds_read_b128 a[128:131], v246 offset:24576\n"
"v_add_f32 v240, v240, v196\n"
"v_mfma_f32_16x16x32_bf16 v[200:203], a[100:103], a[20:23], v[200:203]\n"
"v_exp_f32 v197, v197\n"
"v_mfma_f32_16x16x32_bf16 v[200:203], a[104:107], a[24:27], v[200:203]\n"
"s_cmp_lt_u32 s54, 10\n"
"buffer_store_dwordx4 a[224:227], v255, s[48:51], s68 offen sc1\n"
"v_add_f32 v240, v240, v197\n"
"v_cvt_pk_bf16_f32 v210, v196, v197\n"
"v_mfma_f32_16x16x32_bf16 v[200:203], a[108:111], a[28:31], v[200:203]\n"
"s_cselect_b32 s60, 4352, 0\n"
"v_exp_f32 v198, v198\n"
"v_mfma_f32_16x16x32_bf16 v[204:207], a[112:115], a[16:19], v[220:223]\n"
"ds_read_b128 a[132:135], v246 offset:25088\n"
"v_add_f32 v240, v240, v198\n"
"v_add_u32 v253, s60, v253\n"
"v_mfma_f32_16x16x32_bf16 v[204:207], a[116:119], a[20:23], v[204:207]\n"
"s_waitcnt lgkmcnt(2)\n"
"v_exp_f32 v199, v199\n"
"v_mfma_f32_16x16x32_bf16 v[204:207], a[120:123], a[24:27], v[204:207]\n"
"v_add_f32 v240, v240, v199\n"
"v_cvt_pk_bf16_f32 v211, v198, v199\n"
"v_mfma_f32_16x16x32_bf16 v[204:207], a[124:127], a[28:31], v[204:207]\n"
"s_barrier\n"
"v_exp_f32 v200, v200\n"
"v_mfma_f32_16x16x32_bf16 v[0:3], a[160:163], v[208:211], v[0:3]\n"
"ds_read_b128 a[136:139], v246 offset:25600\n"
"v_add_f32 v241, v241, v200\n"
"v_add_u32 v249, 2176, v253\n"
"v_mfma_f32_16x16x32_bf16 v[4:7], a[164:167], v[208:211], v[4:7]\n"
"v_exp_f32 v201, v201\n"
"v_mfma_f32_16x16x32_bf16 v[8:11], a[168:171], v[208:211], v[8:11]\n"
"v_add_f32 v241, v241, v201\n"
"v_cvt_pk_bf16_f32 v212, v200, v201\n"
"v_mfma_f32_16x16x32_bf16 v[12:15], a[172:175], v[208:211], v[12:15]\n"
"v_exp_f32 v202, v202\n"
"v_mfma_f32_16x16x32_bf16 v[16:19], a[176:179], v[208:211], v[16:19]\n"
"ds_read_b128 a[140:143], v246 offset:26112\n"
"v_add_f32 v241, v241, v202\n"
"v_mfma_f32_16x16x32_bf16 v[20:23], a[180:183], v[208:211], v[20:23]\n"
"v_exp_f32 v203, v203\n"
"v_mfma_f32_16x16x32_bf16 v[24:27], a[184:187], v[208:211], v[24:27]\n"
"ds_read2_b64 a[224:227], v253 offset0:0 offset1:2\n"
"v_add_f32 v241, v241, v203\n"
"v_cvt_pk_bf16_f32 v213, v202, v203\n"
"v_mfma_f32_16x16x32_bf16 v[28:31], a[188:191], v[208:211], v[28:31]\n"
"v_exp_f32 v204, v204\n"
"v_mfma_f32_16x16x32_bf16 v[192:195], a[96:99], a[32:35], v[224:227]\n"
"ds_read_b128 a[144:147], v246 offset:28672\n"
"v_add_f32 v241, v241, v204\n"
"v_mfma_f32_16x16x32_bf16 v[192:195], a[100:103], a[36:39], v[192:195]\n"
"v_exp_f32 v205, v205\n"
"v_mfma_f32_16x16x32_bf16 v[192:195], a[104:107], a[40:43], v[192:195]\n"
"v_add_f32 v241, v241, v205\n"
"v_cvt_pk_bf16_f32 v214, v204, v205\n"
"v_mfma_f32_16x16x32_bf16 v[192:195], a[108:111], a[44:47], v[192:195]\n"
"v_exp_f32 v206, v206\n"
"v_mfma_f32_16x16x32_bf16 v[196:199], a[112:115], a[32:35], v[224:227]\n"
"ds_read_b128 a[148:151], v246 offset:29184\n"
"v_add_f32 v241, v241, v206\n"
"v_mfma_f32_16x16x32_bf16 v[196:199], a[116:119], a[36:39], v[196:199]\n"
"v_exp_f32 v207, v207\n"
"v_mfma_f32_16x16x32_bf16 v[196:199], a[120:123], a[40:43], v[196:199]\n"
"v_add_f32 v241, v241, v207\n"
"v_cvt_pk_bf16_f32 v215, v206, v207\n"
"v_mfma_f32_16x16x32_bf16 v[196:199], a[124:127], a[44:47], v[196:199]\n"
"v_exp_f32 v192, v192\n"
"v_mfma_f32_16x16x32_bf16 v[32:35], a[160:163], v[212:215], v[32:35]\n"
"ds_read_b128 a[152:155], v246 offset:29696\n"
"v_add_f32 v242, v242, v192\n"
"v_mfma_f32_16x16x32_bf16 v[36:39], a[164:167], v[212:215], v[36:39]\n"
"v_exp_f32 v193, v193\n"
"v_mfma_f32_16x16x32_bf16 v[40:43], a[168:171], v[212:215], v[40:43]\n"
"buffer_store_dwordx4 a[228:231], v255, s[48:51], s68 offen offset:1024 sc1\n"
"v_add_f32 v242, v242, v193\n"
"v_cvt_pk_bf16_f32 v208, v192, v193\n"
"v_mfma_f32_16x16x32_bf16 v[44:47], a[172:175], v[212:215], v[44:47]\n"
"v_exp_f32 v194, v194\n"
"v_mfma_f32_16x16x32_bf16 v[48:51], a[176:179], v[212:215], v[48:51]\n"
"ds_read_b128 a[156:159], v246 offset:30208\n"
"v_add_f32 v242, v242, v194\n"
"v_mfma_f32_16x16x32_bf16 v[52:55], a[180:183], v[212:215], v[52:55]\n"
"v_exp_f32 v195, v195\n"
"v_mfma_f32_16x16x32_bf16 v[56:59], a[184:187], v[212:215], v[56:59]\n"
"v_add_f32 v242, v242, v195\n"
"v_cvt_pk_bf16_f32 v209, v194, v195\n"
"v_mfma_f32_16x16x32_bf16 v[60:63], a[188:191], v[212:215], v[60:63]\n"
"v_exp_f32 v196, v196\n"
"v_mfma_f32_16x16x32_bf16 v[200:203], a[96:99], a[48:51], v[228:231]\n"
"ds_read_b64_tr_b16 a[192:193], v247 offset:57344\n"
"v_add_f32 v242, v242, v196\n"
"v_mfma_f32_16x16x32_bf16 v[200:203], a[100:103], a[52:55], v[200:203]\n"
"v_exp_f32 v197, v197\n"
"v_mfma_f32_16x16x32_bf16 v[200:203], a[104:107], a[56:59], v[200:203]\n"
"v_add_f32 v242, v242, v197\n"
"v_cvt_pk_bf16_f32 v210, v196, v197\n"
"v_mfma_f32_16x16x32_bf16 v[200:203], a[108:111], a[60:63], v[200:203]\n"
"v_exp_f32 v198, v198\n"
"v_mfma_f32_16x16x32_bf16 v[204:207], a[112:115], a[48:51], v[228:231]\n"
"ds_read_b64_tr_b16 a[194:195], v247 offset:61440\n"
"v_add_f32 v242, v242, v198\n"
"v_mfma_f32_16x16x32_bf16 v[204:207], a[116:119], a[52:55], v[204:207]\n"
"v_exp_f32 v199, v199\n"
"v_mfma_f32_16x16x32_bf16 v[204:207], a[120:123], a[56:59], v[204:207]\n"
"ds_read2_b64 a[228:231], v253 offset0:136 offset1:138\n"
"v_add_f32 v242, v242, v199\n"
"v_cvt_pk_bf16_f32 v211, v198, v199\n"
"v_mfma_f32_16x16x32_bf16 v[204:207], a[124:127], a[60:63], v[204:207]\n"
"v_exp_f32 v200, v200\n"
"v_mfma_f32_16x16x32_bf16 v[64:67], a[160:163], v[208:211], v[64:67]\n"
"ds_read_b64_tr_b16 a[196:197], v248 offset:57344\n"
"v_add_f32 v243, v243, v200\n"
"v_mfma_f32_16x16x32_bf16 v[68:71], a[164:167], v[208:211], v[68:71]\n"
"v_exp_f32 v201, v201\n"
"v_mfma_f32_16x16x32_bf16 v[72:75], a[168:171], v[208:211], v[72:75]\n"
"v_add_f32 v243, v243, v201\n"
"v_cvt_pk_bf16_f32 v212, v200, v201\n"
"v_mfma_f32_16x16x32_bf16 v[76:79], a[172:175], v[208:211], v[76:79]\n"
"v_exp_f32 v202, v202\n"
"v_mfma_f32_16x16x32_bf16 v[80:83], a[176:179], v[208:211], v[80:83]\n"
"ds_read_b64_tr_b16 a[198:199], v248 offset:61440\n"
"v_add_f32 v243, v243, v202\n"
"v_mfma_f32_16x16x32_bf16 v[84:87], a[180:183], v[208:211], v[84:87]\n"
"v_exp_f32 v203, v203\n"
"v_mfma_f32_16x16x32_bf16 v[88:91], a[184:187], v[208:211], v[88:91]\n"
"v_add_f32 v243, v243, v203\n"
"v_cvt_pk_bf16_f32 v213, v202, v203\n"
"v_mfma_f32_16x16x32_bf16 v[92:95], a[188:191], v[208:211], v[92:95]\n"
"v_exp_f32 v204, v204\n"
"v_mfma_f32_16x16x32_bf16 v[192:195], a[96:99], a[64:67], v[232:235]\n"
"ds_read_b64_tr_b16 a[200:201], v247 offset:57856\n"
"v_add_f32 v243, v243, v204\n"
"v_mfma_f32_16x16x32_bf16 v[192:195], a[100:103], a[68:71], v[192:195]\n"
"v_exp_f32 v205, v205\n"
"v_mfma_f32_16x16x32_bf16 v[192:195], a[104:107], a[72:75], v[192:195]\n"
"buffer_store_dwordx4 a[232:235], v255, s[48:51], s68 offen offset:2048 sc1\n"
"v_add_f32 v243, v243, v205\n"
"v_cvt_pk_bf16_f32 v214, v204, v205\n"
"v_mfma_f32_16x16x32_bf16 v[192:195], a[108:111], a[76:79], v[192:195]\n"
"v_exp_f32 v206, v206\n"
"v_mfma_f32_16x16x32_bf16 v[196:199], a[112:115], a[64:67], v[232:235]\n"
"ds_read_b64_tr_b16 a[202:203], v247 offset:61952\n"
"v_add_f32 v243, v243, v206\n"
"v_mfma_f32_16x16x32_bf16 v[196:199], a[116:119], a[68:71], v[196:199]\n"
"v_exp_f32 v207, v207\n"
"v_mfma_f32_16x16x32_bf16 v[196:199], a[120:123], a[72:75], v[196:199]\n"
"v_add_f32 v243, v243, v207\n"
"v_cvt_pk_bf16_f32 v215, v206, v207\n"
"v_mfma_f32_16x16x32_bf16 v[196:199], a[124:127], a[76:79], v[196:199]\n"
"v_exp_f32 v192, v192\n"
"v_mfma_f32_16x16x32_bf16 v[96:99], a[160:163], v[212:215], v[96:99]\n"
"ds_read_b64_tr_b16 a[204:205], v248 offset:57856\n"
"v_add_f32 v244, v244, v192\n"
"v_mfma_f32_16x16x32_bf16 v[100:103], a[164:167], v[212:215], v[100:103]\n"
"v_exp_f32 v193, v193\n"
"v_mfma_f32_16x16x32_bf16 v[104:107], a[168:171], v[212:215], v[104:107]\n"
"v_add_f32 v244, v244, v193\n"
"v_cvt_pk_bf16_f32 v208, v192, v193\n"
"v_mfma_f32_16x16x32_bf16 v[108:111], a[172:175], v[212:215], v[108:111]\n"
"v_exp_f32 v194, v194\n"
"v_mfma_f32_16x16x32_bf16 v[112:115], a[176:179], v[212:215], v[112:115]\n"
"ds_read_b64_tr_b16 a[206:207], v248 offset:61952\n"
"v_add_f32 v244, v244, v194\n"
"v_mfma_f32_16x16x32_bf16 v[116:119], a[180:183], v[212:215], v[116:119]\n"
"v_exp_f32 v195, v195\n"
"v_mfma_f32_16x16x32_bf16 v[120:123], a[184:187], v[212:215], v[120:123]\n"
"ds_read2_b64 a[232:235], v249 offset0:0 offset1:2\n"
"v_add_f32 v244, v244, v195\n"
"v_cvt_pk_bf16_f32 v209, v194, v195\n"
"v_mfma_f32_16x16x32_bf16 v[124:127], a[188:191], v[212:215], v[124:127]\n"
"v_exp_f32 v196, v196\n"
"v_mfma_f32_16x16x32_bf16 v[200:203], a[96:99], a[80:83], v[236:239]\n"
"ds_read_b64_tr_b16 a[208:209], v247 offset:58368\n"
"v_add_f32 v244, v244, v196\n"
"v_mfma_f32_16x16x32_bf16 v[200:203], a[100:103], a[84:87], v[200:203]\n"
"v_exp_f32 v197, v197\n"
"v_mfma_f32_16x16x32_bf16 v[200:203], a[104:107], a[88:91], v[200:203]\n"
"v_add_f32 v244, v244, v197\n"
"v_cvt_pk_bf16_f32 v210, v196, v197\n"
"v_mfma_f32_16x16x32_bf16 v[200:203], a[108:111], a[92:95], v[200:203]\n"
"v_exp_f32 v198, v198\n"
"v_mfma_f32_16x16x32_bf16 v[204:207], a[112:115], a[80:83], v[236:239]\n"
"ds_read_b64_tr_b16 a[210:211], v247 offset:62464\n"
"v_add_f32 v244, v244, v198\n"
"v_mfma_f32_16x16x32_bf16 v[204:207], a[116:119], a[84:87], v[204:207]\n"
"v_exp_f32 v199, v199\n"
"v_mfma_f32_16x16x32_bf16 v[204:207], a[120:123], a[88:91], v[204:207]\n"
"v_add_f32 v244, v244, v199\n"
"v_cvt_pk_bf16_f32 v211, v198, v199\n"
"v_mfma_f32_16x16x32_bf16 v[204:207], a[124:127], a[92:95], v[204:207]\n"
"v_exp_f32 v200, v200\n"
"v_mfma_f32_16x16x32_bf16 v[128:131], a[160:163], v[208:211], v[128:131]\n"
"ds_read_b64_tr_b16 a[212:213], v248 offset:58368\n"
"v_add_f32 v245, v245, v200\n"
"v_mfma_f32_16x16x32_bf16 v[132:135], a[164:167], v[208:211], v[132:135]\n"
"v_exp_f32 v201, v201\n"
"v_mfma_f32_16x16x32_bf16 v[136:139], a[168:171], v[208:211], v[136:139]\n"
"buffer_store_dwordx4 a[236:239], v255, s[48:51], s68 offen offset:3072 sc1\n"
"v_add_f32 v245, v245, v201\n"
"v_cvt_pk_bf16_f32 v212, v200, v201\n"
"v_mfma_f32_16x16x32_bf16 v[140:143], a[172:175], v[208:211], v[140:143]\n"
"Lhop1%=:\n"
"s_cbranch_execz Lhop5%=\n"
"v_exp_f32 v202, v202\n"
"v_mfma_f32_16x16x32_bf16 v[144:147], a[176:179], v[208:211], v[144:147]\n"
"ds_read_b64_tr_b16 a[214:215], v248 offset:62464\n"
"v_add_f32 v245, v245, v202\n"
"v_mfma_f32_16x16x32_bf16 v[148:151], a[180:183], v[208:211], v[148:151]\n"
"v_exp_f32 v203, v203\n"
"v_mfma_f32_16x16x32_bf16 v[152:155], a[184:187], v[208:211], v[152:155]\n"
"v_add_f32 v245, v245, v203\n"
"v_cvt_pk_bf16_f32 v213, v202, v203\n"
"v_mfma_f32_16x16x32_bf16 v[156:159], a[188:191], v[208:211], v[156:159]\n"
"s_waitcnt lgkmcnt(2)\n"
"v_exp_f32 v204, v204\n"
"v_mfma_f32_16x16x32_bf16 v[192:195], a[128:131], a[0:3], v[216:219]\n"
"ds_read_b64_tr_b16 a[216:217], v247 offset:58880\n"
"v_add_f32 v245, v245, v204\n"
"v_mfma_f32_16x16x32_bf16 v[192:195], a[132:135], a[4:7], v[192:195]\n"
"v_exp_f32 v205, v205\n"
"v_mfma_f32_16x16x32_bf16 v[192:195], a[136:139], a[8:11], v[192:195]\n"
"v_add_f32 v245, v245, v205\n"
"v_cvt_pk_bf16_f32 v214, v204, v205\n"
"v_mfma_f32_16x16x32_bf16 v[192:195], a[140:143], a[12:15], v[192:195]\n"
"v_exp_f32 v206, v206\n"
"v_mfma_f32_16x16x32_bf16 v[196:199], a[144:147], a[0:3], v[216:219]\n"
"ds_read_b64_tr_b16 a[218:219], v247 offset:62976\n"
"v_add_f32 v245, v245, v206\n"
"v_mfma_f32_16x16x32_bf16 v[196:199], a[148:151], a[4:7], v[196:199]\n"
"v_exp_f32 v207, v207\n"
"v_mfma_f32_16x16x32_bf16 v[196:199], a[152:155], a[8:11], v[196:199]\n"
"ds_read2_b64 a[236:239], v249 offset0:136 offset1:138\n"
"v_add_f32 v245, v245, v207\n"
"v_cvt_pk_bf16_f32 v215, v206, v207\n"
"v_mfma_f32_16x16x32_bf16 v[196:199], a[156:159], a[12:15], v[196:199]\n"
"v_exp_f32 v192, v192\n"
"v_mfma_f32_16x16x32_bf16 v[160:163], a[160:163], v[212:215], v[160:163]\n"
"ds_read_b64_tr_b16 a[220:221], v248 offset:58880\n"
"v_add_f32 v240, v240, v192\n"
"v_mfma_f32_16x16x32_bf16 v[164:167], a[164:167], v[212:215], v[164:167]\n"
"v_exp_f32 v193, v193\n"
"v_mfma_f32_16x16x32_bf16 v[168:171], a[168:171], v[212:215], v[168:171]\n"
"v_add_f32 v240, v240, v193\n"
"v_cvt_pk_bf16_f32 v208, v192, v193\n"
"v_mfma_f32_16x16x32_bf16 v[172:175], a[172:175], v[212:215], v[172:175]\n"
"s_add_u32 s68, s68, 4096\n"
"v_exp_f32 v194, v194\n"
"v_mfma_f32_16x16x32_bf16 v[176:179], a[176:179], v[212:215], v[176:179]\n"
"ds_read_b64_tr_b16 a[222:223], v248 offset:62976\n"
"v_add_f32 v240, v240, v194\n"
"v_mfma_f32_16x16x32_bf16 v[180:183], a[180:183], v[212:215], v[180:183]\n"
"v_exp_f32 v195, v195\n"
"v_mfma_f32_16x16x32_bf16 v[184:187], a[184:187], v[212:215], v[184:187]\n"
"s_cmp_lt_u32 s54, 10\n"
"v_add_f32 v240, v240, v195\n"
"v_cvt_pk_bf16_f32 v209, v194, v195\n"
"v_mfma_f32_16x16x32_bf16 v[188:191], a[188:191], v[212:215], v[188:191]\n"
"s_cbranch_scc1 LMA1%=\n"
"v_exp_f32 v196, v196\n"
"v_mfma_f32_16x16x32_bf16 v[200:203], a[128:131], a[16:19], v[220:223]\n"
"ds_read_b128 a[96:99], v246 offset:0\n"
"v_add_f32 v240, v240, v196\n"
"v_mfma_f32_16x16x32_bf16 v[200:203], a[132:135], a[20:23], v[200:203]\n"
"v_exp_f32 v197, v197\n"
"v_mfma_f32_16x16x32_bf16 v[200:203], a[136:139], a[24:27], v[200:203]\n"
"v_add_f32 v240, v240, v197\n"
"v_cvt_pk_bf16_f32 v210, v196, v197\n"
"v_mfma_f32_16x16x32_bf16 v[200:203], a[140:143], a[28:31], v[200:203]\n"
"v_exp_f32 v198, v198\n"
"v_mfma_f32_16x16x32_bf16 v[204:207], a[144:147], a[16:19], v[220:223]\n"
"ds_read_b128 a[100:103], v246 offset:512\n"
"v_add_f32 v240, v240, v198\n"
"v_mfma_f32_16x16x32_bf16 v[204:207], a[148:151], a[20:23], v[204:207]\n"
"s_waitcnt lgkmcnt(2)\n"
"v_exp_f32 v199, v199\n"
"v_mfma_f32_16x16x32_bf16 v[204:207], a[152:155], a[24:27], v[204:207]\n"
"v_add_f32 v240, v240, v199\n"
"v_cvt_pk_bf16_f32 v211, v198, v199\n"
"v_mfma_f32_16x16x32_bf16 v[204:207], a[156:159], a[28:31], v[204:207]\n"
"s_barrier\n"
"v_exp_f32 v200, v200\n"
"v_mfma_f32_16x16x32_bf16 v[0:3], a[192:195], v[208:211], v[0:3]\n"
"ds_read_b128 a[104:107], v246 offset:1024\n"
"v_add_f32 v241, v241, v200\n"
"v_mfma_f32_16x16x32_bf16 v[4:7], a[196:199], v[208:211], v[4:7]\n"
"v_exp_f32 v201, v201\n"
"v_mfma_f32_16x16x32_bf16 v[8:11], a[200:203], v[208:211], v[8:11]\n"
"v_add_f32 v241, v241, v201\n"
"v_cvt_pk_bf16_f32 v212, v200, v201\n"
"v_mfma_f32_16x16x32_bf16 v[12:15], a[204:207], v[208:211], v[12:15]\n"
"v_exp_f32 v202, v202\n"
"v_mfma_f32_16x16x32_bf16 v[16:19], a[208:211], v[208:211], v[16:19]\n"
"ds_read_b128 a[108:111], v246 offset:1536\n"
"v_add_f32 v241, v241, v202\n"
"v_mfma_f32_16x16x32_bf16 v[20:23], a[212:215], v[208:211], v[20:23]\n"
"v_exp_f32 v203, v203\n"
"v_mfma_f32_16x16x32_bf16 v[24:27], a[216:219], v[208:211], v[24:27]\n"
"v_add_f32 v241, v241, v203\n"
"v_cvt_pk_bf16_f32 v213, v202, v203\n"
"v_mfma_f32_16x16x32_bf16 v[28:31], a[220:223], v[208:211], v[28:31]\n"
"v_exp_f32 v204, v204\n"
"v_mfma_f32_16x16x32_bf16 v[192:195], a[128:131], a[32:35], v[224:227]\n"
"ds_read_b128 a[112:115], v246 offset:4096\n"
"v_add_f32 v241, v241, v204\n"
"v_mfma_f32_16x16x32_bf16 v[192:195], a[132:135], a[36:39], v[192:195]\n"
"v_exp_f32 v205, v205\n"
"v_mfma_f32_16x16x32_bf16 v[192:195], a[136:139], a[40:43], v[192:195]\n"
"v_add_f32 v241, v241, v205\n"
"v_cvt_pk_bf16_f32 v214, v204, v205\n"
"v_mfma_f32_16x16x32_bf16 v[192:195], a[140:143], a[44:47], v[192:195]\n"
"v_exp_f32 v206, v206\n"
"v_mfma_f32_16x16x32_bf16 v[196:199], a[144:147], a[32:35], v[224:227]\n"
"ds_read_b128 a[116:119], v246 offset:4608\n"
"v_add_f32 v241, v241, v206\n"
"v_mfma_f32_16x16x32_bf16 v[196:199], a[148:151], a[36:39], v[196:199]\n"
"v_exp_f32 v207, v207\n"
"v_mfma_f32_16x16x32_bf16 v[196:199], a[152:155], a[40:43], v[196:199]\n"
"v_add_f32 v241, v241, v207\n"
"v_cvt_pk_bf16_f32 v215, v206, v207\n"
"v_mfma_f32_16x16x32_bf16 v[196:199], a[156:159], a[44:47], v[196:199]\n"
"v_exp_f32 v192, v192\n"
"v_mfma_f32_16x16x32_bf16 v[32:35], a[192:195], v[212:215], v[32:35]\n"
"ds_read_b128 a[120:123], v246 offset:5120\n"
"v_add_f32 v242, v242, v192\n"
"v_mfma_f32_16x16x32_bf16 v[36:39], a[196:199], v[212:215], v[36:39]\n"
"v_exp_f32 v193, v193\n"
"v_mfma_f32_16x16x32_bf16 v[40:43], a[200:203], v[212:215], v[40:43]\n"
"v_add_f32 v242, v242, v193\n"
"v_cvt_pk_bf16_f32 v208, v192, v193\n"
"v_mfma_f32_16x16x32_bf16 v[44:47], a[204:207], v[212:215], v[44:47]\n"
"v_exp_f32 v194, v194\n"
"v_mfma_f32_16x16x32_bf16 v[48:51], a[208:211], v[212:215], v[48:51]\n"
"ds_read_b128 a[124:127], v246 offset:5632\n"
"v_add_f32 v242, v242, v194\n"
"v_mfma_f32_16x16x32_bf16 v[52:55], a[212:215], v[212:215], v[52:55]\n"
"v_exp_f32 v195, v195\n"
"v_mfma_f32_16x16x32_bf16 v[56:59], a[216:219], v[212:215], v[56:59]\n"
"v_add_f32 v242, v242, v195\n"
"v_cvt_pk_bf16_f32 v209, v194, v195\n"
"v_mfma_f32_16x16x32_bf16 v[60:63], a[220:223], v[212:215], v[60:63]\n"
"v_exp_f32 v196, v196\n"
"v_mfma_f32_16x16x32_bf16 v[200:203], a[128:131], a[48:51], v[228:231]\n"
"ds_read_b64_tr_b16 a[160:161], v247 offset:32768\n"
"v_add_f32 v242, v242, v196\n"
"v_mfma_f32_16x16x32_bf16 v[200:203], a[132:135], a[52:55], v[200:203]\n"
"v_exp_f32 v197, v197\n"
"v_mfma_f32_16x16x32_bf16 v[200:203], a[136:139], a[56:59], v[200:203]\n"
"v_add_f32 v242, v242, v197\n"
"v_cvt_pk_bf16_f32 v210, v196, v197\n"
"v_mfma_f32_16x16x32_bf16 v[200:203], a[140:143], a[60:63], v[200:203]\n"
"v_exp_f32 v198, v198\n"
"v_mfma_f32_16x16x32_bf16 v[204:207], a[144:147], a[48:51], v[228:231]\n"
"ds_read_b64_tr_b16 a[162:163], v247 offset:36864\n"
"v_add_f32 v242, v242, v198\n"
"v_mfma_f32_16x16x32_bf16 v[204:207], a[148:151], a[52:55], v[204:207]\n"
"v_exp_f32 v199, v199\n"
"v_mfma_f32_16x16x32_bf16 v[204:207], a[152:155], a[56:59], v[204:207]\n"
"v_add_f32 v242, v242, v199\n"
"v_cvt_pk_bf16_f32 v211, v198, v199\n"
"v_mfma_f32_16x16x32_bf16 v[204:207], a[156:159], a[60:63], v[204:207]\n"
"v_exp_f32 v200, v200\n"
"v_mfma_f32_16x16x32_bf16 v[64:67], a[192:195], v[208:211], v[64:67]\n"
"ds_read_b64_tr_b16 a[164:165], v248 offset:32768\n"
"v_add_f32 v243, v243, v200\n"
"v_mfma_f32_16x16x32_bf16 v[68:71], a[196:199], v[208:211], v[68:71]\n"
"v_exp_f32 v201, v201\n"
"v_mfma_f32_16x16x32_bf16 v[72:75], a[200:203], v[208:211], v[72:75]\n"
"v_add_f32 v243, v243, v201\n"
"v_cvt_pk_bf16_f32 v212, v200, v201\n"
"v_mfma_f32_16x16x32_bf16 v[76:79], a[204:207], v[208:211], v[76:79]\n"
"v_exp_f32 v202, v202\n"
"v_mfma_f32_16x16x32_bf16 v[80:83], a[208:211], v[208:211], v[80:83]\n"
"ds_read_b64_tr_b16 a[166:167], v248 offset:36864\n"
"v_add_f32 v243, v243, v202\n"
"v_mfma_f32_16x16x32_bf16 v[84:87], a[212:215], v[208:211], v[84:87]\n"
"v_exp_f32 v203, v203\n"
"v_mfma_f32_16x16x32_bf16 v[88:91], a[216:219], v[208:211], v[88:91]\n"
"v_add_f32 v243, v243, v203\n"
"v_cvt_pk_bf16_f32 v213, v202, v203\n"
"v_mfma_f32_16x16x32_bf16 v[92:95], a[220:223], v[208:211], v[92:95]\n"
"v_exp_f32 v204, v204\n"
"v_mfma_f32_16x16x32_bf16 v[192:195], a[128:131], a[64:67], v[232:235]\n"
"ds_read_b64_tr_b16 a[168:169], v247 offset:33280\n"
"v_add_f32 v243, v243, v204\n"
"v_mfma_f32_16x16x32_bf16 v[192:195], a[132:135], a[68:71], v[192:195]\n"
"v_exp_f32 v205, v205\n"
"v_mfma_f32_16x16x32_bf16 v[192:195], a[136:139], a[72:75], v[192:195]\n"
"v_add_f32 v243, v243, v205\n"
"v_cvt_pk_bf16_f32 v214, v204, v205\n"
"v_mfma_f32_16x16x32_bf16 v[192:195], a[140:143], a[76:79], v[192:195]\n"
"v_exp_f32 v206, v206\n"
"v_mfma_f32_16x16x32_bf16 v[196:199], a[144:147], a[64:67], v[232:235]\n"
"ds_read_b64_tr_b16 a[170:171], v247 offset:37376\n"
"v_add_f32 v243, v243, v206\n"
"v_mfma_f32_16x16x32_bf16 v[196:199], a[148:151], a[68:71], v[196:199]\n"
"v_exp_f32 v207, v207\n"
"v_mfma_f32_16x16x32_bf16 v[196:199], a[152:155], a[72:75], v[196:199]\n"
"v_add_f32 v243, v243, v207\n"
"v_cvt_pk_bf16_f32 v215, v206, v207\n"
"v_mfma_f32_16x16x32_bf16 v[196:199], a[156:159], a[76:79], v[196:199]\n"
"v_exp_f32 v192, v192\n"
"v_mfma_f32_16x16x32_bf16 v[96:99], a[192:195], v[212:215], v[96:99]\n"
"ds_read_b64_tr_b16 a[172:173], v248 offset:33280\n"
"v_add_f32 v244, v244, v192\n"
"v_mfma_f32_16x16x32_bf16 v[100:103], a[196:199], v[212:215], v[100:103]\n"
"v_exp_f32 v193, v193\n"
"v_mfma_f32_16x16x32_bf16 v[104:107], a[200:203], v[212:215], v[104:107]\n"
"v_add_f32 v244, v244, v193\n"
"v_cvt_pk_bf16_f32 v208, v192, v193\n"
"v_mfma_f32_16x16x32_bf16 v[108:111], a[204:207], v[212:215], v[108:111]\n"
"v_exp_f32 v194, v194\n"
"v_mfma_f32_16x16x32_bf16 v[112:115], a[208:211], v[212:215], v[112:115]\n"
"ds_read_b64_tr_b16 a[174:175], v248 offset:37376\n"
"v_add_f32 v244, v244, v194\n"
"v_mfma_f32_16x16x32_bf16 v[116:119], a[212:215], v[212:215], v[116:119]\n"
"v_exp_f32 v195, v195\n"
"v_mfma_f32_16x16x32_bf16 v[120:123], a[216:219], v[212:215], v[120:123]\n"
"v_add_f32 v244, v244, v195\n"
"v_cvt_pk_bf16_f32 v209, v194, v195\n"
"v_mfma_f32_16x16x32_bf16 v[124:127], a[220:223], v[212:215], v[124:127]\n"
"v_exp_f32 v196, v196\n"
"v_mfma_f32_16x16x32_bf16 v[200:203], a[128:131], a[80:83], v[236:239]\n"
"ds_read_b64_tr_b16 a[176:177], v247 offset:33792\n"
"v_add_f32 v244, v244, v196\n"
"v_mfma_f32_16x16x32_bf16 v[200:203], a[132:135], a[84:87], v[200:203]\n"
"v_exp_f32 v197, v197\n"
"v_mfma_f32_16x16x32_bf16 v[200:203], a[136:139], a[88:91], v[200:203]\n"
"ds_read_b128 a[128:131], v246 offset:8192\n"
"v_add_f32 v244, v244, v197\n"
"v_cvt_pk_bf16_f32 v210, v196, v197\n"
"v_mfma_f32_16x16x32_bf16 v[200:203], a[140:143], a[92:95], v[200:203]\n"
"v_exp_f32 v198, v198\n"
"v_mfma_f32_16x16x32_bf16 v[204:207], a[144:147], a[80:83], v[236:239]\n"
"ds_read_b64_tr_b16 a[178:179], v247 offset:37888\n"
"v_add_f32 v244, v244, v198\n"
"v_mfma_f32_16x16x32_bf16 v[204:207], a[148:151], a[84:87], v[204:207]\n"
"v_exp_f32 v199, v199\n"
"v_mfma_f32_16x16x32_bf16 v[204:207], a[152:155], a[88:91], v[204:207]\n"
"ds_read_b128 a[132:135], v246 offset:8704\n"
"v_add_f32 v244, v244, v199\n"
"v_cvt_pk_bf16_f32 v211, v198, v199\n"
"v_mfma_f32_16x16x32_bf16 v[204:207], a[156:159], a[92:95], v[204:207]\n"
"v_exp_f32 v200, v200\n"
"v_mfma_f32_16x16x32_bf16 v[128:131], a[192:195], v[208:211], v[128:131]\n"
"ds_read_b64_tr_b16 a[180:181], v248 offset:33792\n"
"v_add_f32 v245, v245, v200\n"
"v_mfma_f32_16x16x32_bf16 v[132:135], a[196:199], v[208:211], v[132:135]\n"
"v_exp_f32 v201, v201\n"
"v_mfma_f32_16x16x32_bf16 v[136:139], a[200:203], v[208:211], v[136:139]\n"
"ds_read_b128 a[136:139], v246 offset:9216\n"
"v_add_f32 v245, v245, v201\n"
"v_cvt_pk_bf16_f32 v212, v200, v201\n"
"v_mfma_f32_16x16x32_bf16 v[140:143], a[204:207], v[208:211], v[140:143]\n"
"v_exp_f32 v202, v202\n"
"v_mfma_f32_16x16x32_bf16 v[144:147], a[208:211], v[208:211], v[144:147]\n"
"ds_read_b64_tr_b16 a[182:183], v248 offset:37888\n"
"v_add_f32 v245, v245, v202\n"
"v_mfma_f32_16x16x32_bf16 v[148:151], a[212:215], v[208:211], v[148:151]\n"
"v_exp_f32 v203, v203\n"
"v_mfma_f32_16x16x32_bf16 v[152:155], a[216:219], v[208:211], v[152:155]\n"
"ds_read_b128 a[140:143], v246 offset:9728\n"
"v_add_f32 v245, v245, v203\n"
"v_cvt_pk_bf16_f32 v213, v202, v203\n"
"v_mfma_f32_16x16x32_bf16 v[156:159], a[220:223], v[208:211], v[156:159]\n"
"s_waitcnt lgkmcnt(5)\n"
"v_exp_f32 v204, v204\n"
"v_mfma_f32_16x16x32_bf16 v[192:195], a[96:99], a[0:3], v[216:219]\n"
"ds_read_b64_tr_b16 a[184:185], v247 offset:34304\n"
"v_add_f32 v245, v245, v204\n"
"v_mfma_f32_16x16x32_bf16 v[192:195], a[100:103], a[4:7], v[192:195]\n"
"v_exp_f32 v205, v205\n"
"v_mfma_f32_16x16x32_bf16 v[192:195], a[104:107], a[8:11], v[192:195]\n"
"ds_read_b128 a[144:147], v246 offset:12288\n"
"v_add_f32 v245, v245, v205\n"
"v_cvt_pk_bf16_f32 v214, v204, v205\n"
"v_mfma_f32_16x16x32_bf16 v[192:195], a[108:111], a[12:15], v[192:195]\n"
"v_exp_f32 v206, v206\n"
"v_mfma_f32_16x16x32_bf16 v[196:199], a[112:115], a[0:3], v[216:219]\n"
"ds_read_b64_tr_b16 a[186:187], v247 offset:38400\n"
"v_add_f32 v245, v245, v206\n"
"v_mfma_f32_16x16x32_bf16 v[196:199], a[116:119], a[4:7], v[196:199]\n"
"v_exp_f32 v207, v207\n"
"v_mfma_f32_16x16x32_bf16 v[196:199], a[120:123], a[8:11], v[196:199]\n"
"ds_read_b128 a[148:151], v246 offset:12800\n"
"v_add_f32 v245, v245, v207\n"
"v_cvt_pk_bf16_f32 v215, v206, v207\n"
"v_mfma_f32_16x16x32_bf16 v[196:199], a[124:127], a[12:15], v[196:199]\n"
"v_exp_f32 v192, v192\n"
"v_mfma_f32_16x16x32_bf16 v[160:163], a[192:195], v[212:215], v[160:163]\n"
"ds_read_b64_tr_b16 a[188:189], v248 offset:34304\n"
"v_add_f32 v240, v240, v192\n"
"v_mfma_f32_16x16x32_bf16 v[164:167], a[196:199], v[212:215], v[164:167]\n"
"v_exp_f32 v193, v193\n"
"v_mfma_f32_16x16x32_bf16 v[168:171], a[200:203], v[212:215], v[168:171]\n"
"ds_read_b128 a[152:155], v246 offset:13312\n"
"v_add_f32 v240, v240, v193\n"
"v_cvt_pk_bf16_f32 v208, v192, v193\n"
"v_mfma_f32_16x16x32_bf16 v[172:175], a[204:207], v[212:215], v[172:175]\n"
"v_exp_f32 v194, v194\n"
"v_mfma_f32_16x16x32_bf16 v[176:179], a[208:211], v[212:215], v[176:179]\n"
"ds_read_b64_tr_b16 a[190:191], v248 offset:38400\n"
"v_add_f32 v240, v240, v194\n"
"v_mfma_f32_16x16x32_bf16 v[180:183], a[212:215], v[212:215], v[180:183]\n"
"v_exp_f32 v195, v195\n"
"v_mfma_f32_16x16x32_bf16 v[184:187], a[216:219], v[212:215], v[184:187]\n"
"ds_read_b128 a[156:159], v246 offset:13824\n"
"v_add_f32 v240, v240, v195\n"
"v_cvt_pk_bf16_f32 v209, v194, v195\n"
"v_mfma_f32_16x16x32_bf16 v[188:191], a[220:223], v[212:215], v[188:191]\n"
"s_barrier\n"
"v_exp_f32 v196, v196\n"
"v_mfma_f32_16x16x32_bf16 v[200:203], a[128:131], a[0:3], v[216:219]\n"
"s_waitcnt lgkmcnt(4)\n"
"ds_read_b64_tr_b16 a[192:193], v247 offset:40960\n"
"v_add_f32 v240, v240, v196\n"
"v_mfma_f32_16x16x32_bf16 v[200:203], a[132:135], a[4:7], v[200:203]\n"
"s_add_u32 s68, s61, 188416\n"
"ds_read_b64_tr_b16 a[194:195], v247 offset:45056\n"
"v_exp_f32 v197, v197\n"
"v_mfma_f32_16x16x32_bf16 v[200:203], a[136:139], a[8:11], v[200:203]\n"
"s_mov_b32 s50, 376832\n"
"ds_read_b64_tr_b16 a[196:197], v248 offset:40960\n"
"v_add_f32 v240, v240, v197\n"
"v_cvt_pk_bf16_f32 v210, v196, v197\n"
"v_mfma_f32_16x16x32_bf16 v[200:203], a[140:143], a[12:15], v[200:203]\n"
"ds_read_b64_tr_b16 a[198:199], v248 offset:45056\n"
"v_exp_f32 v198, v198\n"
"v_mfma_f32_16x16x32_bf16 v[204:207], a[144:147], a[0:3], v[216:219]\n"
"ds_read_b64_tr_b16 a[200:201], v247 offset:41472\n"
"v_add_f32 v240, v240, v198\n"
"v_mfma_f32_16x16x32_bf16 v[204:207], a[148:151], a[4:7], v[204:207]\n"
"s_waitcnt lgkmcnt(5)\n"
"ds_read_b64_tr_b16 a[202:203], v247 offset:45568\n"
"v_exp_f32 v199, v199\n"
"v_mfma_f32_16x16x32_bf16 v[204:207], a[152:155], a[8:11], v[204:207]\n"
"ds_read_b64_tr_b16 a[204:205], v248 offset:41472\n"
"v_add_f32 v240, v240, v199\n"
"v_cvt_pk_bf16_f32 v211, v198, v199\n"
"v_mfma_f32_16x16x32_bf16 v[204:207], a[156:159], a[12:15], v[204:207]\n"
"v_exp_f32 v200, v200\n"
"v_mfma_f32_16x16x32_bf16 v[0:3], a[160:163], v[208:211], v[0:3]\n"
"ds_read_b64_tr_b16 a[206:207], v248 offset:45568\n"
"v_add_f32 v240, v240, v200\n"
"v_mfma_f32_16x16x32_bf16 v[4:7], a[164:167], v[208:211], v[4:7]\n"
"v_exp_f32 v201, v201\n"
"v_mfma_f32_16x16x32_bf16 v[8:11], a[168:171], v[208:211], v[8:11]\n"
"ds_read_b64_tr_b16 a[208:209], v247 offset:41984\n"
"v_add_f32 v240, v240, v201\n"
"v_cvt_pk_bf16_f32 v212, v200, v201\n"
"v_mfma_f32_16x16x32_bf16 v[12:15], a[172:175], v[208:211], v[12:15]\n"
"v_exp_f32 v202, v202\n"
"v_mfma_f32_16x16x32_bf16 v[16:19], a[176:179], v[208:211], v[16:19]\n"
"ds_read_b64_tr_b16 a[210:211], v247 offset:46080\n"
"v_add_f32 v240, v240, v202\n"
"v_mfma_f32_16x16x32_bf16 v[20:23], a[180:183], v[208:211], v[20:23]\n"
"v_exp_f32 v203, v203\n"
"v_mfma_f32_16x16x32_bf16 v[24:27], a[184:187], v[208:211], v[24:27]\n"
"ds_read_b64_tr_b16 a[212:213], v248 offset:41984\n"
"v_add_f32 v240, v240, v203\n"
"v_cvt_pk_bf16_f32 v213, v202, v203\n"
"v_mfma_f32_16x16x32_bf16 v[28:31], a[188:191], v[208:211], v[28:31]\n"
"v_exp_f32 v204, v204\n"
"v_mfma_f32_16x16x32_bf16 v[192:195], a[96:99], a[16:19], v[220:223]\n"
"ds_read_b64_tr_b16 a[214:215], v248 offset:46080\n"
"v_add_f32 v240, v240, v204\n"
"v_mfma_f32_16x16x32_bf16 v[192:195], a[100:103], a[20:23], v[192:195]\n"
"v_exp_f32 v205, v205\n"
"v_mfma_f32_16x16x32_bf16 v[192:195], a[104:107], a[24:27], v[192:195]\n"
"ds_read_b64_tr_b16 a[216:217], v247 offset:42496\n"
"v_add_f32 v240, v240, v205\n"
"v_cvt_pk_bf16_f32 v214, v204, v205\n"
"v_mfma_f32_16x16x32_bf16 v[192:195], a[108:111], a[28:31], v[192:195]\n"
"v_exp_f32 v206, v206\n"
"v_mfma_f32_16x16x32_bf16 v[196:199], a[112:115], a[16:19], v[220:223]\n"
"ds_read_b64_tr_b16 a[218:219], v247 offset:46592\n"
"v_add_f32 v240, v240, v206\n"
"v_mfma_f32_16x16x32_bf16 v[196:199], a[116:119], a[20:23], v[196:199]\n"
"v_exp_f32 v207, v207\n"
"v_mfma_f32_16x16x32_bf16 v[196:199], a[120:123], a[24:27], v[196:199]\n"
"ds_read_b64_tr_b16 a[220:221], v248 offset:42496\n"
"v_add_f32 v240, v240, v207\n"
"v_cvt_pk_bf16_f32 v215, v206, v207\n"
"v_mfma_f32_16x16x32_bf16 v[196:199], a[124:127], a[28:31], v[196:199]\n"
"s_waitcnt lgkmcnt(5)\n"
"v_exp_f32 v192, v192\n"
"v_mfma_f32_16x16x32_bf16 v[0:3], a[192:195], v[212:215], v[0:3]\n"
"ds_read_b64_tr_b16 a[222:223], v248 offset:46592\n"
"v_add_f32 v241, v241, v192\n"
"v_mfma_f32_16x16x32_bf16 v[4:7], a[196:199], v[212:215], v[4:7]\n"
"v_exp_f32 v193, v193\n"
"v_mfma_f32_16x16x32_bf16 v[8:11], a[200:203], v[212:215], v[8:11]\n"
"v_add_f32 v241, v241, v193\n"
"v_cvt_pk_bf16_f32 v208, v192, v193\n"
"v_mfma_f32_16x16x32_bf16 v[12:15], a[204:207], v[212:215], v[12:15]\n"
"v_exp_f32 v194, v194\n"
"v_mfma_f32_16x16x32_bf16 v[16:19], a[208:211], v[212:215], v[16:19]\n"
"s_waitcnt lgkmcnt(3)\n"
"v_add_f32 v241, v241, v194\n"
"v_subrev_u32 v253, 17408, v253\n"
"v_mfma_f32_16x16x32_bf16 v[20:23], a[212:215], v[212:215], v[20:23]\n"
"s_waitcnt lgkmcnt(2)\n"
"v_exp_f32 v195, v195\n"
"v_mfma_f32_16x16x32_bf16 v[24:27], a[216:219], v[212:215], v[24:27]\n"
"s_waitcnt lgkmcnt(0)\n"
"v_add_f32 v241, v241, v195\n"
"v_cvt_pk_bf16_f32 v209, v194, v195\n"
"v_mfma_f32_16x16x32_bf16 v[28:31], a[220:223], v[212:215], v[28:31]\n"
"v_exp_f32 v196, v196\n"
"v_add_u32 v249, 2176, v253\n"
"v_mfma_f32_16x16x4_f32 v[216:219], v250, v240, 0\n"
"s_nop 9\n"
"v_rcp_f32 v218, v216\n"
"v_med3_f32 v217, v216, v251, s73\n"
"v_cmp_neq_f32 vcc, v217, v216\n"
"v_mul_f32 v219, s99, v218\n"
"v_pk_fma_f32 v[0:1], v[0:1], v[218:219], v[218:219] op_sel:[0,0,1] op_sel_hi:[1,0,1] neg_lo:[0,0,1] neg_hi:[0,0,1]\n"
"v_pk_fma_f32 v[2:3], v[2:3], v[218:219], v[218:219] op_sel:[0,0,1] op_sel_hi:[1,0,1] neg_lo:[0,0,1] neg_hi:[0,0,1]\n"
"v_pk_fma_f32 v[4:5], v[4:5], v[218:219], v[218:219] op_sel:[0,0,1] op_sel_hi:[1,0,1] neg_lo:[0,0,1] neg_hi:[0,0,1]\n"
"v_pk_fma_f32 v[6:7], v[6:7], v[218:219], v[218:219] op_sel:[0,0,1] op_sel_hi:[1,0,1] neg_lo:[0,0,1] neg_hi:[0,0,1]\n"
"v_pk_fma_f32 v[8:9], v[8:9], v[218:219], v[218:219] op_sel:[0,0,1] op_sel_hi:[1,0,1] neg_lo:[0,0,1] neg_hi:[0,0,1]\n"
"v_pk_fma_f32 v[10:11], v[10:11], v[218:219], v[218:219] op_sel:[0,0,1] op_sel_hi:[1,0,1] neg_lo:[0,0,1] neg_hi:[0,0,1]\n"
"v_pk_fma_f32 v[12:13], v[12:13], v[218:219], v[218:219] op_sel:[0,0,1] op_sel_hi:[1,0,1] neg_lo:[0,0,1] neg_hi:[0,0,1]\n"
"v_pk_fma_f32 v[14:15], v[14:15], v[218:219], v[218:219] op_sel:[0,0,1] op_sel_hi:[1,0,1] neg_lo:[0,0,1] neg_hi:[0,0,1]\n"
"v_pk_fma_f32 v[16:17], v[16:17], v[218:219], v[218:219] op_sel:[0,0,1] op_sel_hi:[1,0,1] neg_lo:[0,0,1] neg_hi:[0,0,1]\n"
"v_pk_fma_f32 v[18:19], v[18:19], v[218:219], v[218:219] op_sel:[0,0,1] op_sel_hi:[1,0,1] neg_lo:[0,0,1] neg_hi:[0,0,1]\n"
"v_pk_fma_f32 v[20:21], v[20:21], v[218:219], v[218:219] op_sel:[0,0,1] op_sel_hi:[1,0,1] neg_lo:[0,0,1] neg_hi:[0,0,1]\n"
"v_pk_fma_f32 v[22:23], v[22:23], v[218:219], v[218:219] op_sel:[0,0,1] op_sel_hi:[1,0,1] neg_lo:[0,0,1] neg_hi:[0,0,1]\n"
"v_pk_fma_f32 v[24:25], v[24:25], v[218:219], v[218:219] op_sel:[0,0,1] op_sel_hi:[1,0,1] neg_lo:[0,0,1] neg_hi:[0,0,1]\n"
"v_pk_fma_f32 v[26:27], v[26:27], v[218:219], v[218:219] op_sel:[0,0,1] op_sel_hi:[1,0,1] neg_lo:[0,0,1] neg_hi:[0,0,1]\n"
"v_pk_fma_f32 v[28:29], v[28:29], v[218:219], v[218:219] op_sel:[0,0,1] op_sel_hi:[1,0,1] neg_lo:[0,0,1] neg_hi:[0,0,1]\n"
"v_pk_fma_f32 v[30:31], v[30:31], v[218:219], v[218:219] op_sel:[0,0,1] op_sel_hi:[1,0,1] neg_lo:[0,0,1] neg_hi:[0,0,1]\n"
"s_cmp_lg_u64 vcc, 0\n"
"s_addc_u32 s55, s55, s55\n"
"v_cvt_pk_bf16_f32 v0, v0, v1\n"
"v_cvt_pk_bf16_f32 v1, v2, v3\n"
"v_cvt_pk_bf16_f32 v2, v4, v5\n"
"v_cvt_pk_bf16_f32 v3, v6, v7\n"
"v_cvt_pk_bf16_f32 v4, v8, v9\n"
"v_cvt_pk_bf16_f32 v5, v10, v11\n"
"v_cvt_pk_bf16_f32 v6, v12, v13\n"
"v_cvt_pk_bf16_f32 v7, v14, v15\n"
"v_cvt_pk_bf16_f32 v8, v16, v17\n"
"v_cvt_pk_bf16_f32 v9, v18, v19\n"
"v_cvt_pk_bf16_f32 v10, v20, v21\n"
"v_cvt_pk_bf16_f32 v11, v22, v23\n"
"v_cvt_pk_bf16_f32 v12, v24, v25\n"
"v_cvt_pk_bf16_f32 v13, v26, v27\n"
"v_cvt_pk_bf16_f32 v14, v28, v29\n"
"v_cvt_pk_bf16_f32 v15, v30, v31\n"
"v_mfma_f32_16x16x32_bf16 v[200:203], a[128:131], a[16:19], v[220:223]\n"
"ds_write_b128 v252, v[0:3] offset:0\n"
"v_add_f32 v241, v241, v196\n"
"v_mfma_f32_16x16x32_bf16 v[200:203], a[132:135], a[20:23], v[200:203]\n"
"v_exp_f32 v197, v197\n"
"v_mfma_f32_16x16x32_bf16 v[200:203], a[136:139], a[24:27], v[200:203]\n"
"ds_write_b128 v252, v[4:7] offset:64\n"
"v_add_f32 v241, v241, v197\n"
"v_cvt_pk_bf16_f32 v210, v196, v197\n"
"v_mfma_f32_16x16x32_bf16 v[200:203], a[140:143], a[28:31], v[200:203]\n"
"v_exp_f32 v198, v198\n"
"v_mfma_f32_16x16x32_bf16 v[204:207], a[144:147], a[16:19], v[220:223]\n"
"ds_write_b128 v252, v[8:11] offset:128\n"
"v_add_f32 v241, v241, v198\n"
"v_mfma_f32_16x16x32_bf16 v[204:207], a[148:151], a[20:23], v[204:207]\n"
"v_exp_f32 v199, v199\n"
"v_mfma_f32_16x16x32_bf16 v[204:207], a[152:155], a[24:27], v[204:207]\n"
"ds_write_b128 v252, v[12:15] offset:192\n"
"v_add_f32 v241, v241, v199\n"
"v_cvt_pk_bf16_f32 v211, v198, v199\n"
"v_mfma_f32_16x16x32_bf16 v[204:207], a[156:159], a[28:31], v[204:207]\n"
"v_exp_f32 v200, v200\n"
"v_mfma_f32_16x16x32_bf16 v[32:35], a[160:163], v[208:211], v[32:35]\n"
"ds_read2_b64 v[0:3], v253 offset0:0 offset1:2\n"
"v_add_f32 v241, v241, v200\n"
"v_mfma_f32_16x16x32_bf16 v[36:39], a[164:167], v[208:211], v[36:39]\n"
"ds_read2_b64 v[4:7], v253 offset0:136 offset1:138\n"
"v_exp_f32 v201, v201\n"
"v_mfma_f32_16x16x32_bf16 v[40:43], a[168:171], v[208:211], v[40:43]\n"
"ds_read2_b64 v[8:11], v249 offset0:0 offset1:2\n"
"v_add_f32 v241, v241, v201\n"
"v_cvt_pk_bf16_f32 v212, v200, v201\n"
"v_mfma_f32_16x16x32_bf16 v[44:47], a[172:175], v[208:211], v[44:47]\n"
"ds_read2_b64 v[12:15], v249 offset0:136 offset1:138\n"
"v_exp_f32 v202, v202\n"
"v_mfma_f32_16x16x32_bf16 v[48:51], a[176:179], v[208:211], v[48:51]\n"
"v_add_f32 v241, v241, v202\n"
"v_mfma_f32_16x16x32_bf16 v[52:55], a[180:183], v[208:211], v[52:55]\n"
"v_exp_f32 v203, v203\n"
"v_mfma_f32_16x16x32_bf16 v[56:59], a[184:187], v[208:211], v[56:59]\n"
"v_add_f32 v241, v241, v203\n"
"v_cvt_pk_bf16_f32 v213, v202, v203\n"
"v_mfma_f32_16x16x32_bf16 v[60:63], a[188:191], v[208:211], v[60:63]\n"
"v_exp_f32 v204, v204\n"
"v_mfma_f32_16x16x32_bf16 v[192:195], a[96:99], a[32:35], v[224:227]\n"
"v_add_f32 v241, v241, v204\n"
"v_mfma_f32_16x16x32_bf16 v[192:195], a[100:103], a[36:39], v[192:195]\n"
"v_exp_f32 v205, v205\n"
"v_mfma_f32_16x16x32_bf16 v[192:195], a[104:107], a[40:43], v[192:195]\n"
"v_add_f32 v241, v241, v205\n"
"v_cvt_pk_bf16_f32 v214, v204, v205\n"
"v_mfma_f32_16x16x32_bf16 v[192:195], a[108:111], a[44:47], v[192:195]\n"
"s_waitcnt lgkmcnt(0)\n"
"v_exp_f32 v206, v206\n"
"v_mfma_f32_16x16x32_bf16 v[196:199], a[112:115], a[32:35], v[224:227]\n"
"buffer_store_dwordx4 v[0:3], v255, s[48:51], s68 offen sc1\n"
"v_add_f32 v241, v241, v206\n"
"v_mfma_f32_16x16x32_bf16 v[196:199], a[116:119], a[36:39], v[196:199]\n"
"v_exp_f32 v207, v207\n"
"v_mfma_f32_16x16x32_bf16 v[196:199], a[120:123], a[40:43], v[196:199]\n"
"v_add_f32 v241, v241, v207\n"
"v_cvt_pk_bf16_f32 v215, v206, v207\n"
"v_mfma_f32_16x16x32_bf16 v[196:199], a[124:127], a[44:47], v[196:199]\n"
"v_exp_f32 v192, v192\n"
"v_mfma_f32_16x16x32_bf16 v[32:35], a[192:195], v[212:215], v[32:35]\n"
"buffer_store_dwordx4 v[4:7], v255, s[48:51], s68 offen offset:1024 sc1\n"
"v_add_f32 v242, v242, v192\n"
"v_mfma_f32_16x16x32_bf16 v[36:39], a[196:199], v[212:215], v[36:39]\n"
"v_exp_f32 v193, v193\n"
"v_mfma_f32_16x16x32_bf16 v[40:43], a[200:203], v[212:215], v[40:43]\n"
"v_add_f32 v242, v242, v193\n"
"v_cvt_pk_bf16_f32 v208, v192, v193\n"
"v_mfma_f32_16x16x32_bf16 v[44:47], a[204:207], v[212:215], v[44:47]\n"
"v_exp_f32 v194, v194\n"
"v_mfma_f32_16x16x32_bf16 v[48:51], a[208:211], v[212:215], v[48:51]\n"
"buffer_store_dwordx4 v[8:11], v255, s[48:51], s68 offen offset:2048 sc1\n"
"v_add_f32 v242, v242, v194\n"
"v_mfma_f32_16x16x32_bf16 v[52:55], a[212:215], v[212:215], v[52:55]\n"
"v_exp_f32 v195, v195\n"
"v_mfma_f32_16x16x32_bf16 v[56:59], a[216:219], v[212:215], v[56:59]\n"
"v_add_f32 v242, v242, v195\n"
"v_cvt_pk_bf16_f32 v209, v194, v195\n"
"v_mfma_f32_16x16x32_bf16 v[60:63], a[220:223], v[212:215], v[60:63]\n"
"v_exp_f32 v196, v196\n"
"v_mfma_f32_16x16x4_f32 v[220:223], v250, v241, 0\n"
"s_nop 9\n"
"v_rcp_f32 v222, v220\n"
"v_med3_f32 v221, v220, v251, s73\n"
"v_cmp_neq_f32 vcc, v221, v220\n"
"v_mul_f32 v223, s99, v222\n"
"v_pk_fma_f32 v[32:33], v[32:33], v[222:223], v[222:223] op_sel:[0,0,1] op_sel_hi:[1,0,1] neg_lo:[0,0,1] neg_hi:[0,0,1]\n"
"v_pk_fma_f32 v[34:35], v[34:35], v[222:223], v[222:223] op_sel:[0,0,1] op_sel_hi:[1,0,1] neg_lo:[0,0,1] neg_hi:[0,0,1]\n"
"v_pk_fma_f32 v[36:37], v[36:37], v[222:223], v[222:223] op_sel:[0,0,1] op_sel_hi:[1,0,1] neg_lo:[0,0,1] neg_hi:[0,0,1]\n"
"v_pk_fma_f32 v[38:39], v[38:39], v[222:223], v[222:223] op_sel:[0,0,1] op_sel_hi:[1,0,1] neg_lo:[0,0,1] neg_hi:[0,0,1]\n"
"v_pk_fma_f32 v[40:41], v[40:41], v[222:223], v[222:223] op_sel:[0,0,1] op_sel_hi:[1,0,1] neg_lo:[0,0,1] neg_hi:[0,0,1]\n"
"v_pk_fma_f32 v[42:43], v[42:43], v[222:223], v[222:223] op_sel:[0,0,1] op_sel_hi:[1,0,1] neg_lo:[0,0,1] neg_hi:[0,0,1]\n"
"v_pk_fma_f32 v[44:45], v[44:45], v[222:223], v[222:223] op_sel:[0,0,1] op_sel_hi:[1,0,1] neg_lo:[0,0,1] neg_hi:[0,0,1]\n"
"v_pk_fma_f32 v[46:47], v[46:47], v[222:223], v[222:223] op_sel:[0,0,1] op_sel_hi:[1,0,1] neg_lo:[0,0,1] neg_hi:[0,0,1]\n"
"v_pk_fma_f32 v[48:49], v[48:49], v[222:223], v[222:223] op_sel:[0,0,1] op_sel_hi:[1,0,1] neg_lo:[0,0,1] neg_hi:[0,0,1]\n"
"v_pk_fma_f32 v[50:51], v[50:51], v[222:223], v[222:223] op_sel:[0,0,1] op_sel_hi:[1,0,1] neg_lo:[0,0,1] neg_hi:[0,0,1]\n"
"v_pk_fma_f32 v[52:53], v[52:53], v[222:223], v[222:223] op_sel:[0,0,1] op_sel_hi:[1,0,1] neg_lo:[0,0,1] neg_hi:[0,0,1]\n"
"v_pk_fma_f32 v[54:55], v[54:55], v[222:223], v[222:223] op_sel:[0,0,1] op_sel_hi:[1,0,1] neg_lo:[0,0,1] neg_hi:[0,0,1]\n"
"v_pk_fma_f32 v[56:57], v[56:57], v[222:223], v[222:223] op_sel:[0,0,1] op_sel_hi:[1,0,1] neg_lo:[0,0,1] neg_hi:[0,0,1]\n"
"v_pk_fma_f32 v[58:59], v[58:59], v[222:223], v[222:223] op_sel:[0,0,1] op_sel_hi:[1,0,1] neg_lo:[0,0,1] neg_hi:[0,0,1]\n"
"v_pk_fma_f32 v[60:61], v[60:61], v[222:223], v[222:223] op_sel:[0,0,1] op_sel_hi:[1,0,1] neg_lo:[0,0,1] neg_hi:[0,0,1]\n"
"v_pk_fma_f32 v[62:63], v[62:63], v[222:223], v[222:223] op_sel:[0,0,1] op_sel_hi:[1,0,1] neg_lo:[0,0,1] neg_hi:[0,0,1]\n"
"s_cmp_lg_u64 vcc, 0\n"
"s_addc_u32 s55, s55, s55\n"
"v_cvt_pk_bf16_f32 v32, v32, v33\n"
"v_cvt_pk_bf16_f32 v33, v34, v35\n"
"v_cvt_pk_bf16_f32 v34, v36, v37\n"
"v_cvt_pk_bf16_f32 v35, v38, v39\n"
"v_cvt_pk_bf16_f32 v36, v40, v41\n"
"v_cvt_pk_bf16_f32 v37, v42, v43\n"
"v_cvt_pk_bf16_f32 v38, v44, v45\n"
"v_cvt_pk_bf16_f32 v39, v46, v47\n"
"v_cvt_pk_bf16_f32 v40, v48, v49\n"
"v_cvt_pk_bf16_f32 v41, v50, v51\n"
"v_cvt_pk_bf16_f32 v42, v52, v53\n"
"v_cvt_pk_bf16_f32 v43, v54, v55\n"
"v_cvt_pk_bf16_f32 v44, v56, v57\n"
"v_cvt_pk_bf16_f32 v45, v58, v59\n"
"v_cvt_pk_bf16_f32 v46, v60, v61\n"
"v_cvt_pk_bf16_f32 v47, v62, v63\n"
"v_mfma_f32_16x16x32_bf16 v[200:203], a[128:131], a[32:35], v[224:227]\n"
"ds_write_b128 v252, v[32:35] offset:0\n"
"buffer_store_dwordx4 v[12:15], v255, s[48:51], s68 offen offset:3072 sc1\n"
"v_add_f32 v242, v242, v196\n"
"v_mfma_f32_16x16x32_bf16 v[200:203], a[132:135], a[36:39], v[200:203]\n"
"s_add_u32 s68, s68, 4096\n"
"v_exp_f32 v197, v197\n"
"v_mfma_f32_16x16x32_bf16 v[200:203], a[136:139], a[40:43], v[200:203]\n"
"ds_write_b128 v252, v[36:39] offset:64\n"
"v_add_f32 v242, v242, v197\n"
"v_cvt_pk_bf16_f32 v210, v196, v197\n"
"v_mfma_f32_16x16x32_bf16 v[200:203], a[140:143], a[44:47], v[200:203]\n"
"v_exp_f32 v198, v198\n"
"v_mfma_f32_16x16x32_bf16 v[204:207], a[144:147], a[32:35], v[224:227]\n"
"ds_write_b128 v252, v[40:43] offset:128\n"
"v_add_f32 v242, v242, v198\n"
"v_mfma_f32_16x16x32_bf16 v[204:207], a[148:151], a[36:39], v[204:207]\n"
"v_exp_f32 v199, v199\n"
"v_mfma_f32_16x16x32_bf16 v[204:207], a[152:155], a[40:43], v[204:207]\n"
"ds_write_b128 v252, v[44:47] offset:192\n"
"v_add_f32 v242, v242, v199\n"
"v_cvt_pk_bf16_f32 v211, v198, v199\n"
"v_mfma_f32_16x16x32_bf16 v[204:207], a[156:159], a[44:47], v[204:207]\n"
"v_exp_f32 v200, v200\n"
"v_mfma_f32_16x16x32_bf16 v[64:67], a[160:163], v[208:211], v[64:67]\n"
"ds_read2_b64 v[32:35], v253 offset0:0 offset1:2\n"
"v_add_f32 v242, v242, v200\n"
"v_mfma_f32_16x16x32_bf16 v[68:71], a[164:167], v[208:211], v[68:71]\n"
"ds_read2_b64 v[36:39], v253 offset0:136 offset1:138\n"
"v_exp_f32 v201, v201\n"
"v_mfma_f32_16x16x32_bf16 v[72:75], a[168:171], v[208:211], v[72:75]\n"
"ds_read2_b64 v[40:43], v249 offset0:0 offset1:2\n"
"v_add_f32 v242, v242, v201\n"
"v_cvt_pk_bf16_f32 v212, v200, v201\n"
"v_mfma_f32_16x16x32_bf16 v[76:79], a[172:175], v[208:211], v[76:79]\n"
"ds_read2_b64 v[44:47], v249 offset0:136 offset1:138\n"
"v_exp_f32 v202, v202\n"
"v_mfma_f32_16x16x32_bf16 v[80:83], a[176:179], v[208:211], v[80:83]\n"
"v_add_f32 v242, v242, v202\n"
"v_mfma_f32_16x16x32_bf16 v[84:87], a[180:183], v[208:211], v[84:87]\n"
"v_exp_f32 v203, v203\n"
"v_mfma_f32_16x16x32_bf16 v[88:91], a[184:187], v[208:211], v[88:91]\n"
"v_add_f32 v242, v242, v203\n"
"v_cvt_pk_bf16_f32 v213, v202, v203\n"
"v_mfma_f32_16x16x32_bf16 v[92:95], a[188:191], v[208:211], v[92:95]\n"
"v_exp_f32 v204, v204\n"
"v_mfma_f32_16x16x32_bf16 v[192:195], a[96:99], a[48:51], v[228:231]\n"
"v_add_f32 v242, v242, v204\n"
"v_mfma_f32_16x16x32_bf16 v[192:195], a[100:103], a[52:55], v[192:195]\n"
"v_exp_f32 v205, v205\n"
"v_mfma_f32_16x16x32_bf16 v[192:195], a[104:107], a[56:59], v[192:195]\n"
"v_add_f32 v242, v242, v205\n"
"v_cvt_pk_bf16_f32 v214, v204, v205\n"
"v_mfma_f32_16x16x32_bf16 v[192:195], a[108:111], a[60:63], v[192:195]\n"
"s_waitcnt lgkmcnt(0)\n"
"v_exp_f32 v206, v206\n"
"v_mfma_f32_16x16x32_bf16 v[196:199], a[112:115], a[48:51], v[228:231]\n"
"Lhop2%=:\n"
"s_cbranch_execz Lhop6%=\n"
"buffer_store_dwordx4 v[32:35], v255, s[48:51], s68 offen sc1\n"
"v_add_f32 v242, v242, v206\n"
"v_mfma_f32_16x16x32_bf16 v[196:199], a[116:119], a[52:55], v[196:199]\n"
"v_exp_f32 v207, v207\n"
"v_mfma_f32_16x16x32_bf16 v[196:199], a[120:123], a[56:59], v[196:199]\n"
"v_add_f32 v242, v242, v207\n"
"v_cvt_pk_bf16_f32 v215, v206, v207\n"
"v_mfma_f32_16x16x32_bf16 v[196:199], a[124:127], a[60:63], v[196:199]\n"
"v_exp_f32 v192, v192\n"
"v_mfma_f32_16x16x32_bf16 v[64:67], a[192:195], v[212:215], v[64:67]\n"
"buffer_store_dwordx4 v[36:39], v255, s[48:51], s68 offen offset:1024 sc1\n"
"v_add_f32 v243, v243, v192\n"
"v_mfma_f32_16x16x32_bf16 v[68:71], a[196:199], v[212:215], v[68:71]\n"
"v_exp_f32 v193, v193\n"
"v_mfma_f32_16x16x32_bf16 v[72:75], a[200:203], v[212:215], v[72:75]\n"
"v_add_f32 v243, v243, v193\n"
"v_cvt_pk_bf16_f32 v208, v192, v193\n"
"v_mfma_f32_16x16x32_bf16 v[76:79], a[204:207], v[212:215], v[76:79]\n"
"v_exp_f32 v194, v194\n"
"v_mfma_f32_16x16x32_bf16 v[80:83], a[208:211], v[212:215], v[80:83]\n"
"buffer_store_dwordx4 v[40:43], v255, s[48:51], s68 offen offset:2048 sc1\n"
"v_add_f32 v243, v243, v194\n"
"v_mfma_f32_16x16x32_bf16 v[84:87], a[212:215], v[212:215], v[84:87]\n"
"v_exp_f32 v195, v195\n"
"v_mfma_f32_16x16x32_bf16 v[88:91], a[216:219], v[212:215], v[88:91]\n"
"v_add_f32 v243, v243, v195\n"
"v_cvt_pk_bf16_f32 v209, v194, v195\n"
"v_mfma_f32_16x16x32_bf16 v[92:95], a[220:223], v[212:215], v[92:95]\n"
"v_exp_f32 v196, v196\n"
"v_mfma_f32_16x16x4_f32 v[224:227], v250, v242, 0\n"
"s_nop 9\n"
"v_rcp_f32 v226, v224\n"
"v_med3_f32 v225, v224, v251, s73\n"
"v_cmp_neq_f32 vcc, v225, v224\n"
"v_mul_f32 v227, s99, v226\n"
"v_pk_fma_f32 v[64:65], v[64:65], v[226:227], v[226:227] op_sel:[0,0,1] op_sel_hi:[1,0,1] neg_lo:[0,0,1] neg_hi:[0,0,1]\n"
"v_pk_fma_f32 v[66:67], v[66:67], v[226:227], v[226:227] op_sel:[0,0,1] op_sel_hi:[1,0,1] neg_lo:[0,0,1] neg_hi:[0,0,1]\n"
"v_pk_fma_f32 v[68:69], v[68:69], v[226:227], v[226:227] op_sel:[0,0,1] op_sel_hi:[1,0,1] neg_lo:[0,0,1] neg_hi:[0,0,1]\n"
"v_pk_fma_f32 v[70:71], v[70:71], v[226:227], v[226:227] op_sel:[0,0,1] op_sel_hi:[1,0,1] neg_lo:[0,0,1] neg_hi:[0,0,1]\n"
"v_pk_fma_f32 v[72:73], v[72:73], v[226:227], v[226:227] op_sel:[0,0,1] op_sel_hi:[1,0,1] neg_lo:[0,0,1] neg_hi:[0,0,1]\n"
"v_pk_fma_f32 v[74:75], v[74:75], v[226:227], v[226:227] op_sel:[0,0,1] op_sel_hi:[1,0,1] neg_lo:[0,0,1] neg_hi:[0,0,1]\n"
"v_pk_fma_f32 v[76:77], v[76:77], v[226:227], v[226:227] op_sel:[0,0,1] op_sel_hi:[1,0,1] neg_lo:[0,0,1] neg_hi:[0,0,1]\n"
"v_pk_fma_f32 v[78:79], v[78:79], v[226:227], v[226:227] op_sel:[0,0,1] op_sel_hi:[1,0,1] neg_lo:[0,0,1] neg_hi:[0,0,1]\n"
"v_pk_fma_f32 v[80:81], v[80:81], v[226:227], v[226:227] op_sel:[0,0,1] op_sel_hi:[1,0,1] neg_lo:[0,0,1] neg_hi:[0,0,1]\n"
"v_pk_fma_f32 v[82:83], v[82:83], v[226:227], v[226:227] op_sel:[0,0,1] op_sel_hi:[1,0,1] neg_lo:[0,0,1] neg_hi:[0,0,1]\n"
"v_pk_fma_f32 v[84:85], v[84:85], v[226:227], v[226:227] op_sel:[0,0,1] op_sel_hi:[1,0,1] neg_lo:[0,0,1] neg_hi:[0,0,1]\n"
"v_pk_fma_f32 v[86:87], v[86:87], v[226:227], v[226:227] op_sel:[0,0,1] op_sel_hi:[1,0,1] neg_lo:[0,0,1] neg_hi:[0,0,1]\n"
"v_pk_fma_f32 v[88:89], v[88:89], v[226:227], v[226:227] op_sel:[0,0,1] op_sel_hi:[1,0,1] neg_lo:[0,0,1] neg_hi:[0,0,1]\n"
"v_pk_fma_f32 v[90:91], v[90:91], v[226:227], v[226:227] op_sel:[0,0,1] op_sel_hi:[1,0,1] neg_lo:[0,0,1] neg_hi:[0,0,1]\n"
"v_pk_fma_f32 v[92:93], v[92:93], v[226:227], v[226:227] op_sel:[0,0,1] op_sel_hi:[1,0,1] neg_lo:[0,0,1] neg_hi:[0,0,1]\n"
"v_pk_fma_f32 v[94:95], v[94:95], v[226:227], v[226:227] op_sel:[0,0,1] op_sel_hi:[1,0,1] neg_lo:[0,0,1] neg_hi:[0,0,1]\n"
"s_cmp_lg_u64 vcc, 0\n"
"s_addc_u32 s55, s55, s55\n"
"v_cvt_pk_bf16_f32 v64, v64, v65\n"
"v_cvt_pk_bf16_f32 v65, v66, v67\n"
"v_cvt_pk_bf16_f32 v66, v68, v69\n"
"v_cvt_pk_bf16_f32 v67, v70, v71\n"
"v_cvt_pk_bf16_f32 v68, v72, v73\n"
"v_cvt_pk_bf16_f32 v69, v74, v75\n"
"v_cvt_pk_bf16_f32 v70, v76, v77\n"
"v_cvt_pk_bf16_f32 v71, v78, v79\n"
"v_cvt_pk_bf16_f32 v72, v80, v81\n"
"v_cvt_pk_bf16_f32 v73, v82, v83\n"
"v_cvt_pk_bf16_f32 v74, v84, v85\n"
"v_cvt_pk_bf16_f32 v75, v86, v87\n"
"v_cvt_pk_bf16_f32 v76, v88, v89\n"
"v_cvt_pk_bf16_f32 v77, v90, v91\n"
"v_cvt_pk_bf16_f32 v78, v92, v93\n"
"v_cvt_pk_bf16_f32 v79, v94, v95\n"
"v_mfma_f32_16x16x32_bf16 v[200:203], a[128:131], a[48:51], v[228:231]\n"
"ds_write_b128 v252, v[64:67] offset:0\n"
"buffer_store_dwordx4 v[44:47], v255, s[48:51], s68 offen offset:3072 sc1\n"
"v_add_f32 v243, v243, v196\n"
"v_mfma_f32_16x16x32_bf16 v[200:203], a[132:135], a[52:55], v[200:203]\n"
"s_add_u32 s68, s68, 4096\n"
"v_exp_f32 v197, v197\n"
"v_mfma_f32_16x16x32_bf16 v[200:203], a[136:139], a[56:59], v[200:203]\n"
"ds_write_b128 v252, v[68:71] offset:64\n"
"v_add_f32 v243, v243, v197\n"
"v_cvt_pk_bf16_f32 v210, v196, v197\n"
"v_mfma_f32_16x16x32_bf16 v[200:203], a[140:143], a[60:63], v[200:203]\n"
"v_exp_f32 v198, v198\n"
"v_mfma_f32_16x16x32_bf16 v[204:207], a[144:147], a[48:51], v[228:231]\n"
"ds_write_b128 v252, v[72:75] offset:128\n"
"v_add_f32 v243, v243, v198\n"
"v_mfma_f32_16x16x32_bf16 v[204:207], a[148:151], a[52:55], v[204:207]\n"
"v_exp_f32 v199, v199\n"
"v_mfma_f32_16x16x32_bf16 v[204:207], a[152:155], a[56:59], v[204:207]\n"
"ds_write_b128 v252, v[76:79] offset:192\n"
"v_add_f32 v243, v243, v199\n"
"v_cvt_pk_bf16_f32 v211, v198, v199\n"
"v_mfma_f32_16x16x32_bf16 v[204:207], a[156:159], a[60:63], v[204:207]\n"
"v_exp_f32 v200, v200\n"
"v_mfma_f32_16x16x32_bf16 v[96:99], a[160:163], v[208:211], v[96:99]\n"
"ds_read2_b64 v[64:67], v253 offset0:0 offset1:2\n"
"v_add_f32 v243, v243, v200\n"
"v_mfma_f32_16x16x32_bf16 v[100:103], a[164:167], v[208:211], v[100:103]\n"
"ds_read2_b64 v[68:71], v253 offset0:136 offset1:138\n"
"v_exp_f32 v201, v201\n"
"v_mfma_f32_16x16x32_bf16 v[104:107], a[168:171], v[208:211], v[104:107]\n"
"ds_read2_b64 v[72:75], v249 offset0:0 offset1:2\n"
"v_add_f32 v243, v243, v201\n"
"v_cvt_pk_bf16_f32 v212, v200, v201\n"
"v_mfma_f32_16x16x32_bf16 v[108:111], a[172:175], v[208:211], v[108:111]\n"
"ds_read2_b64 v[76:79], v249 offset0:136 offset1:138\n"
"v_exp_f32 v202, v202\n"
"v_mfma_f32_16x16x32_bf16 v[112:115], a[176:179], v[208:211], v[112:115]\n"
"v_add_f32 v243, v243, v202\n"
"v_mfma_f32_16x16x32_bf16 v[116:119], a[180:183], v[208:211], v[116:119]\n"
"v_exp_f32 v203, v203\n"
"v_mfma_f32_16x16x32_bf16 v[120:123], a[184:187], v[208:211], v[120:123]\n"
"v_add_f32 v243, v243, v203\n"
"v_cvt_pk_bf16_f32 v213, v202, v203\n"
"v_mfma_f32_16x16x32_bf16 v[124:127], a[188:191], v[208:211], v[124:127]\n"
"v_exp_f32 v204, v204\n"
"v_mfma_f32_16x16x32_bf16 v[192:195], a[96:99], a[64:67], v[232:235]\n"
"v_add_f32 v243, v243, v204\n"
"v_mfma_f32_16x16x32_bf16 v[192:195], a[100:103], a[68:71], v[192:195]\n"
"v_exp_f32 v205, v205\n"
"v_mfma_f32_16x16x32_bf16 v[192:195], a[104:107], a[72:75], v[192:195]\n"
"v_add_f32 v243, v243, v205\n"
"v_cvt_pk_bf16_f32 v214, v204, v205\n"
"v_mfma_f32_16x16x32_bf16 v[192:195], a[108:111], a[76:79], v[192:195]\n"
"s_waitcnt lgkmcnt(0)\n"
"v_exp_f32 v206, v206\n"
"v_mfma_f32_16x16x32_bf16 v[196:199], a[112:115], a[64:67], v[232:235]\n"
"buffer_store_dwordx4 v[64:67], v255, s[48:51], s68 offen sc1\n"
"v_add_f32 v243, v243, v206\n"
"v_mfma_f32_16x16x32_bf16 v[196:199], a[116:119], a[68:71], v[196:199]\n"
"v_exp_f32 v207, v207\n"
"v_mfma_f32_16x16x32_bf16 v[196:199], a[120:123], a[72:75], v[196:199]\n"
"v_add_f32 v243, v243, v207\n"
"v_cvt_pk_bf16_f32 v215, v206, v207\n"
"v_mfma_f32_16x16x32_bf16 v[196:199], a[124:127], a[76:79], v[196:199]\n"
"v_exp_f32 v192, v192\n"
"v_mfma_f32_16x16x32_bf16 v[96:99], a[192:195], v[212:215], v[96:99]\n"
"buffer_store_dwordx4 v[68:71], v255, s[48:51], s68 offen offset:1024 sc1\n"
"v_add_f32 v244, v244, v192\n"
"v_mfma_f32_16x16x32_bf16 v[100:103], a[196:199], v[212:215], v[100:103]\n"
"v_exp_f32 v193, v193\n"
"v_mfma_f32_16x16x32_bf16 v[104:107], a[200:203], v[212:215], v[104:107]\n"
"v_add_f32 v244, v244, v193\n"
"v_cvt_pk_bf16_f32 v208, v192, v193\n"
"v_mfma_f32_16x16x32_bf16 v[108:111], a[204:207], v[212:215], v[108:111]\n"
"v_exp_f32 v194, v194\n"
"v_mfma_f32_16x16x32_bf16 v[112:115], a[208:211], v[212:215], v[112:115]\n"
"buffer_store_dwordx4 v[72:75], v255, s[48:51], s68 offen offset:2048 sc1\n"
"v_add_f32 v244, v244, v194\n"
"v_mfma_f32_16x16x32_bf16 v[116:119], a[212:215], v[212:215], v[116:119]\n"
"v_exp_f32 v195, v195\n"
"v_mfma_f32_16x16x32_bf16 v[120:123], a[216:219], v[212:215], v[120:123]\n"
"v_add_f32 v244, v244, v195\n"
"v_cvt_pk_bf16_f32 v209, v194, v195\n"
"v_mfma_f32_16x16x32_bf16 v[124:127], a[220:223], v[212:215], v[124:127]\n"
"v_exp_f32 v196, v196\n"
"v_mfma_f32_16x16x4_f32 v[228:231], v250, v243, 0\n"
"s_nop 9\n"
"v_rcp_f32 v230, v228\n"
"v_med3_f32 v229, v228, v251, s73\n"
"v_cmp_neq_f32 vcc, v229, v228\n"
"v_mul_f32 v231, s99, v230\n"
"v_pk_fma_f32 v[96:97], v[96:97], v[230:231], v[230:231] op_sel:[0,0,1] op_sel_hi:[1,0,1] neg_lo:[0,0,1] neg_hi:[0,0,1]\n"
"v_pk_fma_f32 v[98:99], v[98:99], v[230:231], v[230:231] op_sel:[0,0,1] op_sel_hi:[1,0,1] neg_lo:[0,0,1] neg_hi:[0,0,1]\n"
"v_pk_fma_f32 v[100:101], v[100:101], v[230:231], v[230:231] op_sel:[0,0,1] op_sel_hi:[1,0,1] neg_lo:[0,0,1] neg_hi:[0,0,1]\n"
"v_pk_fma_f32 v[102:103], v[102:103], v[230:231], v[230:231] op_sel:[0,0,1] op_sel_hi:[1,0,1] neg_lo:[0,0,1] neg_hi:[0,0,1]\n"
"v_pk_fma_f32 v[104:105], v[104:105], v[230:231], v[230:231] op_sel:[0,0,1] op_sel_hi:[1,0,1] neg_lo:[0,0,1] neg_hi:[0,0,1]\n"
"v_pk_fma_f32 v[106:107], v[106:107], v[230:231], v[230:231] op_sel:[0,0,1] op_sel_hi:[1,0,1] neg_lo:[0,0,1] neg_hi:[0,0,1]\n"
"v_pk_fma_f32 v[108:109], v[108:109], v[230:231], v[230:231] op_sel:[0,0,1] op_sel_hi:[1,0,1] neg_lo:[0,0,1] neg_hi:[0,0,1]\n"
"v_pk_fma_f32 v[110:111], v[110:111], v[230:231], v[230:231] op_sel:[0,0,1] op_sel_hi:[1,0,1] neg_lo:[0,0,1] neg_hi:[0,0,1]\n"
"v_pk_fma_f32 v[112:113], v[112:113], v[230:231], v[230:231] op_sel:[0,0,1] op_sel_hi:[1,0,1] neg_lo:[0,0,1] neg_hi:[0,0,1]\n"
"v_pk_fma_f32 v[114:115], v[114:115], v[230:231], v[230:231] op_sel:[0,0,1] op_sel_hi:[1,0,1] neg_lo:[0,0,1] neg_hi:[0,0,1]\n"
"v_pk_fma_f32 v[116:117], v[116:117], v[230:231], v[230:231] op_sel:[0,0,1] op_sel_hi:[1,0,1] neg_lo:[0,0,1] neg_hi:[0,0,1]\n"
"v_pk_fma_f32 v[118:119], v[118:119], v[230:231], v[230:231] op_sel:[0,0,1] op_sel_hi:[1,0,1] neg_lo:[0,0,1] neg_hi:[0,0,1]\n"
"v_pk_fma_f32 v[120:121], v[120:121], v[230:231], v[230:231] op_sel:[0,0,1] op_sel_hi:[1,0,1] neg_lo:[0,0,1] neg_hi:[0,0,1]\n"
"v_pk_fma_f32 v[122:123], v[122:123], v[230:231], v[230:231] op_sel:[0,0,1] op_sel_hi:[1,0,1] neg_lo:[0,0,1] neg_hi:[0,0,1]\n"
"v_pk_fma_f32 v[124:125], v[124:125], v[230:231], v[230:231] op_sel:[0,0,1] op_sel_hi:[1,0,1] neg_lo:[0,0,1] neg_hi:[0,0,1]\n"
"v_pk_fma_f32 v[126:127], v[126:127], v[230:231], v[230:231] op_sel:[0,0,1] op_sel_hi:[1,0,1] neg_lo:[0,0,1] neg_hi:[0,0,1]\n"
"s_cmp_lg_u64 vcc, 0\n"
"s_addc_u32 s55, s55, s55\n"
"v_cvt_pk_bf16_f32 v96, v96, v97\n"
"v_cvt_pk_bf16_f32 v97, v98, v99\n"
"v_cvt_pk_bf16_f32 v98, v100, v101\n"
"v_cvt_pk_bf16_f32 v99, v102, v103\n"
"v_cvt_pk_bf16_f32 v100, v104, v105\n"
"v_cvt_pk_bf16_f32 v101, v106, v107\n"
"v_cvt_pk_bf16_f32 v102, v108, v109\n"
"v_cvt_pk_bf16_f32 v103, v110, v111\n"
"v_cvt_pk_bf16_f32 v104, v112, v113\n"
"v_cvt_pk_bf16_f32 v105, v114, v115\n"
"v_cvt_pk_bf16_f32 v106, v116, v117\n"
"v_cvt_pk_bf16_f32 v107, v118, v119\n"
"v_cvt_pk_bf16_f32 v108, v120, v121\n"
"v_cvt_pk_bf16_f32 v109, v122, v123\n"
"v_cvt_pk_bf16_f32 v110, v124, v125\n"
"v_cvt_pk_bf16_f32 v111, v126, v127\n"
"v_mfma_f32_16x16x32_bf16 v[200:203], a[128:131], a[64:67], v[232:235]\n"
"ds_write_b128 v252, v[96:99] offset:0\n"
"buffer_store_dwordx4 v[76:79], v255, s[48:51], s68 offen offset:3072 sc1\n"
"v_add_f32 v244, v244, v196\n"
"v_mfma_f32_16x16x32_bf16 v[200:203], a[132:135], a[68:71], v[200:203]\n"
"s_add_u32 s68, s68, 4096\n"
"v_exp_f32 v197, v197\n"
"v_mfma_f32_16x16x32_bf16 v[200:203], a[136:139], a[72:75], v[200:203]\n"
"ds_write_b128 v252, v[100:103] offset:64\n"
"v_add_f32 v244, v244, v197\n"
"v_cvt_pk_bf16_f32 v210, v196, v197\n"
"v_mfma_f32_16x16x32_bf16 v[200:203], a[140:143], a[76:79], v[200:203]\n"
"v_exp_f32 v198, v198\n"
"v_mfma_f32_16x16x32_bf16 v[204:207], a[144:147], a[64:67], v[232:235]\n"
"ds_write_b128 v252, v[104:107] offset:128\n"
"v_add_f32 v244, v244, v198\n"
"v_mfma_f32_16x16x32_bf16 v[204:207], a[148:151], a[68:71], v[204:207]\n"
"v_exp_f32 v199, v199\n"
"v_mfma_f32_16x16x32_bf16 v[204:207], a[152:155], a[72:75], v[204:207]\n"
"ds_write_b128 v252, v[108:111] offset:192\n"
"v_add_f32 v244, v244, v199\n"
"v_cvt_pk_bf16_f32 v211, v198, v199\n"
"v_mfma_f32_16x16x32_bf16 v[204:207], a[156:159], a[76:79], v[204:207]\n"
"v_exp_f32 v200, v200\n"
"v_mfma_f32_16x16x32_bf16 v[128:131], a[160:163], v[208:211], v[128:131]\n"
"ds_read2_b64 v[96:99], v253 offset0:0 offset1:2\n"
"v_add_f32 v244, v244, v200\n"
"v_mfma_f32_16x16x32_bf16 v[132:135], a[164:167], v[208:211], v[132:135]\n"
"ds_read2_b64 v[100:103], v253 offset0:136 offset1:138\n"
"v_exp_f32 v201, v201\n"
"v_mfma_f32_16x16x32_bf16 v[136:139], a[168:171], v[208:211], v[136:139]\n"
"ds_read2_b64 v[104:107], v249 offset0:0 offset1:2\n"
"v_add_f32 v244, v244, v201\n"
"v_cvt_pk_bf16_f32 v212, v200, v201\n"
"v_mfma_f32_16x16x32_bf16 v[140:143], a[172:175], v[208:211], v[140:143]\n"
"ds_read2_b64 v[108:111], v249 offset0:136 offset1:138\n"
"v_exp_f32 v202, v202\n"
"v_mfma_f32_16x16x32_bf16 v[144:147], a[176:179], v[208:211], v[144:147]\n"
"v_add_f32 v244, v244, v202\n"
"v_mfma_f32_16x16x32_bf16 v[148:151], a[180:183], v[208:211], v[148:151]\n"
"v_exp_f32 v203, v203\n"
"v_mfma_f32_16x16x32_bf16 v[152:155], a[184:187], v[208:211], v[152:155]\n"
"v_add_f32 v244, v244, v203\n"
"v_cvt_pk_bf16_f32 v213, v202, v203\n"
"v_mfma_f32_16x16x32_bf16 v[156:159], a[188:191], v[208:211], v[156:159]\n"
"v_exp_f32 v204, v204\n"
"v_mfma_f32_16x16x32_bf16 v[192:195], a[96:99], a[80:83], v[236:239]\n"
"v_add_f32 v244, v244, v204\n"
"v_mfma_f32_16x16x32_bf16 v[192:195], a[100:103], a[84:87], v[192:195]\n"
"v_exp_f32 v205, v205\n"
"v_mfma_f32_16x16x32_bf16 v[192:195], a[104:107], a[88:91], v[192:195]\n"
"v_add_f32 v244, v244, v205\n"
"v_cvt_pk_bf16_f32 v214, v204, v205\n"
"v_mfma_f32_16x16x32_bf16 v[192:195], a[108:111], a[92:95], v[192:195]\n"
"s_waitcnt lgkmcnt(0)\n"
"v_exp_f32 v206, v206\n"
"v_mfma_f32_16x16x32_bf16 v[196:199], a[112:115], a[80:83], v[236:239]\n"
"buffer_store_dwordx4 v[96:99], v255, s[48:51], s68 offen sc1\n"
"v_add_f32 v244, v244, v206\n"
"v_mfma_f32_16x16x32_bf16 v[196:199], a[116:119], a[84:87], v[196:199]\n"
"v_exp_f32 v207, v207\n"
"v_mfma_f32_16x16x32_bf16 v[196:199], a[120:123], a[88:91], v[196:199]\n"
"v_add_f32 v244, v244, v207\n"
"v_cvt_pk_bf16_f32 v215, v206, v207\n"
"v_mfma_f32_16x16x32_bf16 v[196:199], a[124:127], a[92:95], v[196:199]\n"
"v_exp_f32 v192, v192\n"
"v_mfma_f32_16x16x32_bf16 v[128:131], a[192:195], v[212:215], v[128:131]\n"
"buffer_store_dwordx4 v[100:103], v255, s[48:51], s68 offen offset:1024 sc1\n"
"v_add_f32 v245, v245, v192\n"
"v_mfma_f32_16x16x32_bf16 v[132:135], a[196:199], v[212:215], v[132:135]\n"
"v_exp_f32 v193, v193\n"
"v_mfma_f32_16x16x32_bf16 v[136:139], a[200:203], v[212:215], v[136:139]\n"
"v_add_f32 v245, v245, v193\n"
"v_cvt_pk_bf16_f32 v208, v192, v193\n"
"v_mfma_f32_16x16x32_bf16 v[140:143], a[204:207], v[212:215], v[140:143]\n"
"v_exp_f32 v194, v194\n"
"v_mfma_f32_16x16x32_bf16 v[144:147], a[208:211], v[212:215], v[144:147]\n"
"buffer_store_dwordx4 v[104:107], v255, s[48:51], s68 offen offset:2048 sc1\n"
"v_add_f32 v245, v245, v194\n"
"v_mfma_f32_16x16x32_bf16 v[148:151], a[212:215], v[212:215], v[148:151]\n"
"v_exp_f32 v195, v195\n"
"v_mfma_f32_16x16x32_bf16 v[152:155], a[216:219], v[212:215], v[152:155]\n"
"v_add_f32 v245, v245, v195\n"
"v_cvt_pk_bf16_f32 v209, v194, v195\n"
"v_mfma_f32_16x16x32_bf16 v[156:159], a[220:223], v[212:215], v[156:159]\n"
"v_exp_f32 v196, v196\n"
"v_mfma_f32_16x16x4_f32 v[232:235], v250, v244, 0\n"
"s_nop 9\n"
"v_rcp_f32 v234, v232\n"
"v_med3_f32 v233, v232, v251, s73\n"
"v_cmp_neq_f32 vcc, v233, v232\n"
"v_mul_f32 v235, s99, v234\n"
"v_pk_fma_f32 v[128:129], v[128:129], v[234:235], v[234:235] op_sel:[0,0,1] op_sel_hi:[1,0,1] neg_lo:[0,0,1] neg_hi:[0,0,1]\n"
"v_pk_fma_f32 v[130:131], v[130:131], v[234:235], v[234:235] op_sel:[0,0,1] op_sel_hi:[1,0,1] neg_lo:[0,0,1] neg_hi:[0,0,1]\n"
"v_pk_fma_f32 v[132:133], v[132:133], v[234:235], v[234:235] op_sel:[0,0,1] op_sel_hi:[1,0,1] neg_lo:[0,0,1] neg_hi:[0,0,1]\n"
"v_pk_fma_f32 v[134:135], v[134:135], v[234:235], v[234:235] op_sel:[0,0,1] op_sel_hi:[1,0,1] neg_lo:[0,0,1] neg_hi:[0,0,1]\n"
"v_pk_fma_f32 v[136:137], v[136:137], v[234:235], v[234:235] op_sel:[0,0,1] op_sel_hi:[1,0,1] neg_lo:[0,0,1] neg_hi:[0,0,1]\n"
"v_pk_fma_f32 v[138:139], v[138:139], v[234:235], v[234:235] op_sel:[0,0,1] op_sel_hi:[1,0,1] neg_lo:[0,0,1] neg_hi:[0,0,1]\n"
"v_pk_fma_f32 v[140:141], v[140:141], v[234:235], v[234:235] op_sel:[0,0,1] op_sel_hi:[1,0,1] neg_lo:[0,0,1] neg_hi:[0,0,1]\n"
"v_pk_fma_f32 v[142:143], v[142:143], v[234:235], v[234:235] op_sel:[0,0,1] op_sel_hi:[1,0,1] neg_lo:[0,0,1] neg_hi:[0,0,1]\n"
"v_pk_fma_f32 v[144:145], v[144:145], v[234:235], v[234:235] op_sel:[0,0,1] op_sel_hi:[1,0,1] neg_lo:[0,0,1] neg_hi:[0,0,1]\n"
"v_pk_fma_f32 v[146:147], v[146:147], v[234:235], v[234:235] op_sel:[0,0,1] op_sel_hi:[1,0,1] neg_lo:[0,0,1] neg_hi:[0,0,1]\n"
"v_pk_fma_f32 v[148:149], v[148:149], v[234:235], v[234:235] op_sel:[0,0,1] op_sel_hi:[1,0,1] neg_lo:[0,0,1] neg_hi:[0,0,1]\n"
"v_pk_fma_f32 v[150:151], v[150:151], v[234:235], v[234:235] op_sel:[0,0,1] op_sel_hi:[1,0,1] neg_lo:[0,0,1] neg_hi:[0,0,1]\n"
"v_pk_fma_f32 v[152:153], v[152:153], v[234:235], v[234:235] op_sel:[0,0,1] op_sel_hi:[1,0,1] neg_lo:[0,0,1] neg_hi:[0,0,1]\n"
"v_pk_fma_f32 v[154:155], v[154:155], v[234:235], v[234:235] op_sel:[0,0,1] op_sel_hi:[1,0,1] neg_lo:[0,0,1] neg_hi:[0,0,1]\n"
"v_pk_fma_f32 v[156:157], v[156:157], v[234:235], v[234:235] op_sel:[0,0,1] op_sel_hi:[1,0,1] neg_lo:[0,0,1] neg_hi:[0,0,1]\n"
"v_pk_fma_f32 v[158:159], v[158:159], v[234:235], v[234:235] op_sel:[0,0,1] op_sel_hi:[1,0,1] neg_lo:[0,0,1] neg_hi:[0,0,1]\n"
"s_cmp_lg_u64 vcc, 0\n"
"s_addc_u32 s55, s55, s55\n"
"v_cvt_pk_bf16_f32 v128, v128, v129\n"
"v_cvt_pk_bf16_f32 v129, v130, v131\n"
"v_cvt_pk_bf16_f32 v130, v132, v133\n"
"v_cvt_pk_bf16_f32 v131, v134, v135\n"
"v_cvt_pk_bf16_f32 v132, v136, v137\n"
"v_cvt_pk_bf16_f32 v133, v138, v139\n"
"v_cvt_pk_bf16_f32 v134, v140, v141\n"
"v_cvt_pk_bf16_f32 v135, v142, v143\n"
"v_cvt_pk_bf16_f32 v136, v144, v145\n"
"v_cvt_pk_bf16_f32 v137, v146, v147\n"
"v_cvt_pk_bf16_f32 v138, v148, v149\n"
"v_cvt_pk_bf16_f32 v139, v150, v151\n"
"v_cvt_pk_bf16_f32 v140, v152, v153\n"
"v_cvt_pk_bf16_f32 v141, v154, v155\n"
"v_cvt_pk_bf16_f32 v142, v156, v157\n"
"v_cvt_pk_bf16_f32 v143, v158, v159\n"
"v_mfma_f32_16x16x32_bf16 v[200:203], a[128:131], a[80:83], v[236:239]\n"
"ds_write_b128 v252, v[128:131] offset:0\n"
"buffer_store_dwordx4 v[108:111], v255, s[48:51], s68 offen offset:3072 sc1\n"
"v_add_f32 v245, v245, v196\n"
"v_mfma_f32_16x16x32_bf16 v[200:203], a[132:135], a[84:87], v[200:203]\n"
"s_add_u32 s68, s68, 4096\n"
"v_exp_f32 v197, v197\n"
"v_mfma_f32_16x16x32_bf16 v[200:203], a[136:139], a[88:91], v[200:203]\n"
"ds_write_b128 v252, v[132:135] offset:64\n"
"v_add_f32 v245, v245, v197\n"
"v_cvt_pk_bf16_f32 v210, v196, v197\n"
"v_mfma_f32_16x16x32_bf16 v[200:203], a[140:143], a[92:95], v[200:203]\n"
"v_exp_f32 v198, v198\n"
"v_mfma_f32_16x16x32_bf16 v[204:207], a[144:147], a[80:83], v[236:239]\n"
"ds_write_b128 v252, v[136:139] offset:128\n"
"v_add_f32 v245, v245, v198\n"
"v_mfma_f32_16x16x32_bf16 v[204:207], a[148:151], a[84:87], v[204:207]\n"
"v_exp_f32 v199, v199\n"
"v_mfma_f32_16x16x32_bf16 v[204:207], a[152:155], a[88:91], v[204:207]\n"
"ds_write_b128 v252, v[140:143] offset:192\n"
"v_add_f32 v245, v245, v199\n"
"v_cvt_pk_bf16_f32 v211, v198, v199\n"
"v_mfma_f32_16x16x32_bf16 v[204:207], a[156:159], a[92:95], v[204:207]\n"
"v_exp_f32 v200, v200\n"
"v_mfma_f32_16x16x32_bf16 v[160:163], a[160:163], v[208:211], v[160:163]\n"
"ds_read2_b64 v[128:131], v253 offset0:0 offset1:2\n"
"v_add_f32 v245, v245, v200\n"
"v_mfma_f32_16x16x32_bf16 v[164:167], a[164:167], v[208:211], v[164:167]\n"
"ds_read2_b64 v[132:135], v253 offset0:136 offset1:138\n"
"v_exp_f32 v201, v201\n"
"v_mfma_f32_16x16x32_bf16 v[168:171], a[168:171], v[208:211], v[168:171]\n"
"ds_read2_b64 v[136:139], v249 offset0:0 offset1:2\n"
"v_add_f32 v245, v245, v201\n"
"v_cvt_pk_bf16_f32 v212, v200, v201\n"
"v_mfma_f32_16x16x32_bf16 v[172:175], a[172:175], v[208:211], v[172:175]\n"
"ds_read2_b64 v[140:143], v249 offset0:136 offset1:138\n"
"v_exp_f32 v202, v202\n"
"v_mfma_f32_16x16x32_bf16 v[176:179], a[176:179], v[208:211], v[176:179]\n"
"v_add_f32 v245, v245, v202\n"
"v_mfma_f32_16x16x32_bf16 v[180:183], a[180:183], v[208:211], v[180:183]\n"
"v_exp_f32 v203, v203\n"
"v_mfma_f32_16x16x32_bf16 v[184:187], a[184:187], v[208:211], v[184:187]\n"
"v_add_f32 v245, v245, v203\n"
"v_cvt_pk_bf16_f32 v213, v202, v203\n"
"v_mfma_f32_16x16x32_bf16 v[188:191], a[188:191], v[208:211], v[188:191]\n"
"v_exp_f32 v204, v204\n"
"s_nop 0\n"
"v_add_f32 v245, v245, v204\n"
"v_exp_f32 v205, v205\n"
"s_nop 0\n"
"v_add_f32 v245, v245, v205\n"
"v_cvt_pk_bf16_f32 v214, v204, v205\n"
"v_exp_f32 v206, v206\n"
"s_nop 0\n"
"v_add_f32 v245, v245, v206\n"
"v_exp_f32 v207, v207\n"
"s_nop 0\n"
"v_add_f32 v245, v245, v207\n"
"v_cvt_pk_bf16_f32 v215, v206, v207\n"
"s_waitcnt lgkmcnt(0)\n"
"v_mfma_f32_16x16x4_f32 v[236:239], v250, v245, 0\n"
"v_mfma_f32_16x16x32_bf16 v[160:163], a[192:195], v[212:215], v[160:163]\n"
"buffer_store_dwordx4 v[128:131], v255, s[48:51], s68 offen sc1\n"
"v_mfma_f32_16x16x32_bf16 v[164:167], a[196:199], v[212:215], v[164:167]\n"
"v_mfma_f32_16x16x32_bf16 v[168:171], a[200:203], v[212:215], v[168:171]\n"
"buffer_store_dwordx4 v[132:135], v255, s[48:51], s68 offen offset:1024 sc1\n"
"v_mfma_f32_16x16x32_bf16 v[172:175], a[204:207], v[212:215], v[172:175]\n"
"v_mfma_f32_16x16x32_bf16 v[176:179], a[208:211], v[212:215], v[176:179]\n"
"buffer_store_dwordx4 v[136:139], v255, s[48:51], s68 offen offset:2048 sc1\n"
"v_mfma_f32_16x16x32_bf16 v[180:183], a[212:215], v[212:215], v[180:183]\n"
"v_mfma_f32_16x16x32_bf16 v[184:187], a[216:219], v[212:215], v[184:187]\n"
"buffer_store_dwordx4 v[140:143], v255, s[48:51], s68 offen offset:3072 sc1\n"
"v_mfma_f32_16x16x32_bf16 v[188:191], a[220:223], v[212:215], v[188:191]\n"
"s_add_u32 s68, s68, 4096\n"
"v_rcp_f32 v238, v236\n"
"v_med3_f32 v237, v236, v251, s73\n"
"v_cmp_neq_f32 vcc, v237, v236\n"
"v_mul_f32 v239, s99, v238\n"
"v_pk_fma_f32 v[160:161], v[160:161], v[238:239], v[238:239] op_sel:[0,0,1] op_sel_hi:[1,0,1] neg_lo:[0,0,1] neg_hi:[0,0,1]\n"
"v_pk_fma_f32 v[162:163], v[162:163], v[238:239], v[238:239] op_sel:[0,0,1] op_sel_hi:[1,0,1] neg_lo:[0,0,1] neg_hi:[0,0,1]\n"
"v_pk_fma_f32 v[164:165], v[164:165], v[238:239], v[238:239] op_sel:[0,0,1] op_sel_hi:[1,0,1] neg_lo:[0,0,1] neg_hi:[0,0,1]\n"
"v_pk_fma_f32 v[166:167], v[166:167], v[238:239], v[238:239] op_sel:[0,0,1] op_sel_hi:[1,0,1] neg_lo:[0,0,1] neg_hi:[0,0,1]\n"
"v_pk_fma_f32 v[168:169], v[168:169], v[238:239], v[238:239] op_sel:[0,0,1] op_sel_hi:[1,0,1] neg_lo:[0,0,1] neg_hi:[0,0,1]\n"
"v_pk_fma_f32 v[170:171], v[170:171], v[238:239], v[238:239] op_sel:[0,0,1] op_sel_hi:[1,0,1] neg_lo:[0,0,1] neg_hi:[0,0,1]\n"
"v_pk_fma_f32 v[172:173], v[172:173], v[238:239], v[238:239] op_sel:[0,0,1] op_sel_hi:[1,0,1] neg_lo:[0,0,1] neg_hi:[0,0,1]\n"
"v_pk_fma_f32 v[174:175], v[174:175], v[238:239], v[238:239] op_sel:[0,0,1] op_sel_hi:[1,0,1] neg_lo:[0,0,1] neg_hi:[0,0,1]\n"
"v_pk_fma_f32 v[176:177], v[176:177], v[238:239], v[238:239] op_sel:[0,0,1] op_sel_hi:[1,0,1] neg_lo:[0,0,1] neg_hi:[0,0,1]\n"
"v_pk_fma_f32 v[178:179], v[178:179], v[238:239], v[238:239] op_sel:[0,0,1] op_sel_hi:[1,0,1] neg_lo:[0,0,1] neg_hi:[0,0,1]\n"
"v_pk_fma_f32 v[180:181], v[180:181], v[238:239], v[238:239] op_sel:[0,0,1] op_sel_hi:[1,0,1] neg_lo:[0,0,1] neg_hi:[0,0,1]\n"
"v_pk_fma_f32 v[182:183], v[182:183], v[238:239], v[238:239] op_sel:[0,0,1] op_sel_hi:[1,0,1] neg_lo:[0,0,1] neg_hi:[0,0,1]\n"
"v_pk_fma_f32 v[184:185], v[184:185], v[238:239], v[238:239] op_sel:[0,0,1] op_sel_hi:[1,0,1] neg_lo:[0,0,1] neg_hi:[0,0,1]\n"
"v_pk_fma_f32 v[186:187], v[186:187], v[238:239], v[238:239] op_sel:[0,0,1] op_sel_hi:[1,0,1] neg_lo:[0,0,1] neg_hi:[0,0,1]\n"
"v_pk_fma_f32 v[188:189], v[188:189], v[238:239], v[238:239] op_sel:[0,0,1] op_sel_hi:[1,0,1] neg_lo:[0,0,1] neg_hi:[0,0,1]\n"
"v_pk_fma_f32 v[190:191], v[190:191], v[238:239], v[238:239] op_sel:[0,0,1] op_sel_hi:[1,0,1] neg_lo:[0,0,1] neg_hi:[0,0,1]\n"
"s_cmp_lg_u64 vcc, 0\n"
"s_addc_u32 s55, s55, s55\n"
"v_cvt_pk_bf16_f32 v160, v160, v161\n"
"v_cvt_pk_bf16_f32 v161, v162, v163\n"
"v_cvt_pk_bf16_f32 v162, v164, v165\n"
"v_cvt_pk_bf16_f32 v163, v166, v167\n"
"ds_write_b128 v252, v[160:163] offset:0\n"
"v_cvt_pk_bf16_f32 v164, v168, v169\n"
"v_cvt_pk_bf16_f32 v165, v170, v171\n"
"v_cvt_pk_bf16_f32 v166, v172, v173\n"
"v_cvt_pk_bf16_f32 v167, v174, v175\n"
"ds_write_b128 v252, v[164:167] offset:64\n"
"v_cvt_pk_bf16_f32 v168, v176, v177\n"
"v_cvt_pk_bf16_f32 v169, v178, v179\n"
"v_cvt_pk_bf16_f32 v170, v180, v181\n"
"v_cvt_pk_bf16_f32 v171, v182, v183\n"
"ds_write_b128 v252, v[168:171] offset:128\n"
"v_cvt_pk_bf16_f32 v172, v184, v185\n"
"v_cvt_pk_bf16_f32 v173, v186, v187\n"
"v_cvt_pk_bf16_f32 v174, v188, v189\n"
"v_cvt_pk_bf16_f32 v175, v190, v191\n"
"ds_write_b128 v252, v[172:175] offset:192\n"
"ds_read2_b64 v[160:163], v253 offset0:0 offset1:2\n"
"ds_read2_b64 v[164:167], v253 offset0:136 offset1:138\n"
"ds_read2_b64 v[168:171], v249 offset0:0 offset1:2\n"
"ds_read2_b64 v[172:175], v249 offset0:136 offset1:138\n"
"s_waitcnt lgkmcnt(0)\n"
"buffer_store_dwordx4 v[160:163], v255, s[48:51], s68 offen sc1\n"
"buffer_store_dwordx4 v[164:167], v255, s[48:51], s68 offen offset:1024 sc1\n"
"buffer_store_dwordx4 v[168:171], v255, s[48:51], s68 offen offset:2048 sc1\n"
"buffer_store_dwordx4 v[172:175], v255, s[48:51], s68 offen offset:3072 sc1\n"
"s_add_u32 s68, s68, 4096\n"
"s_branch Lend%=\n"
".fill 14, 4, 0xbf800000\n"
"Lhop3%=:\n"
"s_cbranch_execz Lhop7%=\n"
"LSEAMA%=:\n"
"v_exp_f32 v196, v196\n"
"v_mfma_f32_16x16x32_bf16 v[200:203], a[96:99], a[16:19], v[220:223]\n"
"ds_read_b128 a[128:131], v246 offset:24576\n"
"v_add_f32 v240, v240, v196\n"
"v_mfma_f32_16x16x32_bf16 v[200:203], a[100:103], a[20:23], v[200:203]\n"
"v_exp_f32 v197, v197\n"
"v_mfma_f32_16x16x32_bf16 v[200:203], a[104:107], a[24:27], v[200:203]\n"
"v_add_f32 v240, v240, v197\n"
"v_cvt_pk_bf16_f32 v210, v196, v197\n"
"v_mfma_f32_16x16x32_bf16 v[200:203], a[108:111], a[28:31], v[200:203]\n"
"v_exp_f32 v198, v198\n"
"v_mfma_f32_16x16x32_bf16 v[204:207], a[112:115], a[16:19], v[220:223]\n"
"ds_read_b128 a[132:135], v246 offset:25088\n"
"v_add_f32 v240, v240, v198\n"
"v_mfma_f32_16x16x32_bf16 v[204:207], a[116:119], a[20:23], v[204:207]\n"
"s_waitcnt lgkmcnt(2)\n"
"v_exp_f32 v199, v199\n"
"v_mfma_f32_16x16x32_bf16 v[204:207], a[120:123], a[24:27], v[204:207]\n"
"v_add_f32 v240, v240, v199\n"
"v_cvt_pk_bf16_f32 v211, v198, v199\n"
"v_mfma_f32_16x16x32_bf16 v[204:207], a[124:127], a[28:31], v[204:207]\n"
"s_barrier\n"
"v_exp_f32 v200, v200\n"
"v_mfma_f32_16x16x32_bf16 v[0:3], a[160:163], v[208:211], v[0:3]\n"
"ds_read_b128 a[136:139], v246 offset:25600\n"
"v_add_f32 v241, v241, v200\n"
"v_mfma_f32_16x16x32_bf16 v[4:7], a[164:167], v[208:211], v[4:7]\n"
"v_exp_f32 v201, v201\n"
"v_mfma_f32_16x16x32_bf16 v[8:11], a[168:171], v[208:211], v[8:11]\n"
"v_add_f32 v241, v241, v201\n"
"v_cvt_pk_bf16_f32 v212, v200, v201\n"
"v_mfma_f32_16x16x32_bf16 v[12:15], a[172:175], v[208:211], v[12:15]\n"
"v_exp_f32 v202, v202\n"
"v_mfma_f32_16x16x32_bf16 v[16:19], a[176:179], v[208:211], v[16:19]\n"
"ds_read_b128 a[140:143], v246 offset:26112\n"
"v_add_f32 v241, v241, v202\n"
"v_mfma_f32_16x16x32_bf16 v[20:23], a[180:183], v[208:211], v[20:23]\n"
"v_exp_f32 v203, v203\n"
"v_mfma_f32_16x16x32_bf16 v[24:27], a[184:187], v[208:211], v[24:27]\n"
"v_add_f32 v241, v241, v203\n"
"v_cvt_pk_bf16_f32 v213, v202, v203\n"
"v_mfma_f32_16x16x32_bf16 v[28:31], a[188:191], v[208:211], v[28:31]\n"
"v_exp_f32 v204, v204\n"
"v_mfma_f32_16x16x4_f32 v[216:219], v250, v240, 0\n"
"ds_read_b128 a[0:3], v254 offset:0\n"
"ds_read_b128 a[4:7], v254 offset:512\n"
"ds_read_b128 a[8:11], v254 offset:1024\n"
"ds_read_b128 a[12:15], v254 offset:1536\n"
"ds_read_b128 a[16:19], v254 offset:4096\n"
"ds_read_b128 a[20:23], v254 offset:4608\n"
"ds_read_b128 a[24:27], v254 offset:5120\n"
"ds_read_b128 a[28:31], v254 offset:5632\n"
"ds_read_b128 a[240:243], v249 offset:24576\n"
"ds_read_b128 a[244:247], v249 offset:25088\n"
"ds_read_b128 a[248:251], v249 offset:25600\n"
"ds_read_b128 a[252:255], v249 offset:26112\n"
"v_add_u32 v249, 2176, v253\n"
"v_rcp_f32 v218, v216\n"
"v_med3_f32 v217, v216, v251, s63\n"
"v_cmp_neq_f32 vcc, v217, v216\n"
"v_mul_f32 v219, s98, v218\n"
"v_pk_fma_f32 v[0:1], v[0:1], v[218:219], v[218:219] op_sel:[0,0,1] op_sel_hi:[1,0,1] neg_lo:[0,0,1] neg_hi:[0,0,1]\n"
"v_pk_fma_f32 v[2:3], v[2:3], v[218:219], v[218:219] op_sel:[0,0,1] op_sel_hi:[1,0,1] neg_lo:[0,0,1] neg_hi:[0,0,1]\n"
"v_pk_fma_f32 v[4:5], v[4:5], v[218:219], v[218:219] op_sel:[0,0,1] op_sel_hi:[1,0,1] neg_lo:[0,0,1] neg_hi:[0,0,1]\n"
"v_pk_fma_f32 v[6:7], v[6:7], v[218:219], v[218:219] op_sel:[0,0,1] op_sel_hi:[1,0,1] neg_lo:[0,0,1] neg_hi:[0,0,1]\n"
"v_pk_fma_f32 v[8:9], v[8:9], v[218:219], v[218:219] op_sel:[0,0,1] op_sel_hi:[1,0,1] neg_lo:[0,0,1] neg_hi:[0,0,1]\n"
"v_pk_fma_f32 v[10:11], v[10:11], v[218:219], v[218:219] op_sel:[0,0,1] op_sel_hi:[1,0,1] neg_lo:[0,0,1] neg_hi:[0,0,1]\n"
"v_pk_fma_f32 v[12:13], v[12:13], v[218:219], v[218:219] op_sel:[0,0,1] op_sel_hi:[1,0,1] neg_lo:[0,0,1] neg_hi:[0,0,1]\n"
"v_pk_fma_f32 v[14:15], v[14:15], v[218:219], v[218:219] op_sel:[0,0,1] op_sel_hi:[1,0,1] neg_lo:[0,0,1] neg_hi:[0,0,1]\n"
"v_pk_fma_f32 v[16:17], v[16:17], v[218:219], v[218:219] op_sel:[0,0,1] op_sel_hi:[1,0,1] neg_lo:[0,0,1] neg_hi:[0,0,1]\n"
"v_pk_fma_f32 v[18:19], v[18:19], v[218:219], v[218:219] op_sel:[0,0,1] op_sel_hi:[1,0,1] neg_lo:[0,0,1] neg_hi:[0,0,1]\n"
"v_pk_fma_f32 v[20:21], v[20:21], v[218:219], v[218:219] op_sel:[0,0,1] op_sel_hi:[1,0,1] neg_lo:[0,0,1] neg_hi:[0,0,1]\n"
"v_pk_fma_f32 v[22:23], v[22:23], v[218:219], v[218:219] op_sel:[0,0,1] op_sel_hi:[1,0,1] neg_lo:[0,0,1] neg_hi:[0,0,1]\n"
"v_pk_fma_f32 v[24:25], v[24:25], v[218:219], v[218:219] op_sel:[0,0,1] op_sel_hi:[1,0,1] neg_lo:[0,0,1] neg_hi:[0,0,1]\n"
"v_pk_fma_f32 v[26:27], v[26:27], v[218:219], v[218:219] op_sel:[0,0,1] op_sel_hi:[1,0,1] neg_lo:[0,0,1] neg_hi:[0,0,1]\n"
"v_pk_fma_f32 v[28:29], v[28:29], v[218:219], v[218:219] op_sel:[0,0,1] op_sel_hi:[1,0,1] neg_lo:[0,0,1] neg_hi:[0,0,1]\n"
"v_pk_fma_f32 v[30:31], v[30:31], v[218:219], v[218:219] op_sel:[0,0,1] op_sel_hi:[1,0,1] neg_lo:[0,0,1] neg_hi:[0,0,1]\n"
"s_cmp_lg_u64 vcc, 0\n"
"s_addc_u32 s55, s55, s55\n"
"v_cvt_pk_bf16_f32 v0, v0, v1\n"
"v_cvt_pk_bf16_f32 v1, v2, v3\n"
"v_cvt_pk_bf16_f32 v2, v4, v5\n"
"v_cvt_pk_bf16_f32 v3, v6, v7\n"
"ds_write_b128 v252, v[0:3] offset:0\n"
"v_cvt_pk_bf16_f32 v4, v8, v9\n"
"v_cvt_pk_bf16_f32 v5, v10, v11\n"
"v_cvt_pk_bf16_f32 v6, v12, v13\n"
"v_cvt_pk_bf16_f32 v7, v14, v15\n"
"ds_write_b128 v252, v[4:7] offset:64\n"
"v_cvt_pk_bf16_f32 v8, v16, v17\n"
"v_cvt_pk_bf16_f32 v9, v18, v19\n"
"v_cvt_pk_bf16_f32 v10, v20, v21\n"
"v_cvt_pk_bf16_f32 v11, v22, v23\n"
"ds_write_b128 v252, v[8:11] offset:128\n"
"v_cvt_pk_bf16_f32 v12, v24, v25\n"
"v_cvt_pk_bf16_f32 v13, v26, v27\n"
"v_cvt_pk_bf16_f32 v14, v28, v29\n"
"v_cvt_pk_bf16_f32 v15, v30, v31\n"
"ds_write_b128 v252, v[12:15] offset:192\n"
"v_mfma_f32_16x16x32_bf16 v[192:195], a[96:99], a[32:35], v[224:227]\n"
"ds_read_b128 a[144:147], v246 offset:28672\n"
"v_add_f32 v241, v241, v204\n"
"v_mfma_f32_16x16x32_bf16 v[192:195], a[100:103], a[36:39], v[192:195]\n"
"ds_read2_b64 v[0:3], v253 offset0:0 offset1:2\n"
"v_exp_f32 v205, v205\n"
"v_mfma_f32_16x16x32_bf16 v[192:195], a[104:107], a[40:43], v[192:195]\n"
"ds_read2_b64 v[4:7], v253 offset0:136 offset1:138\n"
"v_add_f32 v241, v241, v205\n"
"v_cvt_pk_bf16_f32 v214, v204, v205\n"
"v_mfma_f32_16x16x32_bf16 v[192:195], a[108:111], a[44:47], v[192:195]\n"
"ds_read2_b64 v[8:11], v249 offset0:0 offset1:2\n"
"v_exp_f32 v206, v206\n"
"v_mfma_f32_16x16x32_bf16 v[196:199], a[112:115], a[32:35], v[224:227]\n"
"ds_read2_b64 v[12:15], v249 offset0:136 offset1:138\n"
"ds_read_b128 a[148:151], v246 offset:29184\n"
"v_add_f32 v241, v241, v206\n"
"v_mfma_f32_16x16x32_bf16 v[196:199], a[116:119], a[36:39], v[196:199]\n"
"v_exp_f32 v207, v207\n"
"v_mfma_f32_16x16x32_bf16 v[196:199], a[120:123], a[40:43], v[196:199]\n"
"v_add_f32 v241, v241, v207\n"
"v_cvt_pk_bf16_f32 v215, v206, v207\n"
"v_mfma_f32_16x16x32_bf16 v[196:199], a[124:127], a[44:47], v[196:199]\n"
"v_exp_f32 v192, v192\n"
"v_mfma_f32_16x16x32_bf16 v[32:35], a[160:163], v[212:215], v[32:35]\n"
"ds_read_b128 a[152:155], v246 offset:29696\n"
"v_add_f32 v242, v242, v192\n"
"v_mfma_f32_16x16x32_bf16 v[36:39], a[164:167], v[212:215], v[36:39]\n"
"v_exp_f32 v193, v193\n"
"v_mfma_f32_16x16x32_bf16 v[40:43], a[168:171], v[212:215], v[40:43]\n"
"v_add_f32 v242, v242, v193\n"
"v_cvt_pk_bf16_f32 v208, v192, v193\n"
"v_mfma_f32_16x16x32_bf16 v[44:47], a[172:175], v[212:215], v[44:47]\n"
"v_exp_f32 v194, v194\n"
"v_mfma_f32_16x16x32_bf16 v[48:51], a[176:179], v[212:215], v[48:51]\n"
"ds_read_b128 a[156:159], v246 offset:30208\n"
"v_add_f32 v242, v242, v194\n"
"v_mfma_f32_16x16x32_bf16 v[52:55], a[180:183], v[212:215], v[52:55]\n"
"v_exp_f32 v195, v195\n"
"v_mfma_f32_16x16x32_bf16 v[56:59], a[184:187], v[212:215], v[56:59]\n"
"Lhop4%=:\n"
"s_cbranch_execz Lhop8%=\n"
"v_add_f32 v242, v242, v195\n"
"v_cvt_pk_bf16_f32 v209, v194, v195\n"
"v_mfma_f32_16x16x32_bf16 v[60:63], a[188:191], v[212:215], v[60:63]\n"
"v_exp_f32 v196, v196\n"
"v_mfma_f32_16x16x4_f32 v[220:223], v250, v241, 0\n"
"s_waitcnt lgkmcnt(3)\n"
"buffer_store_dwordx4 v[0:3], v255, s[48:51], s68 offen sc1\n"
"buffer_store_dwordx4 v[4:7], v255, s[48:51], s68 offen offset:1024 sc1\n"
"buffer_store_dwordx4 v[8:11], v255, s[48:51], s68 offen offset:2048 sc1\n"
"buffer_store_dwordx4 v[12:15], v255, s[48:51], s68 offen offset:3072 sc1\n"
"s_add_u32 s68, s68, 4096\n"
"s_nop 3\n"
"v_rcp_f32 v222, v220\n"
"v_med3_f32 v221, v220, v251, s63\n"
"v_cmp_neq_f32 vcc, v221, v220\n"
"v_mul_f32 v223, s98, v222\n"
"v_pk_fma_f32 v[32:33], v[32:33], v[222:223], v[222:223] op_sel:[0,0,1] op_sel_hi:[1,0,1] neg_lo:[0,0,1] neg_hi:[0,0,1]\n"
"v_pk_fma_f32 v[34:35], v[34:35], v[222:223], v[222:223] op_sel:[0,0,1] op_sel_hi:[1,0,1] neg_lo:[0,0,1] neg_hi:[0,0,1]\n"
"v_pk_fma_f32 v[36:37], v[36:37], v[222:223], v[222:223] op_sel:[0,0,1] op_sel_hi:[1,0,1] neg_lo:[0,0,1] neg_hi:[0,0,1]\n"
"v_pk_fma_f32 v[38:39], v[38:39], v[222:223], v[222:223] op_sel:[0,0,1] op_sel_hi:[1,0,1] neg_lo:[0,0,1] neg_hi:[0,0,1]\n"
"v_pk_fma_f32 v[40:41], v[40:41], v[222:223], v[222:223] op_sel:[0,0,1] op_sel_hi:[1,0,1] neg_lo:[0,0,1] neg_hi:[0,0,1]\n"
"v_pk_fma_f32 v[42:43], v[42:43], v[222:223], v[222:223] op_sel:[0,0,1] op_sel_hi:[1,0,1] neg_lo:[0,0,1] neg_hi:[0,0,1]\n"
"v_pk_fma_f32 v[44:45], v[44:45], v[222:223], v[222:223] op_sel:[0,0,1] op_sel_hi:[1,0,1] neg_lo:[0,0,1] neg_hi:[0,0,1]\n"
"v_pk_fma_f32 v[46:47], v[46:47], v[222:223], v[222:223] op_sel:[0,0,1] op_sel_hi:[1,0,1] neg_lo:[0,0,1] neg_hi:[0,0,1]\n"
"v_pk_fma_f32 v[48:49], v[48:49], v[222:223], v[222:223] op_sel:[0,0,1] op_sel_hi:[1,0,1] neg_lo:[0,0,1] neg_hi:[0,0,1]\n"
"v_pk_fma_f32 v[50:51], v[50:51], v[222:223], v[222:223] op_sel:[0,0,1] op_sel_hi:[1,0,1] neg_lo:[0,0,1] neg_hi:[0,0,1]\n"
"v_pk_fma_f32 v[52:53], v[52:53], v[222:223], v[222:223] op_sel:[0,0,1] op_sel_hi:[1,0,1] neg_lo:[0,0,1] neg_hi:[0,0,1]\n"
"v_pk_fma_f32 v[54:55], v[54:55], v[222:223], v[222:223] op_sel:[0,0,1] op_sel_hi:[1,0,1] neg_lo:[0,0,1] neg_hi:[0,0,1]\n"
"v_pk_fma_f32 v[56:57], v[56:57], v[222:223], v[222:223] op_sel:[0,0,1] op_sel_hi:[1,0,1] neg_lo:[0,0,1] neg_hi:[0,0,1]\n"
"v_pk_fma_f32 v[58:59], v[58:59], v[222:223], v[222:223] op_sel:[0,0,1] op_sel_hi:[1,0,1] neg_lo:[0,0,1] neg_hi:[0,0,1]\n"
"v_pk_fma_f32 v[60:61], v[60:61], v[222:223], v[222:223] op_sel:[0,0,1] op_sel_hi:[1,0,1] neg_lo:[0,0,1] neg_hi:[0,0,1]\n"
"v_pk_fma_f32 v[62:63], v[62:63], v[222:223], v[222:223] op_sel:[0,0,1] op_sel_hi:[1,0,1] neg_lo:[0,0,1] neg_hi:[0,0,1]\n"
"s_cmp_lg_u64 vcc, 0\n"
"s_addc_u32 s55, s55, s55\n"
"v_mfma_f32_32x32x16_bf16 v[16:31], a[224:227], a[8:11], 0\n"
"v_cvt_pk_bf16_f32 v32, v32, v33\n"
"v_cvt_pk_bf16_f32 v33, v34, v35\n"
"v_cvt_pk_bf16_f32 v34, v36, v37\n"
"v_cvt_pk_bf16_f32 v35, v38, v39\n"
"v_mfma_f32_32x32x16_bf16 v[16:31], a[228:231], a[8:11], v[16:31]\n"
"ds_write_b128 v252, v[32:35] offset:0\n"
"v_mfma_f32_32x32x16_bf16 v[16:31], a[232:235], a[12:15], v[16:31]\n"
"v_cvt_pk_bf16_f32 v36, v40, v41\n"
"v_cvt_pk_bf16_f32 v37, v42, v43\n"
"v_cvt_pk_bf16_f32 v38, v44, v45\n"
"v_cvt_pk_bf16_f32 v39, v46, v47\n"
"v_mfma_f32_32x32x16_bf16 v[16:31], a[236:239], a[12:15], v[16:31]\n"
"ds_write_b128 v252, v[36:39] offset:64\n"
"v_mfma_f32_32x32x16_bf16 v[0:15], a[224:227], a[0:3], 0\n"
"v_cvt_pk_bf16_f32 v40, v48, v49\n"
"v_cvt_pk_bf16_f32 v41, v50, v51\n"
"v_cvt_pk_bf16_f32 v42, v52, v53\n"
"v_cvt_pk_bf16_f32 v43, v54, v55\n"
"v_mfma_f32_32x32x16_bf16 v[0:15], a[228:231], a[0:3], v[0:15]\n"
"ds_write_b128 v252, v[40:43] offset:128\n"
"v_mfma_f32_32x32x16_bf16 v[0:15], a[232:235], a[4:7], v[0:15]\n"
"v_cvt_pk_bf16_f32 v44, v56, v57\n"
"v_cvt_pk_bf16_f32 v45, v58, v59\n"
"v_cvt_pk_bf16_f32 v46, v60, v61\n"
"v_cvt_pk_bf16_f32 v47, v62, v63\n"
"v_mfma_f32_32x32x16_bf16 v[0:15], a[236:239], a[4:7], v[0:15]\n"
"ds_write_b128 v252, v[44:47] offset:192\n"
"v_mfma_f32_16x16x32_bf16 v[200:203], a[96:99], a[48:51], v[228:231]\n"
"ds_read_b64_tr_b16 a[192:193], v247 offset:57344\n"
"v_add_f32 v242, v242, v196\n"
"v_mfma_f32_16x16x32_bf16 v[200:203], a[100:103], a[52:55], v[200:203]\n"
"v_exp_f32 v197, v197\n"
"v_mfma_f32_16x16x32_bf16 v[200:203], a[104:107], a[56:59], v[200:203]\n"
"v_add_f32 v242, v242, v197\n"
"v_cvt_pk_bf16_f32 v210, v196, v197\n"
"v_mfma_f32_16x16x32_bf16 v[200:203], a[108:111], a[60:63], v[200:203]\n"
"v_exp_f32 v198, v198\n"
"v_mfma_f32_16x16x32_bf16 v[204:207], a[112:115], a[48:51], v[228:231]\n"
"ds_read_b64_tr_b16 a[194:195], v247 offset:61440\n"
"v_add_f32 v242, v242, v198\n"
"v_mfma_f32_16x16x32_bf16 v[204:207], a[116:119], a[52:55], v[204:207]\n"
"v_exp_f32 v199, v199\n"
"v_mfma_f32_16x16x32_bf16 v[204:207], a[120:123], a[56:59], v[204:207]\n"
"v_add_f32 v242, v242, v199\n"
"v_cvt_pk_bf16_f32 v211, v198, v199\n"
"v_mfma_f32_16x16x32_bf16 v[204:207], a[124:127], a[60:63], v[204:207]\n"
"v_exp_f32 v200, v200\n"
"v_mfma_f32_16x16x32_bf16 v[64:67], a[160:163], v[208:211], v[64:67]\n"
"ds_read_b64_tr_b16 a[196:197], v248 offset:57344\n"
"v_add_f32 v243, v243, v200\n"
"v_mfma_f32_16x16x32_bf16 v[68:71], a[164:167], v[208:211], v[68:71]\n"
"v_exp_f32 v201, v201\n"
"v_mfma_f32_16x16x32_bf16 v[72:75], a[168:171], v[208:211], v[72:75]\n"
"v_add_f32 v243, v243, v201\n"
"v_cvt_pk_bf16_f32 v212, v200, v201\n"
"v_mfma_f32_16x16x32_bf16 v[76:79], a[172:175], v[208:211], v[76:79]\n"
"v_exp_f32 v202, v202\n"
"v_mfma_f32_16x16x32_bf16 v[80:83], a[176:179], v[208:211], v[80:83]\n"
"ds_read_b64_tr_b16 a[198:199], v248 offset:61440\n"
"v_add_f32 v243, v243, v202\n"
"v_mfma_f32_16x16x32_bf16 v[84:87], a[180:183], v[208:211], v[84:87]\n"
"v_exp_f32 v203, v203\n"
"v_mfma_f32_16x16x32_bf16 v[88:91], a[184:187], v[208:211], v[88:91]\n"
"v_add_f32 v243, v243, v203\n"
"v_cvt_pk_bf16_f32 v213, v202, v203\n"
"v_mfma_f32_16x16x32_bf16 v[92:95], a[188:191], v[208:211], v[92:95]\n"
"v_exp_f32 v204, v204\n"
"v_mfma_f32_16x16x4_f32 v[224:227], v250, v242, 0\n"
"ds_read_b128 a[32:35], v254 offset:8192\n"
"ds_read_b128 a[36:39], v254 offset:8704\n"
"ds_read_b128 a[40:43], v254 offset:9216\n"
"ds_read_b128 a[44:47], v254 offset:9728\n"
"v_cvt_pk_bf16_f32 v0, v0, v1\n"
"v_cvt_pk_bf16_f32 v2, v2, v3\n"
"v_cvt_pk_bf16_f32 v4, v4, v5\n"
"v_cvt_pk_bf16_f32 v6, v6, v7\n"
"v_accvgpr_write_b32 a0, v0\n"
"v_accvgpr_write_b32 a1, v2\n"
"v_accvgpr_write_b32 a2, v4\n"
"v_accvgpr_write_b32 a3, v6\n"
"v_rcp_f32 v226, v224\n"
"v_med3_f32 v225, v224, v251, s63\n"
"v_cmp_neq_f32 vcc, v225, v224\n"
"v_mul_f32 v227, s98, v226\n"
"v_pk_fma_f32 v[64:65], v[64:65], v[226:227], v[226:227] op_sel:[0,0,1] op_sel_hi:[1,0,1] neg_lo:[0,0,1] neg_hi:[0,0,1]\n"
"v_pk_fma_f32 v[66:67], v[66:67], v[226:227], v[226:227] op_sel:[0,0,1] op_sel_hi:[1,0,1] neg_lo:[0,0,1] neg_hi:[0,0,1]\n"
"v_pk_fma_f32 v[68:69], v[68:69], v[226:227], v[226:227] op_sel:[0,0,1] op_sel_hi:[1,0,1] neg_lo:[0,0,1] neg_hi:[0,0,1]\n"
"v_pk_fma_f32 v[70:71], v[70:71], v[226:227], v[226:227] op_sel:[0,0,1] op_sel_hi:[1,0,1] neg_lo:[0,0,1] neg_hi:[0,0,1]\n"
"v_pk_fma_f32 v[72:73], v[72:73], v[226:227], v[226:227] op_sel:[0,0,1] op_sel_hi:[1,0,1] neg_lo:[0,0,1] neg_hi:[0,0,1]\n"
"v_pk_fma_f32 v[74:75], v[74:75], v[226:227], v[226:227] op_sel:[0,0,1] op_sel_hi:[1,0,1] neg_lo:[0,0,1] neg_hi:[0,0,1]\n"
"v_pk_fma_f32 v[76:77], v[76:77], v[226:227], v[226:227] op_sel:[0,0,1] op_sel_hi:[1,0,1] neg_lo:[0,0,1] neg_hi:[0,0,1]\n"
"v_pk_fma_f32 v[78:79], v[78:79], v[226:227], v[226:227] op_sel:[0,0,1] op_sel_hi:[1,0,1] neg_lo:[0,0,1] neg_hi:[0,0,1]\n"
"v_pk_fma_f32 v[80:81], v[80:81], v[226:227], v[226:227] op_sel:[0,0,1] op_sel_hi:[1,0,1] neg_lo:[0,0,1] neg_hi:[0,0,1]\n"
"v_pk_fma_f32 v[82:83], v[82:83], v[226:227], v[226:227] op_sel:[0,0,1] op_sel_hi:[1,0,1] neg_lo:[0,0,1] neg_hi:[0,0,1]\n"
"v_pk_fma_f32 v[84:85], v[84:85], v[226:227], v[226:227] op_sel:[0,0,1] op_sel_hi:[1,0,1] neg_lo:[0,0,1] neg_hi:[0,0,1]\n"
"v_pk_fma_f32 v[86:87], v[86:87], v[226:227], v[226:227] op_sel:[0,0,1] op_sel_hi:[1,0,1] neg_lo:[0,0,1] neg_hi:[0,0,1]\n"
"v_pk_fma_f32 v[88:89], v[88:89], v[226:227], v[226:227] op_sel:[0,0,1] op_sel_hi:[1,0,1] neg_lo:[0,0,1] neg_hi:[0,0,1]\n"
"v_pk_fma_f32 v[90:91], v[90:91], v[226:227], v[226:227] op_sel:[0,0,1] op_sel_hi:[1,0,1] neg_lo:[0,0,1] neg_hi:[0,0,1]\n"
"v_pk_fma_f32 v[92:93], v[92:93], v[226:227], v[226:227] op_sel:[0,0,1] op_sel_hi:[1,0,1] neg_lo:[0,0,1] neg_hi:[0,0,1]\n"
"v_pk_fma_f32 v[94:95], v[94:95], v[226:227], v[226:227] op_sel:[0,0,1] op_sel_hi:[1,0,1] neg_lo:[0,0,1] neg_hi:[0,0,1]\n"
"s_cmp_lg_u64 vcc, 0\n"
"s_addc_u32 s55, s55, s55\n"
"v_mfma_f32_32x32x16_bf16 v[48:63], a[224:227], a[24:27], 0\n"
"v_cvt_pk_bf16_f32 v64, v64, v65\n"
"v_cvt_pk_bf16_f32 v65, v66, v67\n"
"v_cvt_pk_bf16_f32 v66, v68, v69\n"
"v_cvt_pk_bf16_f32 v67, v70, v71\n"
"v_cvt_pk_bf16_f32 v8, v8, v9\n"
"v_cvt_pk_bf16_f32 v10, v10, v11\n"
"v_mfma_f32_32x32x16_bf16 v[48:63], a[228:231], a[24:27], v[48:63]\n"
"ds_write_b128 v252, v[64:67] offset:4352\n"
"v_cvt_pk_bf16_f32 v12, v12, v13\n"
"v_cvt_pk_bf16_f32 v14, v14, v15\n"
"v_accvgpr_write_b32 a4, v8\n"
"v_accvgpr_write_b32 a5, v10\n"
"v_mfma_f32_32x32x16_bf16 v[48:63], a[232:235], a[28:31], v[48:63]\n"
"v_cvt_pk_bf16_f32 v68, v72, v73\n"
"v_cvt_pk_bf16_f32 v69, v74, v75\n"
"v_cvt_pk_bf16_f32 v70, v76, v77\n"
"v_cvt_pk_bf16_f32 v71, v78, v79\n"
"v_accvgpr_write_b32 a6, v12\n"
"v_accvgpr_write_b32 a7, v14\n"
"v_mfma_f32_32x32x16_bf16 v[48:63], a[236:239], a[28:31], v[48:63]\n"
"ds_write_b128 v252, v[68:71] offset:4416\n"
"v_cvt_pk_bf16_f32 v16, v16, v17\n"
"v_cvt_pk_bf16_f32 v18, v18, v19\n"
"v_cvt_pk_bf16_f32 v20, v20, v21\n"
"v_cvt_pk_bf16_f32 v22, v22, v23\n"
"v_mfma_f32_32x32x16_bf16 v[32:47], a[224:227], a[16:19], 0\n"
"v_cvt_pk_bf16_f32 v72, v80, v81\n"
"v_cvt_pk_bf16_f32 v73, v82, v83\n"
"v_cvt_pk_bf16_f32 v74, v84, v85\n"
"v_cvt_pk_bf16_f32 v75, v86, v87\n"
"v_accvgpr_write_b32 a8, v16\n"
"v_accvgpr_write_b32 a9, v18\n"
"v_mfma_f32_32x32x16_bf16 v[32:47], a[228:231], a[16:19], v[32:47]\n"
"ds_write_b128 v252, v[72:75] offset:4480\n"
"v_accvgpr_write_b32 a10, v20\n"
"v_accvgpr_write_b32 a11, v22\n"
"v_cvt_pk_bf16_f32 v24, v24, v25\n"
"v_cvt_pk_bf16_f32 v26, v26, v27\n"
"v_mfma_f32_32x32x16_bf16 v[32:47], a[232:235], a[20:23], v[32:47]\n"
"v_cvt_pk_bf16_f32 v76, v88, v89\n"
"v_cvt_pk_bf16_f32 v77, v90, v91\n"
"v_cvt_pk_bf16_f32 v78, v92, v93\n"
"v_cvt_pk_bf16_f32 v79, v94, v95\n"
"v_cvt_pk_bf16_f32 v28, v28, v29\n"
"v_cvt_pk_bf16_f32 v30, v30, v31\n"
"v_mfma_f32_32x32x16_bf16 v[32:47], a[236:239], a[20:23], v[32:47]\n"
"ds_write_b128 v252, v[76:79] offset:4544\n"
"v_accvgpr_write_b32 a12, v24\n"
"v_accvgpr_write_b32 a13, v26\n"
"v_accvgpr_write_b32 a14, v28\n"
"v_accvgpr_write_b32 a15, v30\n"
"v_mfma_f32_16x16x32_bf16 v[216:219], a[240:243], a[0:3], 0\n"
"v_mfma_f32_16x16x32_bf16 v[216:219], a[244:247], a[4:7], v[216:219]\n"
"v_mfma_f32_16x16x32_bf16 v[216:219], a[248:251], a[8:11], v[216:219]\n"
"v_mfma_f32_16x16x32_bf16 v[216:219], a[252:255], a[12:15], v[216:219]\n"
"v_mfma_f32_16x16x32_bf16 v[192:195], a[96:99], a[64:67], v[232:235]\n"
"ds_read_b64_tr_b16 a[200:201], v247 offset:57856\n"
"v_add_f32 v243, v243, v204\n"
"v_mfma_f32_16x16x32_bf16 v[192:195], a[100:103], a[68:71], v[192:195]\n"
"v_exp_f32 v205, v205\n"
"v_mfma_f32_16x16x32_bf16 v[192:195], a[104:107], a[72:75], v[192:195]\n"
"v_add_f32 v243, v243, v205\n"
"v_cvt_pk_bf16_f32 v214, v204, v205\n"
"v_mfma_f32_16x16x32_bf16 v[192:195], a[108:111], a[76:79], v[192:195]\n"
"v_exp_f32 v206, v206\n"
"v_mfma_f32_16x16x32_bf16 v[196:199], a[112:115], a[64:67], v[232:235]\n"
"ds_read_b64_tr_b16 a[202:203], v247 offset:61952\n"
"v_add_f32 v243, v243, v206\n"
"v_mfma_f32_16x16x32_bf16 v[196:199], a[116:119], a[68:71], v[196:199]\n"
"v_exp_f32 v207, v207\n"
"v_mfma_f32_16x16x32_bf16 v[196:199], a[120:123], a[72:75], v[196:199]\n"
"v_add_f32 v243, v243, v207\n"
"v_cvt_pk_bf16_f32 v215, v206, v207\n"
"v_mfma_f32_16x16x32_bf16 v[196:199], a[124:127], a[76:79], v[196:199]\n"
"v_exp_f32 v192, v192\n"
"v_mfma_f32_16x16x32_bf16 v[96:99], a[160:163], v[212:215], v[96:99]\n"
"ds_read_b64_tr_b16 a[204:205], v248 offset:57856\n"
"v_add_f32 v244, v244, v192\n"
"v_mfma_f32_16x16x32_bf16 v[100:103], a[164:167], v[212:215], v[100:103]\n"
"v_exp_f32 v193, v193\n"
"v_mfma_f32_16x16x32_bf16 v[104:107], a[168:171], v[212:215], v[104:107]\n"
"v_add_f32 v244, v244, v193\n"
"v_cvt_pk_bf16_f32 v208, v192, v193\n"
"v_mfma_f32_16x16x32_bf16 v[108:111], a[172:175], v[212:215], v[108:111]\n"
"v_exp_f32 v194, v194\n"
"v_mfma_f32_16x16x32_bf16 v[112:115], a[176:179], v[212:215], v[112:115]\n"
"ds_read_b64_tr_b16 a[206:207], v248 offset:61952\n"
"v_add_f32 v244, v244, v194\n"
"v_mfma_f32_16x16x32_bf16 v[116:119], a[180:183], v[212:215], v[116:119]\n"
"v_exp_f32 v195, v195\n"
"v_mfma_f32_16x16x32_bf16 v[120:123], a[184:187], v[212:215], v[120:123]\n"
"v_add_f32 v244, v244, v195\n"
"v_cvt_pk_bf16_f32 v209, v194, v195\n"
"v_mfma_f32_16x16x32_bf16 v[124:127], a[188:191], v[212:215], v[124:127]\n"
"v_exp_f32 v196, v196\n"
"v_mfma_f32_16x16x4_f32 v[228:231], v250, v243, 0\n"
"ds_read_b128 a[48:51], v254 offset:12288\n"
"ds_read_b128 a[52:55], v254 offset:12800\n"
"ds_read_b128 a[56:59], v254 offset:13312\n"
"ds_read_b128 a[60:63], v254 offset:13824\n"
"v_cvt_pk_bf16_f32 v32, v32, v33\n"
"v_cvt_pk_bf16_f32 v34, v34, v35\n"
"v_cvt_pk_bf16_f32 v36, v36, v37\n"
"v_cvt_pk_bf16_f32 v38, v38, v39\n"
"v_accvgpr_write_b32 a16, v32\n"
"v_accvgpr_write_b32 a17, v34\n"
"v_accvgpr_write_b32 a18, v36\n"
"v_accvgpr_write_b32 a19, v38\n"
"v_rcp_f32 v230, v228\n"
"v_med3_f32 v229, v228, v251, s63\n"
"v_cmp_neq_f32 vcc, v229, v228\n"
"v_mul_f32 v231, s98, v230\n"
"v_pk_fma_f32 v[96:97], v[96:97], v[230:231], v[230:231] op_sel:[0,0,1] op_sel_hi:[1,0,1] neg_lo:[0,0,1] neg_hi:[0,0,1]\n"
"v_pk_fma_f32 v[98:99], v[98:99], v[230:231], v[230:231] op_sel:[0,0,1] op_sel_hi:[1,0,1] neg_lo:[0,0,1] neg_hi:[0,0,1]\n"
"v_pk_fma_f32 v[100:101], v[100:101], v[230:231], v[230:231] op_sel:[0,0,1] op_sel_hi:[1,0,1] neg_lo:[0,0,1] neg_hi:[0,0,1]\n"
"v_pk_fma_f32 v[102:103], v[102:103], v[230:231], v[230:231] op_sel:[0,0,1] op_sel_hi:[1,0,1] neg_lo:[0,0,1] neg_hi:[0,0,1]\n"
"v_pk_fma_f32 v[104:105], v[104:105], v[230:231], v[230:231] op_sel:[0,0,1] op_sel_hi:[1,0,1] neg_lo:[0,0,1] neg_hi:[0,0,1]\n"
"v_pk_fma_f32 v[106:107], v[106:107], v[230:231], v[230:231] op_sel:[0,0,1] op_sel_hi:[1,0,1] neg_lo:[0,0,1] neg_hi:[0,0,1]\n"
"v_pk_fma_f32 v[108:109], v[108:109], v[230:231], v[230:231] op_sel:[0,0,1] op_sel_hi:[1,0,1] neg_lo:[0,0,1] neg_hi:[0,0,1]\n"
"v_pk_fma_f32 v[110:111], v[110:111], v[230:231], v[230:231] op_sel:[0,0,1] op_sel_hi:[1,0,1] neg_lo:[0,0,1] neg_hi:[0,0,1]\n"
"v_pk_fma_f32 v[112:113], v[112:113], v[230:231], v[230:231] op_sel:[0,0,1] op_sel_hi:[1,0,1] neg_lo:[0,0,1] neg_hi:[0,0,1]\n"
"v_pk_fma_f32 v[114:115], v[114:115], v[230:231], v[230:231] op_sel:[0,0,1] op_sel_hi:[1,0,1] neg_lo:[0,0,1] neg_hi:[0,0,1]\n"
"v_pk_fma_f32 v[116:117], v[116:117], v[230:231], v[230:231] op_sel:[0,0,1] op_sel_hi:[1,0,1] neg_lo:[0,0,1] neg_hi:[0,0,1]\n"
"v_pk_fma_f32 v[118:119], v[118:119], v[230:231], v[230:231] op_sel:[0,0,1] op_sel_hi:[1,0,1] neg_lo:[0,0,1] neg_hi:[0,0,1]\n"
"v_pk_fma_f32 v[120:121], v[120:121], v[230:231], v[230:231] op_sel:[0,0,1] op_sel_hi:[1,0,1] neg_lo:[0,0,1] neg_hi:[0,0,1]\n"
"v_pk_fma_f32 v[122:123], v[122:123], v[230:231], v[230:231] op_sel:[0,0,1] op_sel_hi:[1,0,1] neg_lo:[0,0,1] neg_hi:[0,0,1]\n"
"v_pk_fma_f32 v[124:125], v[124:125], v[230:231], v[230:231] op_sel:[0,0,1] op_sel_hi:[1,0,1] neg_lo:[0,0,1] neg_hi:[0,0,1]\n"
"v_pk_fma_f32 v[126:127], v[126:127], v[230:231], v[230:231] op_sel:[0,0,1] op_sel_hi:[1,0,1] neg_lo:[0,0,1] neg_hi:[0,0,1]\n"
"s_cmp_lg_u64 vcc, 0\n"
"s_addc_u32 s55, s55, s55\n"
"s_waitcnt lgkmcnt(0)\n"
"v_mfma_f32_32x32x16_bf16 v[80:95], a[224:227], a[40:43], 0\n"
"v_cvt_pk_bf16_f32 v96, v96, v97\n"
"v_cvt_pk_bf16_f32 v97, v98, v99\n"
"v_cvt_pk_bf16_f32 v98, v100, v101\n"
"v_cvt_pk_bf16_f32 v99, v102, v103\n"
"v_cvt_pk_bf16_f32 v40, v40, v41\n"
"v_cvt_pk_bf16_f32 v42, v42, v43\n"
"v_mfma_f32_32x32x16_bf16 v[80:95], a[228:231], a[40:43], v[80:95]\n"
"ds_write_b128 v252, v[96:99] offset:8704\n"
"v_cvt_pk_bf16_f32 v44, v44, v45\n"
"v_cvt_pk_bf16_f32 v46, v46, v47\n"
"v_accvgpr_write_b32 a20, v40\n"
"v_accvgpr_write_b32 a21, v42\n"
"v_mfma_f32_32x32x16_bf16 v[80:95], a[232:235], a[44:47], v[80:95]\n"
"v_cvt_pk_bf16_f32 v100, v104, v105\n"
"v_cvt_pk_bf16_f32 v101, v106, v107\n"
"v_cvt_pk_bf16_f32 v102, v108, v109\n"
"v_cvt_pk_bf16_f32 v103, v110, v111\n"
"v_accvgpr_write_b32 a22, v44\n"
"v_accvgpr_write_b32 a23, v46\n"
"v_mfma_f32_32x32x16_bf16 v[80:95], a[236:239], a[44:47], v[80:95]\n"
"ds_write_b128 v252, v[100:103] offset:8768\n"
"v_cvt_pk_bf16_f32 v48, v48, v49\n"
"v_cvt_pk_bf16_f32 v50, v50, v51\n"
"v_cvt_pk_bf16_f32 v52, v52, v53\n"
"v_cvt_pk_bf16_f32 v54, v54, v55\n"
"v_mfma_f32_32x32x16_bf16 v[64:79], a[224:227], a[32:35], 0\n"
"v_cvt_pk_bf16_f32 v104, v112, v113\n"
"v_cvt_pk_bf16_f32 v105, v114, v115\n"
"v_cvt_pk_bf16_f32 v106, v116, v117\n"
"v_cvt_pk_bf16_f32 v107, v118, v119\n"
"v_accvgpr_write_b32 a24, v48\n"
"v_accvgpr_write_b32 a25, v50\n"
"v_mfma_f32_32x32x16_bf16 v[64:79], a[228:231], a[32:35], v[64:79]\n"
"ds_write_b128 v252, v[104:107] offset:8832\n"
"v_accvgpr_write_b32 a26, v52\n"
"v_accvgpr_write_b32 a27, v54\n"
"v_cvt_pk_bf16_f32 v56, v56, v57\n"
"v_cvt_pk_bf16_f32 v58, v58, v59\n"
"v_mfma_f32_32x32x16_bf16 v[64:79], a[232:235], a[36:39], v[64:79]\n"
"v_cvt_pk_bf16_f32 v108, v120, v121\n"
"v_cvt_pk_bf16_f32 v109, v122, v123\n"
"v_cvt_pk_bf16_f32 v110, v124, v125\n"
"v_cvt_pk_bf16_f32 v111, v126, v127\n"
"v_cvt_pk_bf16_f32 v60, v60, v61\n"
"v_cvt_pk_bf16_f32 v62, v62, v63\n"
"v_mfma_f32_32x32x16_bf16 v[64:79], a[236:239], a[36:39], v[64:79]\n"
"ds_write_b128 v252, v[108:111] offset:8896\n"
"v_accvgpr_write_b32 a28, v56\n"
"v_accvgpr_write_b32 a29, v58\n"
"v_accvgpr_write_b32 a30, v60\n"
"v_accvgpr_write_b32 a31, v62\n"
"v_mfma_f32_16x16x32_bf16 v[220:223], a[240:243], a[16:19], 0\n"
"v_mfma_f32_16x16x32_bf16 v[220:223], a[244:247], a[20:23], v[220:223]\n"
"v_sub_f32 v216, s71, v216\n"
"v_mfma_f32_16x16x32_bf16 v[220:223], a[248:251], a[24:27], v[220:223]\n"
"v_sub_f32 v217, s71, v217\n"
"v_mfma_f32_16x16x32_bf16 v[220:223], a[252:255], a[28:31], v[220:223]\n"
"v_mfma_f32_16x16x32_bf16 v[0:3], a[192:195], a[192:195], 0\n"
"v_sub_f32 v218, s71, v218\n"
"v_mfma_f32_16x16x32_bf16 v[0:3], a[196:199], a[196:199], v[0:3]\n"
"v_sub_f32 v219, s71, v219\n"
"v_mfma_f32_16x16x32_bf16 v[200:203], a[96:99], a[80:83], v[236:239]\n"
"ds_read_b64_tr_b16 a[208:209], v247 offset:58368\n"
"v_add_f32 v244, v244, v196\n"
"v_mfma_f32_16x16x32_bf16 v[200:203], a[100:103], a[84:87], v[200:203]\n"
"v_exp_f32 v197, v197\n"
"v_mfma_f32_16x16x32_bf16 v[200:203], a[104:107], a[88:91], v[200:203]\n"
"v_add_f32 v244, v244, v197\n"
"v_cvt_pk_bf16_f32 v210, v196, v197\n"
"v_mfma_f32_16x16x32_bf16 v[200:203], a[108:111], a[92:95], v[200:203]\n"
"v_exp_f32 v198, v198\n"
"v_mfma_f32_16x16x32_bf16 v[204:207], a[112:115], a[80:83], v[236:239]\n"
"ds_read_b64_tr_b16 a[210:211], v247 offset:62464\n"
"v_add_f32 v244, v244, v198\n"
"v_mfma_f32_16x16x32_bf16 v[204:207], a[116:119], a[84:87], v[204:207]\n"
"v_exp_f32 v199, v199\n"
"v_mfma_f32_16x16x32_bf16 v[204:207], a[120:123], a[88:91], v[204:207]\n"
"v_add_f32 v244, v244, v199\n"
"v_cvt_pk_bf16_f32 v211, v198, v199\n"
"v_mfma_f32_16x16x32_bf16 v[204:207], a[124:127], a[92:95], v[204:207]\n"
"v_exp_f32 v200, v200\n"
"v_mfma_f32_16x16x32_bf16 v[128:131], a[160:163], v[208:211], v[128:131]\n"
"ds_read_b64_tr_b16 a[212:213], v248 offset:58368\n"
"v_add_f32 v245, v245, v200\n"
"v_mfma_f32_16x16x32_bf16 v[132:135], a[164:167], v[208:211], v[132:135]\n"
"v_exp_f32 v201, v201\n"
"v_mfma_f32_16x16x32_bf16 v[136:139], a[168:171], v[208:211], v[136:139]\n"
"v_add_f32 v245, v245, v201\n"
"v_cvt_pk_bf16_f32 v212, v200, v201\n"
"v_mfma_f32_16x16x32_bf16 v[140:143], a[172:175], v[208:211], v[140:143]\n"
"v_exp_f32 v202, v202\n"
"v_mfma_f32_16x16x32_bf16 v[144:147], a[176:179], v[208:211], v[144:147]\n"
"ds_read_b64_tr_b16 a[214:215], v248 offset:62464\n"
"v_add_f32 v245, v245, v202\n"
"v_mfma_f32_16x16x32_bf16 v[148:151], a[180:183], v[208:211], v[148:151]\n"
"v_exp_f32 v203, v203\n"
"v_mfma_f32_16x16x32_bf16 v[152:155], a[184:187], v[208:211], v[152:155]\n"
"v_add_f32 v245, v245, v203\n"
"v_cvt_pk_bf16_f32 v213, v202, v203\n"
"v_mfma_f32_16x16x32_bf16 v[156:159], a[188:191], v[208:211], v[156:159]\n"
"v_exp_f32 v204, v204\n"
"v_mfma_f32_16x16x4_f32 v[232:235], v250, v244, 0\n"
"ds_read_b128 a[64:67], v254 offset:16384\n"
"ds_read_b128 a[68:71], v254 offset:16896\n"
"ds_read_b128 a[72:75], v254 offset:17408\n"
"ds_read_b128 a[76:79], v254 offset:17920\n"
"v_cvt_pk_bf16_f32 v64, v64, v65\n"
"v_cvt_pk_bf16_f32 v66, v66, v67\n"
"v_cvt_pk_bf16_f32 v68, v68, v69\n"
"v_cvt_pk_bf16_f32 v70, v70, v71\n"
"v_accvgpr_write_b32 a32, v64\n"
"v_accvgpr_write_b32 a33, v66\n"
"v_accvgpr_write_b32 a34, v68\n"
"v_accvgpr_write_b32 a35, v70\n"
"v_readlane_b32 s99, v0, 0\n"
"v_readlane_b32 s72, v1, 21\n"
"v_readlane_b32 s73, v2, 42\n"
"v_readlane_b32 s60, v3, 63\n"
"v_rcp_f32 v234, v232\n"
"v_med3_f32 v233, v232, v251, s63\n"
"v_cmp_neq_f32 vcc, v233, v232\n"
"v_mul_f32 v235, s98, v234\n"
"v_pk_fma_f32 v[128:129], v[128:129], v[234:235], v[234:235] op_sel:[0,0,1] op_sel_hi:[1,0,1] neg_lo:[0,0,1] neg_hi:[0,0,1]\n"
"v_pk_fma_f32 v[130:131], v[130:131], v[234:235], v[234:235] op_sel:[0,0,1] op_sel_hi:[1,0,1] neg_lo:[0,0,1] neg_hi:[0,0,1]\n"
"v_pk_fma_f32 v[132:133], v[132:133], v[234:235], v[234:235] op_sel:[0,0,1] op_sel_hi:[1,0,1] neg_lo:[0,0,1] neg_hi:[0,0,1]\n"
"v_pk_fma_f32 v[134:135], v[134:135], v[234:235], v[234:235] op_sel:[0,0,1] op_sel_hi:[1,0,1] neg_lo:[0,0,1] neg_hi:[0,0,1]\n"
"v_pk_fma_f32 v[136:137], v[136:137], v[234:235], v[234:235] op_sel:[0,0,1] op_sel_hi:[1,0,1] neg_lo:[0,0,1] neg_hi:[0,0,1]\n"
"v_pk_fma_f32 v[138:139], v[138:139], v[234:235], v[234:235] op_sel:[0,0,1] op_sel_hi:[1,0,1] neg_lo:[0,0,1] neg_hi:[0,0,1]\n"
"v_pk_fma_f32 v[140:141], v[140:141], v[234:235], v[234:235] op_sel:[0,0,1] op_sel_hi:[1,0,1] neg_lo:[0,0,1] neg_hi:[0,0,1]\n"
"v_pk_fma_f32 v[142:143], v[142:143], v[234:235], v[234:235] op_sel:[0,0,1] op_sel_hi:[1,0,1] neg_lo:[0,0,1] neg_hi:[0,0,1]\n"
"v_pk_fma_f32 v[144:145], v[144:145], v[234:235], v[234:235] op_sel:[0,0,1] op_sel_hi:[1,0,1] neg_lo:[0,0,1] neg_hi:[0,0,1]\n"
"v_pk_fma_f32 v[146:147], v[146:147], v[234:235], v[234:235] op_sel:[0,0,1] op_sel_hi:[1,0,1] neg_lo:[0,0,1] neg_hi:[0,0,1]\n"
"v_pk_fma_f32 v[148:149], v[148:149], v[234:235], v[234:235] op_sel:[0,0,1] op_sel_hi:[1,0,1] neg_lo:[0,0,1] neg_hi:[0,0,1]\n"
"v_pk_fma_f32 v[150:151], v[150:151], v[234:235], v[234:235] op_sel:[0,0,1] op_sel_hi:[1,0,1] neg_lo:[0,0,1] neg_hi:[0,0,1]\n"
"v_pk_fma_f32 v[152:153], v[152:153], v[234:235], v[234:235] op_sel:[0,0,1] op_sel_hi:[1,0,1] neg_lo:[0,0,1] neg_hi:[0,0,1]\n"
"v_pk_fma_f32 v[154:155], v[154:155], v[234:235], v[234:235] op_sel:[0,0,1] op_sel_hi:[1,0,1] neg_lo:[0,0,1] neg_hi:[0,0,1]\n"
"v_pk_fma_f32 v[156:157], v[156:157], v[234:235], v[234:235] op_sel:[0,0,1] op_sel_hi:[1,0,1] neg_lo:[0,0,1] neg_hi:[0,0,1]\n"
"v_pk_fma_f32 v[158:159], v[158:159], v[234:235], v[234:235] op_sel:[0,0,1] op_sel_hi:[1,0,1] neg_lo:[0,0,1] neg_hi:[0,0,1]\n"
"s_cmp_lg_u64 vcc, 0\n"
"s_addc_u32 s55, s55, s55\n"
"v_mfma_f32_32x32x16_bf16 v[112:127], a[224:227], a[56:59], 0\n"
"s_min_u32 s99, s99, s72\n"
"v_cvt_pk_bf16_f32 v128, v128, v129\n"
"v_cvt_pk_bf16_f32 v129, v130, v131\n"
"v_cvt_pk_bf16_f32 v130, v132, v133\n"
"v_cvt_pk_bf16_f32 v131, v134, v135\n"
"v_cvt_pk_bf16_f32 v72, v72, v73\n"
"v_cvt_pk_bf16_f32 v74, v74, v75\n"
"v_mfma_f32_32x32x16_bf16 v[112:127], a[228:231], a[56:59], v[112:127]\n"
"s_min_u32 s73, s73, s60\n"
"ds_write_b128 v252, v[128:131] offset:13056\n"
"v_cvt_pk_bf16_f32 v76, v76, v77\n"
"v_cvt_pk_bf16_f32 v78, v78, v79\n"
"v_accvgpr_write_b32 a36, v72\n"
"v_accvgpr_write_b32 a37, v74\n"
"v_mfma_f32_32x32x16_bf16 v[112:127], a[232:235], a[60:63], v[112:127]\n"
"s_min_u32 s99, s99, s73\n"
"v_cvt_pk_bf16_f32 v132, v136, v137\n"
"v_cvt_pk_bf16_f32 v133, v138, v139\n"
"v_cvt_pk_bf16_f32 v134, v140, v141\n"
"v_cvt_pk_bf16_f32 v135, v142, v143\n"
"v_accvgpr_write_b32 a38, v76\n"
"v_accvgpr_write_b32 a39, v78\n"
"v_mfma_f32_32x32x16_bf16 v[112:127], a[236:239], a[60:63], v[112:127]\n"
"s_lshr_b32 s99, s99, 23\n"
"ds_write_b128 v252, v[132:135] offset:13120\n"
"v_cvt_pk_bf16_f32 v80, v80, v81\n"
"v_cvt_pk_bf16_f32 v82, v82, v83\n"
"v_cvt_pk_bf16_f32 v84, v84, v85\n"
"v_cvt_pk_bf16_f32 v86, v86, v87\n"
"v_mfma_f32_32x32x16_bf16 v[96:111], a[224:227], a[48:51], 0\n"
"s_add_u32 s72, s99, 45\n"
"v_cvt_pk_bf16_f32 v136, v144, v145\n"
"v_cvt_pk_bf16_f32 v137, v146, v147\n"
"v_cvt_pk_bf16_f32 v138, v148, v149\n"
"v_cvt_pk_bf16_f32 v139, v150, v151\n"
"v_accvgpr_write_b32 a40, v80\n"
"v_accvgpr_write_b32 a41, v82\n"
"v_mfma_f32_32x32x16_bf16 v[96:111], a[228:231], a[48:51], v[96:111]\n"
"s_lshr_b32 s72, s72, 1\n"
"ds_write_b128 v252, v[136:139] offset:13184\n"
"v_accvgpr_write_b32 a42, v84\n"
"v_accvgpr_write_b32 a43, v86\n"
"v_cvt_pk_bf16_f32 v88, v88, v89\n"
"v_cvt_pk_bf16_f32 v90, v90, v91\n"
"v_mfma_f32_32x32x16_bf16 v[96:111], a[232:235], a[52:55], v[96:111]\n"
"s_lshl_b32 s72, s72, 23\n"
"v_cvt_pk_bf16_f32 v140, v152, v153\n"
"v_cvt_pk_bf16_f32 v141, v154, v155\n"
"v_cvt_pk_bf16_f32 v142, v156, v157\n"
"v_cvt_pk_bf16_f32 v143, v158, v159\n"
"v_cvt_pk_bf16_f32 v92, v92, v93\n"
"v_cvt_pk_bf16_f32 v94, v94, v95\n"
"v_mfma_f32_32x32x16_bf16 v[96:111], a[236:239], a[52:55], v[96:111]\n"
"s_or_b32 s72, s72, 4194304\n"
"s_sub_u32 s99, s99, 33\n"
"s_mov_b32 s73, 671088640\n"
"s_cmp_lt_u32 s99, 200\n"
"s_cselect_b32 s99, s72, 0\n"
"s_cselect_b32 s73, 1568669696, s73\n"
"ds_write_b128 v252, v[140:143] offset:13248\n"
"v_accvgpr_write_b32 a44, v88\n"
"v_accvgpr_write_b32 a45, v90\n"
"v_accvgpr_write_b32 a46, v92\n"
"v_accvgpr_write_b32 a47, v94\n"
"v_mfma_f32_16x16x32_bf16 v[224:227], a[240:243], a[32:35], 0\n"
"v_sub_f32 v220, s71, v220\n"
"v_mfma_f32_16x16x32_bf16 v[224:227], a[244:247], a[36:39], v[224:227]\n"
"v_sub_f32 v221, s71, v221\n"
"v_mfma_f32_16x16x32_bf16 v[224:227], a[248:251], a[40:43], v[224:227]\n"
"v_sub_f32 v222, s71, v222\n"
"v_mfma_f32_16x16x32_bf16 v[224:227], a[252:255], a[44:47], v[224:227]\n"
"v_sub_f32 v223, s71, v223\n"
"v_mfma_f32_16x16x32_bf16 v[192:195], a[128:131], a[0:3], v[216:219]\n"
"ds_read_b64_tr_b16 a[216:217], v247 offset:58880\n"
"v_add_f32 v245, v245, v204\n"
"v_mfma_f32_16x16x32_bf16 v[192:195], a[132:135], a[4:7], v[192:195]\n"
"v_exp_f32 v205, v205\n"
"v_mfma_f32_16x16x32_bf16 v[192:195], a[136:139], a[8:11], v[192:195]\n"
"v_add_f32 v245, v245, v205\n"
"v_cvt_pk_bf16_f32 v214, v204, v205\n"
"v_mfma_f32_16x16x32_bf16 v[192:195], a[140:143], a[12:15], v[192:195]\n"
"v_exp_f32 v206, v206\n"
"v_mfma_f32_16x16x32_bf16 v[196:199], a[144:147], a[0:3], v[216:219]\n"
"ds_read_b64_tr_b16 a[218:219], v247 offset:62976\n"
"v_add_f32 v245, v245, v206\n"
"v_mfma_f32_16x16x32_bf16 v[196:199], a[148:151], a[4:7], v[196:199]\n"
"v_exp_f32 v207, v207\n"
"v_mfma_f32_16x16x32_bf16 v[196:199], a[152:155], a[8:11], v[196:199]\n"
"v_add_f32 v245, v245, v207\n"
"v_cvt_pk_bf16_f32 v215, v206, v207\n"
"v_mfma_f32_16x16x32_bf16 v[196:199], a[156:159], a[12:15], v[196:199]\n"
"v_exp_f32 v192, v192\n"
"v_mfma_f32_16x16x32_bf16 v[160:163], a[160:163], v[212:215], v[160:163]\n"
"ds_read_b64_tr_b16 a[220:221], v248 offset:58880\n"
"v_mov_b32 v28, s99\n"
"v_mov_b32 v240, v192\n"
"v_mfma_f32_16x16x32_bf16 v[164:167], a[164:167], v[212:215], v[164:167]\n"
"v_exp_f32 v193, v193\n"
"v_mfma_f32_16x16x32_bf16 v[168:171], a[168:171], v[212:215], v[168:171]\n"
"v_add_f32 v240, v240, v193\n"
"v_cvt_pk_bf16_f32 v208, v192, v193\n"
"v_mfma_f32_16x16x32_bf16 v[172:175], a[172:175], v[212:215], v[172:175]\n"
"v_exp_f32 v194, v194\n"
"v_mfma_f32_16x16x32_bf16 v[176:179], a[176:179], v[212:215], v[176:179]\n"
"ds_read_b64_tr_b16 a[222:223], v248 offset:62976\n"
"v_mov_b32 v29, s99\n"
"v_add_f32 v240, v240, v194\n"
"v_mfma_f32_16x16x32_bf16 v[180:183], a[180:183], v[212:215], v[180:183]\n"
"v_exp_f32 v195, v195\n"
"v_mfma_f32_16x16x32_bf16 v[184:187], a[184:187], v[212:215], v[184:187]\n"
"v_add_f32 v240, v240, v195\n"
"v_cvt_pk_bf16_f32 v209, v194, v195\n"
"v_mfma_f32_16x16x32_bf16 v[188:191], a[188:191], v[212:215], v[188:191]\n"
"v_exp_f32 v196, v196\n"
"v_mfma_f32_16x16x4_f32 v[236:239], v250, v245, 0\n"
"ds_read_b128 a[80:83], v254 offset:20480\n"
"ds_read_b128 a[84:87], v254 offset:20992\n"
"ds_read_b128 a[88:91], v254 offset:21504\n"
"ds_read_b128 a[92:95], v254 offset:22016\n"
"v_cvt_pk_bf16_f32 v96, v96, v97\n"
"v_cvt_pk_bf16_f32 v98, v98, v99\n"
"v_cvt_pk_bf16_f32 v100, v100, v101\n"
"v_cvt_pk_bf16_f32 v102, v102, v103\n"
"v_accvgpr_write_b32 a48, v96\n"
"v_accvgpr_write_b32 a49, v98\n"
"v_accvgpr_write_b32 a50, v100\n"
"v_accvgpr_write_b32 a51, v102\n"
"v_rcp_f32 v238, v236\n"
"v_med3_f32 v237, v236, v251, s63\n"
"v_cmp_neq_f32 vcc, v237, v236\n"
"v_mul_f32 v239, s98, v238\n"
"v_pk_fma_f32 v[160:161], v[160:161], v[238:239], v[238:239] op_sel:[0,0,1] op_sel_hi:[1,0,1] neg_lo:[0,0,1] neg_hi:[0,0,1]\n"
"v_pk_fma_f32 v[162:163], v[162:163], v[238:239], v[238:239] op_sel:[0,0,1] op_sel_hi:[1,0,1] neg_lo:[0,0,1] neg_hi:[0,0,1]\n"
"v_pk_fma_f32 v[164:165], v[164:165], v[238:239], v[238:239] op_sel:[0,0,1] op_sel_hi:[1,0,1] neg_lo:[0,0,1] neg_hi:[0,0,1]\n"
"v_pk_fma_f32 v[166:167], v[166:167], v[238:239], v[238:239] op_sel:[0,0,1] op_sel_hi:[1,0,1] neg_lo:[0,0,1] neg_hi:[0,0,1]\n"
"v_pk_fma_f32 v[168:169], v[168:169], v[238:239], v[238:239] op_sel:[0,0,1] op_sel_hi:[1,0,1] neg_lo:[0,0,1] neg_hi:[0,0,1]\n"
"v_pk_fma_f32 v[170:171], v[170:171], v[238:239], v[238:239] op_sel:[0,0,1] op_sel_hi:[1,0,1] neg_lo:[0,0,1] neg_hi:[0,0,1]\n"
"v_pk_fma_f32 v[172:173], v[172:173], v[238:239], v[238:239] op_sel:[0,0,1] op_sel_hi:[1,0,1] neg_lo:[0,0,1] neg_hi:[0,0,1]\n"
"v_pk_fma_f32 v[174:175], v[174:175], v[238:239], v[238:239] op_sel:[0,0,1] op_sel_hi:[1,0,1] neg_lo:[0,0,1] neg_hi:[0,0,1]\n"
"v_pk_fma_f32 v[176:177], v[176:177], v[238:239], v[238:239] op_sel:[0,0,1] op_sel_hi:[1,0,1] neg_lo:[0,0,1] neg_hi:[0,0,1]\n"
"v_pk_fma_f32 v[178:179], v[178:179], v[238:239], v[238:239] op_sel:[0,0,1] op_sel_hi:[1,0,1] neg_lo:[0,0,1] neg_hi:[0,0,1]\n"
"v_pk_fma_f32 v[180:181], v[180:181], v[238:239], v[238:239] op_sel:[0,0,1] op_sel_hi:[1,0,1] neg_lo:[0,0,1] neg_hi:[0,0,1]\n"
"v_pk_fma_f32 v[182:183], v[182:183], v[238:239], v[238:239] op_sel:[0,0,1] op_sel_hi:[1,0,1] neg_lo:[0,0,1] neg_hi:[0,0,1]\n"
"v_pk_fma_f32 v[184:185], v[184:185], v[238:239], v[238:239] op_sel:[0,0,1] op_sel_hi:[1,0,1] neg_lo:[0,0,1] neg_hi:[0,0,1]\n"
"v_pk_fma_f32 v[186:187], v[186:187], v[238:239], v[238:239] op_sel:[0,0,1] op_sel_hi:[1,0,1] neg_lo:[0,0,1] neg_hi:[0,0,1]\n"
"v_pk_fma_f32 v[188:189], v[188:189], v[238:239], v[238:239] op_sel:[0,0,1] op_sel_hi:[1,0,1] neg_lo:[0,0,1] neg_hi:[0,0,1]\n"
"v_pk_fma_f32 v[190:191], v[190:191], v[238:239], v[238:239] op_sel:[0,0,1] op_sel_hi:[1,0,1] neg_lo:[0,0,1] neg_hi:[0,0,1]\n"
"s_cmp_lg_u64 vcc, 0\n"
"s_addc_u32 s55, s55, s55\n"
"s_waitcnt lgkmcnt(0)\n"
"v_mfma_f32_32x32x16_bf16 v[144:159], a[224:227], a[72:75], 0\n"
"v_cvt_pk_bf16_f32 v160, v160, v161\n"
"v_cvt_pk_bf16_f32 v161, v162, v163\n"
"v_cvt_pk_bf16_f32 v162, v164, v165\n"
"v_cvt_pk_bf16_f32 v163, v166, v167\n"
"v_cvt_pk_bf16_f32 v104, v104, v105\n"
"v_cvt_pk_bf16_f32 v106, v106, v107\n"
"v_mfma_f32_32x32x16_bf16 v[144:159], a[228:231], a[72:75], v[144:159]\n"
"Lhop5%=:\n"
"s_cbranch_execz Lhopret%=\n"
"ds_write_b128 v252, v[160:163] offset:17408\n"
"v_cvt_pk_bf16_f32 v108, v108, v109\n"
"v_cvt_pk_bf16_f32 v110, v110, v111\n"
"v_accvgpr_write_b32 a52, v104\n"
"v_accvgpr_write_b32 a53, v106\n"
"v_mfma_f32_32x32x16_bf16 v[144:159], a[232:235], a[76:79], v[144:159]\n"
"v_cvt_pk_bf16_f32 v164, v168, v169\n"
"v_cvt_pk_bf16_f32 v165, v170, v171\n"
"v_cvt_pk_bf16_f32 v166, v172, v173\n"
"v_cvt_pk_bf16_f32 v167, v174, v175\n"
"v_accvgpr_write_b32 a54, v108\n"
"v_accvgpr_write_b32 a55, v110\n"
"v_mfma_f32_32x32x16_bf16 v[144:159], a[236:239], a[76:79], v[144:159]\n"
"ds_write_b128 v252, v[164:167] offset:17472\n"
"v_cvt_pk_bf16_f32 v112, v112, v113\n"
"v_cvt_pk_bf16_f32 v114, v114, v115\n"
"v_cvt_pk_bf16_f32 v116, v116, v117\n"
"v_cvt_pk_bf16_f32 v118, v118, v119\n"
"v_mfma_f32_32x32x16_bf16 v[128:143], a[224:227], a[64:67], 0\n"
"v_cvt_pk_bf16_f32 v168, v176, v177\n"
"v_cvt_pk_bf16_f32 v169, v178, v179\n"
"v_cvt_pk_bf16_f32 v170, v180, v181\n"
"v_cvt_pk_bf16_f32 v171, v182, v183\n"
"v_accvgpr_write_b32 a56, v112\n"
"v_accvgpr_write_b32 a57, v114\n"
"v_mfma_f32_32x32x16_bf16 v[128:143], a[228:231], a[64:67], v[128:143]\n"
"ds_write_b128 v252, v[168:171] offset:17536\n"
"v_accvgpr_write_b32 a58, v116\n"
"v_accvgpr_write_b32 a59, v118\n"
"v_cvt_pk_bf16_f32 v120, v120, v121\n"
"v_cvt_pk_bf16_f32 v122, v122, v123\n"
"v_mfma_f32_32x32x16_bf16 v[128:143], a[232:235], a[68:71], v[128:143]\n"
"v_cvt_pk_bf16_f32 v172, v184, v185\n"
"v_cvt_pk_bf16_f32 v173, v186, v187\n"
"v_cvt_pk_bf16_f32 v174, v188, v189\n"
"v_cvt_pk_bf16_f32 v175, v190, v191\n"
"v_cvt_pk_bf16_f32 v124, v124, v125\n"
"v_cvt_pk_bf16_f32 v126, v126, v127\n"
"v_mfma_f32_32x32x16_bf16 v[128:143], a[236:239], a[68:71], v[128:143]\n"
"ds_write_b128 v252, v[172:175] offset:17600\n"
"v_accvgpr_write_b32 a60, v120\n"
"v_accvgpr_write_b32 a61, v122\n"
"v_accvgpr_write_b32 a62, v124\n"
"v_accvgpr_write_b32 a63, v126\n"
"v_mfma_f32_16x16x32_bf16 v[228:231], a[240:243], a[48:51], 0\n"
"v_sub_f32 v224, s71, v224\n"
"v_mfma_f32_16x16x32_bf16 v[228:231], a[244:247], a[52:55], v[228:231]\n"
"v_sub_f32 v225, s71, v225\n"
"v_mfma_f32_16x16x32_bf16 v[228:231], a[248:251], a[56:59], v[228:231]\n"
"v_sub_f32 v226, s71, v226\n"
"v_mfma_f32_16x16x32_bf16 v[228:231], a[252:255], a[60:63], v[228:231]\n"
"v_sub_f32 v227, s71, v227\n"
"v_mfma_f32_16x16x32_bf16 v[200:203], a[128:131], a[16:19], v[220:223]\n"
"ds_read_b128 a[96:99], v246 offset:0\n"
"v_mov_b32 v30, s99\n"
"v_add_f32 v240, v240, v196\n"
"v_mfma_f32_16x16x32_bf16 v[200:203], a[132:135], a[20:23], v[200:203]\n"
"v_exp_f32 v197, v197\n"
"v_mfma_f32_16x16x32_bf16 v[200:203], a[136:139], a[24:27], v[200:203]\n"
"v_add_f32 v240, v240, v197\n"
"v_cvt_pk_bf16_f32 v210, v196, v197\n"
"v_mfma_f32_16x16x32_bf16 v[200:203], a[140:143], a[28:31], v[200:203]\n"
"v_exp_f32 v198, v198\n"
"v_mfma_f32_16x16x32_bf16 v[204:207], a[144:147], a[16:19], v[220:223]\n"
"ds_read_b128 a[100:103], v246 offset:512\n"
"v_mov_b32 v31, s99\n"
"v_add_f32 v240, v240, v198\n"
"v_mfma_f32_16x16x32_bf16 v[204:207], a[148:151], a[20:23], v[204:207]\n"
"v_exp_f32 v199, v199\n"
"v_mfma_f32_16x16x32_bf16 v[204:207], a[152:155], a[24:27], v[204:207]\n"
"v_add_f32 v240, v240, v199\n"
"v_cvt_pk_bf16_f32 v211, v198, v199\n"
"v_mfma_f32_16x16x32_bf16 v[204:207], a[156:159], a[28:31], v[204:207]\n"
"s_barrier\n"
"v_exp_f32 v200, v200\n"
"v_mfma_f32_16x16x32_bf16 v[0:3], a[192:195], v[208:211], v[28:31]\n"
"ds_read_b128 a[104:107], v246 offset:1024\n"
"v_mov_b32 v60, s99\n"
"v_mov_b32 v241, v200\n"
"v_mfma_f32_16x16x32_bf16 v[4:7], a[196:199], v[208:211], v[28:31]\n"
"v_exp_f32 v201, v201\n"
"v_mfma_f32_16x16x32_bf16 v[8:11], a[200:203], v[208:211], v[28:31]\n"
"v_add_f32 v241, v241, v201\n"
"v_cvt_pk_bf16_f32 v212, v200, v201\n"
"v_mfma_f32_16x16x32_bf16 v[12:15], a[204:207], v[208:211], v[28:31]\n"
"v_exp_f32 v202, v202\n"
"v_mfma_f32_16x16x32_bf16 v[16:19], a[208:211], v[208:211], v[28:31]\n"
"ds_read_b128 a[108:111], v246 offset:1536\n"
"v_mov_b32 v61, s99\n"
"v_add_f32 v241, v241, v202\n"
"v_mfma_f32_16x16x32_bf16 v[20:23], a[212:215], v[208:211], v[28:31]\n"
"v_exp_f32 v203, v203\n"
"v_mfma_f32_16x16x32_bf16 v[24:27], a[216:219], v[208:211], v[28:31]\n"
"v_add_f32 v241, v241, v203\n"
"v_cvt_pk_bf16_f32 v213, v202, v203\n"
"v_mfma_f32_16x16x32_bf16 v[28:31], a[220:223], v[208:211], v[28:31]\n"
"v_exp_f32 v204, v204\n"
"v_mfma_f32_32x32x16_bf16 v[176:191], a[224:227], a[88:91], 0\n"
"v_cvt_pk_bf16_f32 v128, v128, v129\n"
"v_cvt_pk_bf16_f32 v130, v130, v131\n"
"v_cvt_pk_bf16_f32 v132, v132, v133\n"
"v_cvt_pk_bf16_f32 v134, v134, v135\n"
"v_mfma_f32_32x32x16_bf16 v[176:191], a[228:231], a[88:91], v[176:191]\n"
"v_accvgpr_write_b32 a64, v128\n"
"v_accvgpr_write_b32 a65, v130\n"
"v_accvgpr_write_b32 a66, v132\n"
"v_accvgpr_write_b32 a67, v134\n"
"v_cvt_pk_bf16_f32 v136, v136, v137\n"
"v_mfma_f32_32x32x16_bf16 v[176:191], a[232:235], a[92:95], v[176:191]\n"
"v_cvt_pk_bf16_f32 v138, v138, v139\n"
"v_cvt_pk_bf16_f32 v140, v140, v141\n"
"v_cvt_pk_bf16_f32 v142, v142, v143\n"
"v_accvgpr_write_b32 a68, v136\n"
"v_mfma_f32_32x32x16_bf16 v[176:191], a[236:239], a[92:95], v[176:191]\n"
"v_accvgpr_write_b32 a69, v138\n"
"v_accvgpr_write_b32 a70, v140\n"
"v_accvgpr_write_b32 a71, v142\n"
"v_cvt_pk_bf16_f32 v144, v144, v145\n"
"v_cvt_pk_bf16_f32 v146, v146, v147\n"
"v_mfma_f32_32x32x16_bf16 v[160:175], a[224:227], a[80:83], 0\n"
"v_cvt_pk_bf16_f32 v148, v148, v149\n"
"v_cvt_pk_bf16_f32 v150, v150, v151\n"
"v_accvgpr_write_b32 a72, v144\n"
"v_accvgpr_write_b32 a73, v146\n"
"v_mfma_f32_32x32x16_bf16 v[160:175], a[228:231], a[80:83], v[160:175]\n"
"v_accvgpr_write_b32 a74, v148\n"
"v_accvgpr_write_b32 a75, v150\n"
"v_cvt_pk_bf16_f32 v152, v152, v153\n"
"v_cvt_pk_bf16_f32 v154, v154, v155\n"
"v_cvt_pk_bf16_f32 v156, v156, v157\n"
"v_mfma_f32_32x32x16_bf16 v[160:175], a[232:235], a[84:87], v[160:175]\n"
"v_cvt_pk_bf16_f32 v158, v158, v159\n"
"v_accvgpr_write_b32 a76, v152\n"
"v_accvgpr_write_b32 a77, v154\n"
"v_accvgpr_write_b32 a78, v156\n"
"v_mfma_f32_32x32x16_bf16 v[160:175], a[236:239], a[84:87], v[160:175]\n"
"v_accvgpr_write_b32 a79, v158\n"
"v_sub_f32 v228, s71, v228\n"
"v_sub_f32 v229, s71, v229\n"
"v_sub_f32 v230, s71, v230\n"
"v_sub_f32 v231, s71, v231\n"
"v_mfma_f32_16x16x32_bf16 v[232:235], a[240:243], a[64:67], 0\n"
"v_mfma_f32_16x16x32_bf16 v[232:235], a[244:247], a[68:71], v[232:235]\n"
"v_mfma_f32_16x16x32_bf16 v[232:235], a[248:251], a[72:75], v[232:235]\n"
"v_mfma_f32_16x16x32_bf16 v[232:235], a[252:255], a[76:79], v[232:235]\n"
"v_mfma_f32_16x16x32_bf16 v[192:195], a[128:131], a[32:35], v[224:227]\n"
"ds_read_b128 a[112:115], v246 offset:4096\n"
"v_mov_b32 v62, s99\n"
"v_add_f32 v241, v241, v204\n"
"v_mfma_f32_16x16x32_bf16 v[192:195], a[132:135], a[36:39], v[192:195]\n"
"v_exp_f32 v205, v205\n"
"v_mfma_f32_16x16x32_bf16 v[192:195], a[136:139], a[40:43], v[192:195]\n"
"v_add_f32 v241, v241, v205\n"
"v_cvt_pk_bf16_f32 v214, v204, v205\n"
"v_mfma_f32_16x16x32_bf16 v[192:195], a[140:143], a[44:47], v[192:195]\n"
"v_exp_f32 v206, v206\n"
"v_mfma_f32_16x16x32_bf16 v[196:199], a[144:147], a[32:35], v[224:227]\n"
"ds_read_b128 a[116:119], v246 offset:4608\n"
"v_mov_b32 v63, s99\n"
"v_add_f32 v241, v241, v206\n"
"v_mfma_f32_16x16x32_bf16 v[196:199], a[148:151], a[36:39], v[196:199]\n"
"v_exp_f32 v207, v207\n"
"v_mfma_f32_16x16x32_bf16 v[196:199], a[152:155], a[40:43], v[196:199]\n"
"v_add_f32 v241, v241, v207\n"
"v_cvt_pk_bf16_f32 v215, v206, v207\n"
"v_mfma_f32_16x16x32_bf16 v[196:199], a[156:159], a[44:47], v[196:199]\n"
"v_exp_f32 v192, v192\n"
"v_mfma_f32_16x16x32_bf16 v[32:35], a[192:195], v[212:215], v[60:63]\n"
"ds_read_b128 a[120:123], v246 offset:5120\n"
"v_mov_b32 v92, s99\n"
"v_mov_b32 v242, v192\n"
"v_mfma_f32_16x16x32_bf16 v[36:39], a[196:199], v[212:215], v[60:63]\n"
"v_exp_f32 v193, v193\n"
"v_mfma_f32_16x16x32_bf16 v[40:43], a[200:203], v[212:215], v[60:63]\n"
"v_add_f32 v242, v242, v193\n"
"v_cvt_pk_bf16_f32 v208, v192, v193\n"
"v_mfma_f32_16x16x32_bf16 v[44:47], a[204:207], v[212:215], v[60:63]\n"
"v_exp_f32 v194, v194\n"
"v_mfma_f32_16x16x32_bf16 v[48:51], a[208:211], v[212:215], v[60:63]\n"
"ds_read_b128 a[124:127], v246 offset:5632\n"
"v_mov_b32 v93, s99\n"
"v_add_f32 v242, v242, v194\n"
"v_mfma_f32_16x16x32_bf16 v[52:55], a[212:215], v[212:215], v[60:63]\n"
"v_exp_f32 v195, v195\n"
"v_mfma_f32_16x16x32_bf16 v[56:59], a[216:219], v[212:215], v[60:63]\n"
"v_add_f32 v242, v242, v195\n"
"v_cvt_pk_bf16_f32 v209, v194, v195\n"
"v_mfma_f32_16x16x32_bf16 v[60:63], a[220:223], v[212:215], v[60:63]\n"
"v_exp_f32 v196, v196\n"
"v_cvt_pk_bf16_f32 v160, v160, v161\n"
"v_cvt_pk_bf16_f32 v162, v162, v163\n"
"v_cvt_pk_bf16_f32 v164, v164, v165\n"
"v_cvt_pk_bf16_f32 v166, v166, v167\n"
"v_accvgpr_write_b32 a80, v160\n"
"v_accvgpr_write_b32 a81, v162\n"
"v_accvgpr_write_b32 a82, v164\n"
"v_accvgpr_write_b32 a83, v166\n"
"v_cvt_pk_bf16_f32 v168, v168, v169\n"
"v_cvt_pk_bf16_f32 v170, v170, v171\n"
"v_cvt_pk_bf16_f32 v172, v172, v173\n"
"v_cvt_pk_bf16_f32 v174, v174, v175\n"
"v_accvgpr_write_b32 a84, v168\n"
"v_accvgpr_write_b32 a85, v170\n"
"v_accvgpr_write_b32 a86, v172\n"
"v_accvgpr_write_b32 a87, v174\n"
"v_cvt_pk_bf16_f32 v176, v176, v177\n"
"v_cvt_pk_bf16_f32 v178, v178, v179\n"
"v_cvt_pk_bf16_f32 v180, v180, v181\n"
"v_cvt_pk_bf16_f32 v182, v182, v183\n"
"v_accvgpr_write_b32 a88, v176\n"
"v_accvgpr_write_b32 a89, v178\n"
"v_accvgpr_write_b32 a90, v180\n"
"v_accvgpr_write_b32 a91, v182\n"
"v_cvt_pk_bf16_f32 v184, v184, v185\n"
"v_cvt_pk_bf16_f32 v186, v186, v187\n"
"v_cvt_pk_bf16_f32 v188, v188, v189\n"
"v_cvt_pk_bf16_f32 v190, v190, v191\n"
"v_accvgpr_write_b32 a92, v184\n"
"v_accvgpr_write_b32 a93, v186\n"
"v_accvgpr_write_b32 a94, v188\n"
"v_accvgpr_write_b32 a95, v190\n"
"v_mfma_f32_16x16x32_bf16 v[236:239], a[240:243], a[80:83], 0\n"
"v_sub_f32 v232, s71, v232\n"
"v_mfma_f32_16x16x32_bf16 v[236:239], a[244:247], a[84:87], v[236:239]\n"
"v_sub_f32 v233, s71, v233\n"
"v_mfma_f32_16x16x32_bf16 v[236:239], a[248:251], a[88:91], v[236:239]\n"
"v_sub_f32 v234, s71, v234\n"
"v_mfma_f32_16x16x32_bf16 v[236:239], a[252:255], a[92:95], v[236:239]\n"
"v_sub_f32 v235, s71, v235\n"
"v_mfma_f32_16x16x32_bf16 v[200:203], a[128:131], a[48:51], v[228:231]\n"
"ds_read2_b64 a[224:227], v253 offset0:0 offset1:2\n"
"ds_read_b64_tr_b16 a[160:161], v247 offset:32768\n"
"v_mov_b32 v94, s99\n"
"v_add_f32 v242, v242, v196\n"
"v_mfma_f32_16x16x32_bf16 v[200:203], a[132:135], a[52:55], v[200:203]\n"
"ds_read2_b64 a[228:231], v253 offset0:136 offset1:138\n"
"v_exp_f32 v197, v197\n"
"v_mfma_f32_16x16x32_bf16 v[200:203], a[136:139], a[56:59], v[200:203]\n"
"ds_read2_b64 a[232:235], v249 offset0:0 offset1:2\n"
"v_add_f32 v242, v242, v197\n"
"v_cvt_pk_bf16_f32 v210, v196, v197\n"
"v_mfma_f32_16x16x32_bf16 v[200:203], a[140:143], a[60:63], v[200:203]\n"
"ds_read2_b64 a[236:239], v249 offset0:136 offset1:138\n"
"v_exp_f32 v198, v198\n"
"v_mfma_f32_16x16x32_bf16 v[204:207], a[144:147], a[48:51], v[228:231]\n"
"ds_read_b64_tr_b16 a[162:163], v247 offset:36864\n"
"v_mov_b32 v95, s99\n"
"v_add_f32 v242, v242, v198\n"
"v_mfma_f32_16x16x32_bf16 v[204:207], a[148:151], a[52:55], v[204:207]\n"
"v_exp_f32 v199, v199\n"
"v_mfma_f32_16x16x32_bf16 v[204:207], a[152:155], a[56:59], v[204:207]\n"
"v_add_f32 v242, v242, v199\n"
"v_cvt_pk_bf16_f32 v211, v198, v199\n"
"v_mfma_f32_16x16x32_bf16 v[204:207], a[156:159], a[60:63], v[204:207]\n"
"v_exp_f32 v200, v200\n"
"v_mfma_f32_16x16x32_bf16 v[64:67], a[192:195], v[208:211], v[92:95]\n"
"ds_read_b64_tr_b16 a[164:165], v248 offset:32768\n"
"v_mov_b32 v124, s99\n"
"v_mov_b32 v243, v200\n"
"v_mfma_f32_16x16x32_bf16 v[68:71], a[196:199], v[208:211], v[92:95]\n"
"v_exp_f32 v201, v201\n"
"v_mfma_f32_16x16x32_bf16 v[72:75], a[200:203], v[208:211], v[92:95]\n"
"v_add_f32 v243, v243, v201\n"
"v_cvt_pk_bf16_f32 v212, v200, v201\n"
"v_mfma_f32_16x16x32_bf16 v[76:79], a[204:207], v[208:211], v[92:95]\n"
"v_exp_f32 v202, v202\n"
"v_mfma_f32_16x16x32_bf16 v[80:83], a[208:211], v[208:211], v[92:95]\n"
"ds_read_b64_tr_b16 a[166:167], v248 offset:36864\n"
"v_mov_b32 v125, s99\n"
"v_add_f32 v243, v243, v202\n"
"v_mfma_f32_16x16x32_bf16 v[84:87], a[212:215], v[208:211], v[92:95]\n"
"v_exp_f32 v203, v203\n"
"v_mfma_f32_16x16x32_bf16 v[88:91], a[216:219], v[208:211], v[92:95]\n"
"v_add_f32 v243, v243, v203\n"
"v_cvt_pk_bf16_f32 v213, v202, v203\n"
"v_mfma_f32_16x16x32_bf16 v[92:95], a[220:223], v[208:211], v[92:95]\n"
"v_exp_f32 v204, v204\n"
"v_sub_f32 v236, s71, v236\n"
"v_sub_f32 v237, s71, v237\n"
"v_sub_f32 v238, s71, v238\n"
"v_sub_f32 v239, s71, v239\n"
"v_mfma_f32_16x16x32_bf16 v[192:195], a[128:131], a[64:67], v[232:235]\n"
"ds_read_b64_tr_b16 a[168:169], v247 offset:33280\n"
"v_mov_b32 v126, s99\n"
"v_add_f32 v243, v243, v204\n"
"v_mfma_f32_16x16x32_bf16 v[192:195], a[132:135], a[68:71], v[192:195]\n"
"v_exp_f32 v205, v205\n"
"v_mfma_f32_16x16x32_bf16 v[192:195], a[136:139], a[72:75], v[192:195]\n"
"v_add_f32 v243, v243, v205\n"
"v_cvt_pk_bf16_f32 v214, v204, v205\n"
"v_mfma_f32_16x16x32_bf16 v[192:195], a[140:143], a[76:79], v[192:195]\n"
"v_exp_f32 v206, v206\n"
"v_mfma_f32_16x16x32_bf16 v[196:199], a[144:147], a[64:67], v[232:235]\n"
"ds_read_b64_tr_b16 a[170:171], v247 offset:37376\n"
"v_mov_b32 v127, s99\n"
"v_add_f32 v243, v243, v206\n"
"v_mfma_f32_16x16x32_bf16 v[196:199], a[148:151], a[68:71], v[196:199]\n"
"v_exp_f32 v207, v207\n"
"v_mfma_f32_16x16x32_bf16 v[196:199], a[152:155], a[72:75], v[196:199]\n"
"v_add_f32 v243, v243, v207\n"
"v_cvt_pk_bf16_f32 v215, v206, v207\n"
"v_mfma_f32_16x16x32_bf16 v[196:199], a[156:159], a[76:79], v[196:199]\n"
"v_exp_f32 v192, v192\n"
"v_mfma_f32_16x16x32_bf16 v[96:99], a[192:195], v[212:215], v[124:127]\n"
"ds_read_b64_tr_b16 a[172:173], v248 offset:33280\n"
"v_mov_b32 v156, s99\n"
"v_mov_b32 v244, v192\n"
"v_mfma_f32_16x16x32_bf16 v[100:103], a[196:199], v[212:215], v[124:127]\n"
"v_exp_f32 v193, v193\n"
"v_mfma_f32_16x16x32_bf16 v[104:107], a[200:203], v[212:215], v[124:127]\n"
"v_add_f32 v244, v244, v193\n"
"v_cvt_pk_bf16_f32 v208, v192, v193\n"
"v_mfma_f32_16x16x32_bf16 v[108:111], a[204:207], v[212:215], v[124:127]\n"
"v_exp_f32 v194, v194\n"
"v_mfma_f32_16x16x32_bf16 v[112:115], a[208:211], v[212:215], v[124:127]\n"
"ds_read_b64_tr_b16 a[174:175], v248 offset:37376\n"
"v_mov_b32 v157, s99\n"
"v_add_f32 v244, v244, v194\n"
"v_mfma_f32_16x16x32_bf16 v[116:119], a[212:215], v[212:215], v[124:127]\n"
"v_exp_f32 v195, v195\n"
"v_mfma_f32_16x16x32_bf16 v[120:123], a[216:219], v[212:215], v[124:127]\n"
"v_add_f32 v244, v244, v195\n"
"v_cvt_pk_bf16_f32 v209, v194, v195\n"
"v_mfma_f32_16x16x32_bf16 v[124:127], a[220:223], v[212:215], v[124:127]\n"
"v_exp_f32 v196, v196\n"
"v_mfma_f32_16x16x32_bf16 v[200:203], a[128:131], a[80:83], v[236:239]\n"
"ds_read_b64_tr_b16 a[176:177], v247 offset:33792\n"
"v_mov_b32 v158, s99\n"
"v_add_f32 v244, v244, v196\n"
"v_mfma_f32_16x16x32_bf16 v[200:203], a[132:135], a[84:87], v[200:203]\n"
"v_exp_f32 v197, v197\n"
"v_mfma_f32_16x16x32_bf16 v[200:203], a[136:139], a[88:91], v[200:203]\n"
"v_add_f32 v244, v244, v197\n"
"v_cvt_pk_bf16_f32 v210, v196, v197\n"
"v_mfma_f32_16x16x32_bf16 v[200:203], a[140:143], a[92:95], v[200:203]\n"
"v_exp_f32 v198, v198\n"
"v_mfma_f32_16x16x32_bf16 v[204:207], a[144:147], a[80:83], v[236:239]\n"
"ds_read_b64_tr_b16 a[178:179], v247 offset:37888\n"
"v_mov_b32 v159, s99\n"
"v_add_f32 v244, v244, v198\n"
"v_mfma_f32_16x16x32_bf16 v[204:207], a[148:151], a[84:87], v[204:207]\n"
"v_exp_f32 v199, v199\n"
"v_mfma_f32_16x16x32_bf16 v[204:207], a[152:155], a[88:91], v[204:207]\n"
"v_add_f32 v244, v244, v199\n"
"v_cvt_pk_bf16_f32 v211, v198, v199\n"
"v_mfma_f32_16x16x32_bf16 v[204:207], a[156:159], a[92:95], v[204:207]\n"
"v_exp_f32 v200, v200\n"
"v_mfma_f32_16x16x32_bf16 v[128:131], a[192:195], v[208:211], v[156:159]\n"
"ds_read_b64_tr_b16 a[180:181], v248 offset:33792\n"
"v_mov_b32 v188, s99\n"
"v_mov_b32 v245, v200\n"
"v_mfma_f32_16x16x32_bf16 v[132:135], a[196:199], v[208:211], v[156:159]\n"
"v_exp_f32 v201, v201\n"
"v_mfma_f32_16x16x32_bf16 v[136:139], a[200:203], v[208:211], v[156:159]\n"
"v_add_f32 v245, v245, v201\n"
"v_cvt_pk_bf16_f32 v212, v200, v201\n"
"v_mfma_f32_16x16x32_bf16 v[140:143], a[204:207], v[208:211], v[156:159]\n"
"v_exp_f32 v202, v202\n"
"v_mfma_f32_16x16x32_bf16 v[144:147], a[208:211], v[208:211], v[156:159]\n"
"ds_read_b64_tr_b16 a[182:183], v248 offset:37888\n"
"v_mov_b32 v189, s99\n"
"v_add_f32 v245, v245, v202\n"
"v_mfma_f32_16x16x32_bf16 v[148:151], a[212:215], v[208:211], v[156:159]\n"
"v_exp_f32 v203, v203\n"
"v_mfma_f32_16x16x32_bf16 v[152:155], a[216:219], v[208:211], v[156:159]\n"
"v_add_f32 v245, v245, v203\n"
"v_cvt_pk_bf16_f32 v213, v202, v203\n"
"v_mfma_f32_16x16x32_bf16 v[156:159], a[220:223], v[208:211], v[156:159]\n"
"s_waitcnt lgkmcnt(2)\n"
"v_exp_f32 v204, v204\n"
"v_mfma_f32_16x16x32_bf16 v[192:195], a[96:99], a[0:3], v[216:219]\n"
"ds_read_b64_tr_b16 a[184:185], v247 offset:34304\n"
"v_mov_b32 v190, s99\n"
"v_add_f32 v245, v245, v204\n"
"v_mfma_f32_16x16x32_bf16 v[192:195], a[100:103], a[4:7], v[192:195]\n"
"v_exp_f32 v205, v205\n"
"v_mfma_f32_16x16x32_bf16 v[192:195], a[104:107], a[8:11], v[192:195]\n"
"v_add_f32 v245, v245, v205\n"
"v_cvt_pk_bf16_f32 v214, v204, v205\n"
"v_mfma_f32_16x16x32_bf16 v[192:195], a[108:111], a[12:15], v[192:195]\n"
"v_exp_f32 v206, v206\n"
"v_mfma_f32_16x16x32_bf16 v[196:199], a[112:115], a[0:3], v[216:219]\n"
"ds_read_b64_tr_b16 a[186:187], v247 offset:38400\n"
"v_mov_b32 v191, s99\n"
"v_add_f32 v245, v245, v206\n"
"v_mfma_f32_16x16x32_bf16 v[196:199], a[116:119], a[4:7], v[196:199]\n"
"v_exp_f32 v207, v207\n"
"v_mfma_f32_16x16x32_bf16 v[196:199], a[120:123], a[8:11], v[196:199]\n"
"v_add_f32 v245, v245, v207\n"
"v_cvt_pk_bf16_f32 v215, v206, v207\n"
"v_mfma_f32_16x16x32_bf16 v[196:199], a[124:127], a[12:15], v[196:199]\n"
"v_exp_f32 v192, v192\n"
"v_mfma_f32_16x16x32_bf16 v[160:163], a[192:195], v[212:215], v[188:191]\n"
"ds_read_b64_tr_b16 a[188:189], v248 offset:34304\n"
"v_add_f32 v240, v240, v192\n"
"v_mfma_f32_16x16x32_bf16 v[164:167], a[196:199], v[212:215], v[188:191]\n"
"v_exp_f32 v193, v193\n"
"v_mfma_f32_16x16x32_bf16 v[168:171], a[200:203], v[212:215], v[188:191]\n"
"v_add_f32 v240, v240, v193\n"
"v_cvt_pk_bf16_f32 v208, v192, v193\n"
"v_mfma_f32_16x16x32_bf16 v[172:175], a[204:207], v[212:215], v[188:191]\n"
"v_exp_f32 v194, v194\n"
"v_mfma_f32_16x16x32_bf16 v[176:179], a[208:211], v[212:215], v[188:191]\n"
"ds_read_b64_tr_b16 a[190:191], v248 offset:38400\n"
"v_add_f32 v240, v240, v194\n"
"v_mfma_f32_16x16x32_bf16 v[180:183], a[212:215], v[212:215], v[188:191]\n"
"v_exp_f32 v195, v195\n"
"v_mfma_f32_16x16x32_bf16 v[184:187], a[216:219], v[212:215], v[188:191]\n"
"v_add_f32 v240, v240, v195\n"
"v_cvt_pk_bf16_f32 v209, v194, v195\n"
"v_mfma_f32_16x16x32_bf16 v[188:191], a[220:223], v[212:215], v[188:191]\n"
"s_branch LMA2%=\n"
".p2align 12\n"
".fill 272, 4, 0xbf800000\n"
"LroleB%=:\n"
"v_and_b32 v32, 48, v249\n"
"s_waitcnt vmcnt(16)\n"
"ds_read_b128 a[8:11], v254 offset:1024\n"
"ds_read_b128 a[12:15], v254 offset:1536\n"
"ds_read_b128 v[8:11], v254 offset:0\n"
"ds_read_b128 v[12:15], v254 offset:512\n"
"s_waitcnt lgkmcnt(3)\n"
"v_mfma_f32_32x32x16_bf16 v[16:31], a[224:227], a[8:11], 0\n"
"s_waitcnt lgkmcnt(1)\n"
"v_lshlrev_b32 v0, 16, v8\n"
"v_and_b32 v1, s64, v8\n"
"v_lshlrev_b32 v2, 16, v9\n"
"v_and_b32 v3, s64, v9\n"
"v_mfma_f32_32x32x16_bf16 v[16:31], a[228:231], a[8:11], v[16:31]\n"
"v_lshlrev_b32 v4, 16, v10\n"
"v_and_b32 v5, s64, v10\n"
"v_lshlrev_b32 v6, 16, v11\n"
"v_and_b32 v7, s64, v11\n"
"v_mfma_f32_32x32x16_bf16 v[16:31], a[232:235], a[12:15], v[16:31]\n"
"s_waitcnt lgkmcnt(0)\n"
"v_lshlrev_b32 v8, 16, v12\n"
"v_and_b32 v9, s64, v12\n"
"v_lshlrev_b32 v10, 16, v13\n"
"v_and_b32 v11, s64, v13\n"
"v_mfma_f32_32x32x16_bf16 v[16:31], a[236:239], a[12:15], v[16:31]\n"
"v_lshlrev_b32 v12, 16, v14\n"
"v_and_b32 v13, s64, v14\n"
"v_lshlrev_b32 v14, 16, v15\n"
"v_and_b32 v15, s64, v15\n"
"v_pk_mul_f32 v[0:1], v[0:1], s[52:53]\n"
"v_pk_mul_f32 v[2:3], v[2:3], s[52:53]\n"
"v_pk_mul_f32 v[4:5], v[4:5], s[52:53]\n"
"v_pk_mul_f32 v[6:7], v[6:7], s[52:53]\n"
"v_pk_mul_f32 v[8:9], v[8:9], s[52:53]\n"
"v_pk_mul_f32 v[10:11], v[10:11], s[52:53]\n"
"v_pk_mul_f32 v[12:13], v[12:13], s[52:53]\n"
"v_pk_mul_f32 v[14:15], v[14:15], s[52:53]\n"
"v_cvt_pk_bf16_f32 v0, v0, v1\n"
"v_cvt_pk_bf16_f32 v2, v2, v3\n"
"v_cvt_pk_bf16_f32 v4, v4, v5\n"
"v_cvt_pk_bf16_f32 v6, v6, v7\n"
"v_accvgpr_write_b32 a0, v0\n"
"v_accvgpr_write_b32 a1, v2\n"
"v_accvgpr_write_b32 a2, v4\n"
"v_accvgpr_write_b32 a3, v6\n"
"v_cvt_pk_bf16_f32 v8, v8, v9\n"
"v_cvt_pk_bf16_f32 v10, v10, v11\n"
"v_cvt_pk_bf16_f32 v12, v12, v13\n"
"v_cvt_pk_bf16_f32 v14, v14, v15\n"
"v_accvgpr_write_b32 a4, v8\n"
"v_accvgpr_write_b32 a5, v10\n"
"v_accvgpr_write_b32 a6, v12\n"
"v_accvgpr_write_b32 a7, v14\n"
"v_cvt_pk_bf16_f32 v16, v16, v17\n"
"v_cvt_pk_bf16_f32 v18, v18, v19\n"
"v_cvt_pk_bf16_f32 v20, v20, v21\n"
"v_cvt_pk_bf16_f32 v22, v22, v23\n"
"v_accvgpr_write_b32 a8, v16\n"
"v_accvgpr_write_b32 a9, v18\n"
"v_accvgpr_write_b32 a10, v20\n"
"v_accvgpr_write_b32 a11, v22\n"
"v_cvt_pk_bf16_f32 v24, v24, v25\n"
"v_cvt_pk_bf16_f32 v26, v26, v27\n"
"v_cvt_pk_bf16_f32 v28, v28, v29\n"
"v_cvt_pk_bf16_f32 v30, v30, v31\n"
"v_accvgpr_write_b32 a12, v24\n"
"v_accvgpr_write_b32 a13, v26\n"
"v_accvgpr_write_b32 a14, v28\n"
"v_accvgpr_write_b32 a15, v30\n"
"s_waitcnt vmcnt(12)\n"
"s_barrier\n"
"ds_read_b128 v[64:67], v32 offset:0\n"
"ds_read_b128 v[68:71], v32 offset:512\n"
"ds_read_b128 v[72:75], v32 offset:1024\n"
"ds_read_b128 v[76:79], v32 offset:1536\n"
"ds_read_b128 a[96:99], v246 offset:0\n"
"ds_read_b128 a[100:103], v246 offset:512\n"
"ds_read_b128 a[104:107], v246 offset:1024\n"
"ds_read_b128 a[108:111], v246 offset:1536\n"
"ds_read_b128 a[112:115], v246 offset:4096\n"
"ds_read_b128 a[116:119], v246 offset:4608\n"
"ds_read_b128 a[120:123], v246 offset:5120\n"
"ds_read_b128 a[124:127], v246 offset:5632\n"
"ds_read_b128 a[128:131], v246 offset:8192\n"
"ds_read_b128 a[132:135], v246 offset:8704\n"
"ds_read_b128 a[136:139], v246 offset:9216\n"
"ds_read_b128 a[140:143], v246 offset:9728\n"
"ds_read_b128 a[144:147], v246 offset:12288\n"
"ds_read_b128 a[148:151], v246 offset:12800\n"
"ds_read_b128 a[152:155], v246 offset:13312\n"
"ds_read_b128 a[156:159], v246 offset:13824\n"
"s_waitcnt lgkmcnt(15)\n"
"v_xor_b32 v64, 2147516416, v64\n"
"v_xor_b32 v65, 2147516416, v65\n"
"v_xor_b32 v66, 2147516416, v66\n"
"v_xor_b32 v67, 2147516416, v67\n"
"v_accvgpr_write_b32 a240, v64\n"
"v_accvgpr_write_b32 a241, v65\n"
"v_accvgpr_write_b32 a242, v66\n"
"v_accvgpr_write_b32 a243, v67\n"
"v_xor_b32 v68, 2147516416, v68\n"
"v_xor_b32 v69, 2147516416, v69\n"
"v_xor_b32 v70, 2147516416, v70\n"
"v_xor_b32 v71, 2147516416, v71\n"
"v_mfma_f32_16x16x32_bf16 v[216:219], a[240:243], a[0:3], v[216:219]\n"
"v_accvgpr_write_b32 a244, v68\n"
"v_accvgpr_write_b32 a245, v69\n"
"v_accvgpr_write_b32 a246, v70\n"
"v_accvgpr_write_b32 a247, v71\n"
"v_xor_b32 v72, 2147516416, v72\n"
"v_xor_b32 v73, 2147516416, v73\n"
"v_xor_b32 v74, 2147516416, v74\n"
"v_xor_b32 v75, 2147516416, v75\n"
"v_mfma_f32_16x16x32_bf16 v[216:219], a[244:247], a[4:7], v[216:219]\n"
"v_accvgpr_write_b32 a248, v72\n"
"v_accvgpr_write_b32 a249, v73\n"
"v_accvgpr_write_b32 a250, v74\n"
"v_accvgpr_write_b32 a251, v75\n"
"v_xor_b32 v76, 2147516416, v76\n"
"v_xor_b32 v77, 2147516416, v77\n"
"v_xor_b32 v78, 2147516416, v78\n"
"v_xor_b32 v79, 2147516416, v79\n"
"v_mfma_f32_16x16x32_bf16 v[216:219], a[248:251], a[8:11], v[216:219]\n"
"v_accvgpr_write_b32 a252, v76\n"
"v_accvgpr_write_b32 a253, v77\n"
"v_accvgpr_write_b32 a254, v78\n"
"v_accvgpr_write_b32 a255, v79\n"
"s_waitcnt vmcnt(8)\n"
"s_barrier\n"
"v_mfma_f32_16x16x32_bf16 v[216:219], a[252:255], a[12:15], v[216:219]\n"
"v_mfma_f32_16x16x32_bf16 v[192:195], a[96:99], a[0:3], v[216:219]\n"
"s_waitcnt lgkmcnt(0)\n"
"ds_read_b64_tr_b16 a[160:161], v247 offset:32768\n"
"ds_read_b64_tr_b16 a[162:163], v247 offset:36864\n"
"v_mfma_f32_16x16x32_bf16 v[192:195], a[100:103], a[4:7], v[192:195]\n"
"ds_read_b64_tr_b16 a[164:165], v248 offset:32768\n"
"ds_read_b64_tr_b16 a[166:167], v248 offset:36864\n"
"v_mfma_f32_16x16x32_bf16 v[192:195], a[104:107], a[8:11], v[192:195]\n"
"ds_read_b64_tr_b16 a[168:169], v247 offset:33280\n"
"ds_read_b64_tr_b16 a[170:171], v247 offset:37376\n"
"v_mfma_f32_16x16x32_bf16 v[192:195], a[108:111], a[12:15], v[192:195]\n"
"ds_read_b64_tr_b16 a[172:173], v248 offset:33280\n"
"ds_read_b64_tr_b16 a[174:175], v248 offset:37376\n"
"v_mfma_f32_16x16x32_bf16 v[196:199], a[112:115], a[0:3], v[216:219]\n"
"ds_read_b64_tr_b16 a[176:177], v247 offset:33792\n"
"ds_read_b64_tr_b16 a[178:179], v247 offset:37888\n"
"v_mfma_f32_16x16x32_bf16 v[196:199], a[116:119], a[4:7], v[196:199]\n"
"ds_read_b64_tr_b16 a[180:181], v248 offset:33792\n"
"ds_read_b64_tr_b16 a[182:183], v248 offset:37888\n"
"v_mfma_f32_16x16x32_bf16 v[196:199], a[120:123], a[8:11], v[196:199]\n"
"ds_read_b64_tr_b16 a[184:185], v247 offset:34304\n"
"ds_read_b64_tr_b16 a[186:187], v247 offset:38400\n"
"v_mfma_f32_16x16x32_bf16 v[196:199], a[124:127], a[12:15], v[196:199]\n"
"ds_read_b64_tr_b16 a[188:189], v248 offset:34304\n"
"ds_read_b64_tr_b16 a[190:191], v248 offset:38400\n"
"v_exp_f32 v192, v192\n"
"ds_read_b64_tr_b16 a[192:193], v247 offset:40960\n"
"ds_read_b64_tr_b16 a[194:195], v247 offset:45056\n"
"ds_read_b64_tr_b16 a[196:197], v248 offset:40960\n"
"ds_read_b64_tr_b16 a[198:199], v248 offset:45056\n"
"ds_read_b64_tr_b16 a[200:201], v247 offset:41472\n"
"ds_read_b64_tr_b16 a[202:203], v247 offset:45568\n"
"ds_read_b64_tr_b16 a[204:205], v248 offset:41472\n"
"ds_read_b64_tr_b16 a[206:207], v248 offset:45568\n"
"v_mov_b32 v240, v192\n"
"s_waitcnt lgkmcnt(15)\n"
"v_mfma_f32_16x16x32_bf16 v[0:3], a[160:163], a[160:163], 0\n"
"v_mfma_f32_16x16x32_bf16 v[0:3], a[164:167], a[164:167], v[0:3]\n"
"v_exp_f32 v193, v193\n"
"s_add_u32 m0, s62, 12288\n"
"s_add_u32 s59, s61, 12288\n"
"buffer_load_dwordx4 v252, s[44:47], s59 offen nt lds\n"
"buffer_load_dwordx4 v253, s[44:47], s59 offen offset:2048 nt lds\n"
"s_add_u32 m0, s62, 13312\n"
"s_add_u32 s59, s61, 12416\n"
"buffer_load_dwordx4 v252, s[44:47], s59 offen nt lds\n"
"buffer_load_dwordx4 v253, s[44:47], s59 offen offset:2048 nt lds\n"
"v_add_f32 v240, v240, v193\n"
"v_cvt_pk_bf16_f32 v208, v192, v193\n"
"v_exp_f32 v194, v194\n"
"s_nop 0\n"
"v_add_f32 v240, v240, v194\n"
"v_readlane_b32 s98, v0, 0\n"
"v_readlane_b32 s72, v1, 21\n"
"v_readlane_b32 s73, v2, 42\n"
"v_readlane_b32 s60, v3, 63\n"
"v_exp_f32 v195, v195\n"
"s_nop 0\n"
"v_add_f32 v240, v240, v195\n"
"v_cvt_pk_bf16_f32 v209, v194, v195\n"
"s_min_u32 s98, s98, s72\n"
"s_min_u32 s73, s73, s60\n"
"s_min_u32 s98, s98, s73\n"
"s_lshr_b32 s98, s98, 23\n"
"s_add_u32 s72, s98, 45\n"
"s_lshr_b32 s72, s72, 1\n"
"s_lshl_b32 s72, s72, 23\n"
"s_or_b32 s72, s72, 4194304\n"
"s_sub_u32 s98, s98, 33\n"
"s_mov_b32 s73, 671088640\n"
"s_cmp_lt_u32 s98, 200\n"
"s_cselect_b32 s98, s72, 0\n"
"s_cselect_b32 s63, 1568669696, s73\n"
"v_exp_f32 v196, v196\n"
"v_mfma_f32_16x16x32_bf16 v[200:203], a[128:131], a[0:3], v[216:219]\n"
"ds_read_b64_tr_b16 a[208:209], v247 offset:41984\n"
"v_add_f32 v240, v240, v196\n"
"v_mfma_f32_16x16x32_bf16 v[200:203], a[132:135], a[4:7], v[200:203]\n"
"ds_read_b64_tr_b16 a[210:211], v247 offset:46080\n"
"v_exp_f32 v197, v197\n"
"v_mfma_f32_16x16x32_bf16 v[200:203], a[136:139], a[8:11], v[200:203]\n"
"ds_read_b64_tr_b16 a[212:213], v248 offset:41984\n"
"v_add_f32 v240, v240, v197\n"
"v_cvt_pk_bf16_f32 v210, v196, v197\n"
"v_mfma_f32_16x16x32_bf16 v[200:203], a[140:143], a[12:15], v[200:203]\n"
"ds_read_b64_tr_b16 a[214:215], v248 offset:46080\n"
"v_exp_f32 v198, v198\n"
"v_mfma_f32_16x16x32_bf16 v[204:207], a[144:147], a[0:3], v[216:219]\n"
"ds_read_b64_tr_b16 a[216:217], v247 offset:42496\n"
"v_mov_b32 v28, s98\n"
"v_mov_b32 v29, s98\n"
"v_add_f32 v240, v240, v198\n"
"v_mfma_f32_16x16x32_bf16 v[204:207], a[148:151], a[4:7], v[204:207]\n"
"ds_read_b64_tr_b16 a[218:219], v247 offset:46592\n"
"v_mov_b32 v30, s98\n"
"v_mov_b32 v31, s98\n"
"v_exp_f32 v199, v199\n"
"v_mfma_f32_16x16x32_bf16 v[204:207], a[152:155], a[8:11], v[204:207]\n"
"ds_read_b64_tr_b16 a[220:221], v248 offset:42496\n"
"v_add_f32 v240, v240, v199\n"
"v_cvt_pk_bf16_f32 v211, v198, v199\n"
"v_mfma_f32_16x16x32_bf16 v[204:207], a[156:159], a[12:15], v[204:207]\n"
"s_waitcnt vmcnt(8)\n"
"ds_read_b64_tr_b16 a[222:223], v248 offset:46592\n"
"v_exp_f32 v200, v200\n"
"v_mfma_f32_16x16x32_bf16 v[0:3], a[160:163], v[208:211], v[28:31]\n"
"ds_read_b128 a[24:27], v254 offset:5120\n"
"v_add_f32 v240, v240, v200\n"
"v_mfma_f32_16x16x32_bf16 v[4:7], a[164:167], v[208:211], v[28:31]\n"
"ds_read_b128 a[28:31], v254 offset:5632\n"
"v_exp_f32 v201, v201\n"
"v_mfma_f32_16x16x32_bf16 v[8:11], a[168:171], v[208:211], v[28:31]\n"
"ds_read_b128 v[40:43], v254 offset:4096\n"
"v_add_f32 v240, v240, v201\n"
"v_cvt_pk_bf16_f32 v212, v200, v201\n"
"v_mfma_f32_16x16x32_bf16 v[12:15], a[172:175], v[208:211], v[28:31]\n"
"s_waitcnt lgkmcnt(6)\n"
"ds_read_b128 v[44:47], v254 offset:4608\n"
"v_exp_f32 v202, v202\n"
"v_mfma_f32_16x16x32_bf16 v[16:19], a[176:179], v[208:211], v[28:31]\n"
"v_add_f32 v240, v240, v202\n"
"v_mfma_f32_16x16x32_bf16 v[20:23], a[180:183], v[208:211], v[28:31]\n"
"v_exp_f32 v203, v203\n"
"v_mfma_f32_16x16x32_bf16 v[24:27], a[184:187], v[208:211], v[28:31]\n"
"v_add_f32 v240, v240, v203\n"
"v_cvt_pk_bf16_f32 v213, v202, v203\n"
"v_mfma_f32_16x16x32_bf16 v[28:31], a[188:191], v[208:211], v[28:31]\n"
"s_waitcnt lgkmcnt(3)\n"
"v_exp_f32 v204, v204\n"
"v_mfma_f32_32x32x16_bf16 v[48:63], a[224:227], a[24:27], 0\n"
"s_waitcnt lgkmcnt(1)\n"
"v_lshlrev_b32 v32, 16, v40\n"
"v_and_b32 v33, s64, v40\n"
"v_lshlrev_b32 v34, 16, v41\n"
"v_and_b32 v35, s64, v41\n"
"v_mfma_f32_32x32x16_bf16 v[48:63], a[228:231], a[24:27], v[48:63]\n"
"v_lshlrev_b32 v36, 16, v42\n"
"v_and_b32 v37, s64, v42\n"
"v_lshlrev_b32 v38, 16, v43\n"
"v_and_b32 v39, s64, v43\n"
"v_mfma_f32_32x32x16_bf16 v[48:63], a[232:235], a[28:31], v[48:63]\n"
"s_waitcnt lgkmcnt(0)\n"
"v_lshlrev_b32 v40, 16, v44\n"
"v_and_b32 v41, s64, v44\n"
"v_lshlrev_b32 v42, 16, v45\n"
"v_and_b32 v43, s64, v45\n"
"v_mfma_f32_32x32x16_bf16 v[48:63], a[236:239], a[28:31], v[48:63]\n"
"v_lshlrev_b32 v44, 16, v46\n"
"v_and_b32 v45, s64, v46\n"
"v_lshlrev_b32 v46, 16, v47\n"
"v_and_b32 v47, s64, v47\n"
"v_pk_mul_f32 v[32:33], v[32:33], s[52:53]\n"
"v_pk_mul_f32 v[34:35], v[34:35], s[52:53]\n"
"v_pk_mul_f32 v[36:37], v[36:37], s[52:53]\n"
"v_pk_mul_f32 v[38:39], v[38:39], s[52:53]\n"
"v_pk_mul_f32 v[40:41], v[40:41], s[52:53]\n"
"v_pk_mul_f32 v[42:43], v[42:43], s[52:53]\n"
"v_pk_mul_f32 v[44:45], v[44:45], s[52:53]\n"
"v_pk_mul_f32 v[46:47], v[46:47], s[52:53]\n"
"v_cvt_pk_bf16_f32 v32, v32, v33\n"
"v_cvt_pk_bf16_f32 v34, v34, v35\n"
"v_cvt_pk_bf16_f32 v36, v36, v37\n"
"v_cvt_pk_bf16_f32 v38, v38, v39\n"
"v_accvgpr_write_b32 a16, v32\n"
"v_accvgpr_write_b32 a17, v34\n"
"v_accvgpr_write_b32 a18, v36\n"
"v_accvgpr_write_b32 a19, v38\n"
"v_cvt_pk_bf16_f32 v40, v40, v41\n"
"v_cvt_pk_bf16_f32 v42, v42, v43\n"
"v_mfma_f32_16x16x32_bf16 v[220:223], a[240:243], a[16:19], v[220:223]\n"
"v_cvt_pk_bf16_f32 v44, v44, v45\n"
"v_cvt_pk_bf16_f32 v46, v46, v47\n"
"v_accvgpr_write_b32 a20, v40\n"
"v_accvgpr_write_b32 a21, v42\n"
"v_accvgpr_write_b32 a22, v44\n"
"v_accvgpr_write_b32 a23, v46\n"
"v_cvt_pk_bf16_f32 v48, v48, v49\n"
"v_cvt_pk_bf16_f32 v50, v50, v51\n"
"v_mfma_f32_16x16x32_bf16 v[220:223], a[244:247], a[20:23], v[220:223]\n"
"v_cvt_pk_bf16_f32 v52, v52, v53\n"
"v_cvt_pk_bf16_f32 v54, v54, v55\n"
"v_accvgpr_write_b32 a24, v48\n"
"v_accvgpr_write_b32 a25, v50\n"
"v_accvgpr_write_b32 a26, v52\n"
"v_accvgpr_write_b32 a27, v54\n"
"v_cvt_pk_bf16_f32 v56, v56, v57\n"
"v_cvt_pk_bf16_f32 v58, v58, v59\n"
"v_mfma_f32_16x16x32_bf16 v[220:223], a[248:251], a[24:27], v[220:223]\n"
"v_cvt_pk_bf16_f32 v60, v60, v61\n"
"v_cvt_pk_bf16_f32 v62, v62, v63\n"
"v_accvgpr_write_b32 a28, v56\n"
"v_accvgpr_write_b32 a29, v58\n"
"v_accvgpr_write_b32 a30, v60\n"
"v_accvgpr_write_b32 a31, v62\n"
"s_nop 1\n"
"v_mfma_f32_16x16x32_bf16 v[220:223], a[252:255], a[28:31], v[220:223]\n"
"v_mfma_f32_16x16x32_bf16 v[192:195], a[96:99], a[16:19], v[220:223]\n"
"s_add_u32 m0, s62, 16384\n"
"s_add_u32 s59, s61, 16384\n"
"buffer_load_dwordx4 v252, s[44:47], s59 offen nt lds\n"
"v_add_f32 v240, v240, v204\n"
"v_mfma_f32_16x16x32_bf16 v[192:195], a[100:103], a[20:23], v[192:195]\n"
"v_exp_f32 v205, v205\n"
"v_mfma_f32_16x16x32_bf16 v[192:195], a[104:107], a[24:27], v[192:195]\n"
"v_add_f32 v240, v240, v205\n"
"v_cvt_pk_bf16_f32 v214, v204, v205\n"
"v_mfma_f32_16x16x32_bf16 v[192:195], a[108:111], a[28:31], v[192:195]\n"
"v_exp_f32 v206, v206\n"
"v_mfma_f32_16x16x32_bf16 v[196:199], a[112:115], a[16:19], v[220:223]\n"
"s_add_u32 m0, s62, 16384\n"
"s_add_u32 s59, s61, 16384\n"
"buffer_load_dwordx4 v253, s[44:47], s59 offen offset:2048 nt lds\n"
"v_add_f32 v240, v240, v206\n"
"v_mfma_f32_16x16x32_bf16 v[196:199], a[116:119], a[20:23], v[196:199]\n"
"v_exp_f32 v207, v207\n"
"v_mfma_f32_16x16x32_bf16 v[196:199], a[120:123], a[24:27], v[196:199]\n"
"v_add_f32 v240, v240, v207\n"
"v_cvt_pk_bf16_f32 v215, v206, v207\n"
"v_mfma_f32_16x16x32_bf16 v[196:199], a[124:127], a[28:31], v[196:199]\n"
"v_exp_f32 v192, v192\n"
"v_mfma_f32_16x16x32_bf16 v[0:3], a[192:195], v[212:215], v[0:3]\n"
"s_add_u32 m0, s62, 17408\n"
"s_add_u32 s59, s61, 16512\n"
"buffer_load_dwordx4 v252, s[44:47], s59 offen nt lds\n"
"v_mov_b32 v60, s98\n"
"v_mov_b32 v241, v192\n"
"v_mfma_f32_16x16x32_bf16 v[4:7], a[196:199], v[212:215], v[4:7]\n"
"v_exp_f32 v193, v193\n"
"v_mfma_f32_16x16x32_bf16 v[8:11], a[200:203], v[212:215], v[8:11]\n"
"v_add_f32 v241, v241, v193\n"
"v_cvt_pk_bf16_f32 v208, v192, v193\n"
"v_mfma_f32_16x16x32_bf16 v[12:15], a[204:207], v[212:215], v[12:15]\n"
"v_exp_f32 v194, v194\n"
"v_mfma_f32_16x16x32_bf16 v[16:19], a[208:211], v[212:215], v[16:19]\n"
"s_add_u32 m0, s62, 17408\n"
"s_add_u32 s59, s61, 16512\n"
"buffer_load_dwordx4 v253, s[44:47], s59 offen offset:2048 nt lds\n"
"v_mov_b32 v61, s98\n"
"v_add_f32 v241, v241, v194\n"
"v_mfma_f32_16x16x32_bf16 v[20:23], a[212:215], v[212:215], v[20:23]\n"
"v_exp_f32 v195, v195\n"
"v_mfma_f32_16x16x32_bf16 v[24:27], a[216:219], v[212:215], v[24:27]\n"
"v_add_f32 v241, v241, v195\n"
"v_cvt_pk_bf16_f32 v209, v194, v195\n"
"v_mfma_f32_16x16x32_bf16 v[28:31], a[220:223], v[212:215], v[28:31]\n"
"v_exp_f32 v196, v196\n"
"v_mfma_f32_16x16x32_bf16 v[200:203], a[128:131], a[16:19], v[220:223]\n"
"v_mov_b32 v62, s98\n"
"v_add_f32 v241, v241, v196\n"
"v_mfma_f32_16x16x32_bf16 v[200:203], a[132:135], a[20:23], v[200:203]\n"
"v_exp_f32 v197, v197\n"
"v_mfma_f32_16x16x32_bf16 v[200:203], a[136:139], a[24:27], v[200:203]\n"
"v_add_f32 v241, v241, v197\n"
"v_cvt_pk_bf16_f32 v210, v196, v197\n"
"v_mfma_f32_16x16x32_bf16 v[200:203], a[140:143], a[28:31], v[200:203]\n"
"v_exp_f32 v198, v198\n"
"v_mfma_f32_16x16x32_bf16 v[204:207], a[144:147], a[16:19], v[220:223]\n"
"v_mov_b32 v63, s98\n"
"v_add_f32 v241, v241, v198\n"
"v_mfma_f32_16x16x32_bf16 v[204:207], a[148:151], a[20:23], v[204:207]\n"
"v_exp_f32 v199, v199\n"
"v_mfma_f32_16x16x32_bf16 v[204:207], a[152:155], a[24:27], v[204:207]\n"
"v_add_f32 v241, v241, v199\n"
"v_cvt_pk_bf16_f32 v211, v198, v199\n"
"v_mfma_f32_16x16x32_bf16 v[204:207], a[156:159], a[28:31], v[204:207]\n"
"s_waitcnt vmcnt(8)\n"
"v_exp_f32 v200, v200\n"
"v_mfma_f32_16x16x32_bf16 v[32:35], a[160:163], v[208:211], v[60:63]\n"
"ds_read_b128 a[40:43], v254 offset:9216\n"
"v_add_f32 v241, v241, v200\n"
"v_mfma_f32_16x16x32_bf16 v[36:39], a[164:167], v[208:211], v[60:63]\n"
"ds_read_b128 a[44:47], v254 offset:9728\n"
"v_exp_f32 v201, v201\n"
"v_mfma_f32_16x16x32_bf16 v[40:43], a[168:171], v[208:211], v[60:63]\n"
"ds_read_b128 v[72:75], v254 offset:8192\n"
"v_add_f32 v241, v241, v201\n"
"v_cvt_pk_bf16_f32 v212, v200, v201\n"
"v_mfma_f32_16x16x32_bf16 v[44:47], a[172:175], v[208:211], v[60:63]\n"
"ds_read_b128 v[76:79], v254 offset:8704\n"
"v_exp_f32 v202, v202\n"
"v_mfma_f32_16x16x32_bf16 v[48:51], a[176:179], v[208:211], v[60:63]\n"
"v_add_f32 v241, v241, v202\n"
"v_mfma_f32_16x16x32_bf16 v[52:55], a[180:183], v[208:211], v[60:63]\n"
"v_exp_f32 v203, v203\n"
"v_mfma_f32_16x16x32_bf16 v[56:59], a[184:187], v[208:211], v[60:63]\n"
"v_add_f32 v241, v241, v203\n"
"v_cvt_pk_bf16_f32 v213, v202, v203\n"
"v_mfma_f32_16x16x32_bf16 v[60:63], a[188:191], v[208:211], v[60:63]\n"
"s_waitcnt lgkmcnt(3)\n"
"v_exp_f32 v204, v204\n"
"v_mfma_f32_32x32x16_bf16 v[80:95], a[224:227], a[40:43], 0\n"
"s_waitcnt lgkmcnt(1)\n"
"v_lshlrev_b32 v64, 16, v72\n"
"v_and_b32 v65, s64, v72\n"
"v_lshlrev_b32 v66, 16, v73\n"
"v_and_b32 v67, s64, v73\n"
"v_mfma_f32_32x32x16_bf16 v[80:95], a[228:231], a[40:43], v[80:95]\n"
"v_lshlrev_b32 v68, 16, v74\n"
"v_and_b32 v69, s64, v74\n"
"v_lshlrev_b32 v70, 16, v75\n"
"v_and_b32 v71, s64, v75\n"
"v_mfma_f32_32x32x16_bf16 v[80:95], a[232:235], a[44:47], v[80:95]\n"
"s_waitcnt lgkmcnt(0)\n"
"v_lshlrev_b32 v72, 16, v76\n"
"v_and_b32 v73, s64, v76\n"
"v_lshlrev_b32 v74, 16, v77\n"
"v_and_b32 v75, s64, v77\n"
"v_mfma_f32_32x32x16_bf16 v[80:95], a[236:239], a[44:47], v[80:95]\n"
"v_lshlrev_b32 v76, 16, v78\n"
"v_and_b32 v77, s64, v78\n"
"v_lshlrev_b32 v78, 16, v79\n"
"v_and_b32 v79, s64, v79\n"
"v_pk_mul_f32 v[64:65], v[64:65], s[52:53]\n"
"v_pk_mul_f32 v[66:67], v[66:67], s[52:53]\n"
"v_pk_mul_f32 v[68:69], v[68:69], s[52:53]\n"
"v_pk_mul_f32 v[70:71], v[70:71], s[52:53]\n"
"v_pk_mul_f32 v[72:73], v[72:73], s[52:53]\n"
"v_pk_mul_f32 v[74:75], v[74:75], s[52:53]\n"
"v_pk_mul_f32 v[76:77], v[76:77], s[52:53]\n"
"v_pk_mul_f32 v[78:79], v[78:79], s[52:53]\n"
"v_cvt_pk_bf16_f32 v64, v64, v65\n"
"v_cvt_pk_bf16_f32 v66, v66, v67\n"
"v_cvt_pk_bf16_f32 v68, v68, v69\n"
"v_cvt_pk_bf16_f32 v70, v70, v71\n"
"v_accvgpr_write_b32 a32, v64\n"
"v_accvgpr_write_b32 a33, v66\n"
"v_accvgpr_write_b32 a34, v68\n"
"v_accvgpr_write_b32 a35, v70\n"
"v_cvt_pk_bf16_f32 v72, v72, v73\n"
"v_cvt_pk_bf16_f32 v74, v74, v75\n"
"v_mfma_f32_16x16x32_bf16 v[224:227], a[240:243], a[32:35], v[224:227]\n"
"v_cvt_pk_bf16_f32 v76, v76, v77\n"
"v_cvt_pk_bf16_f32 v78, v78, v79\n"
"v_accvgpr_write_b32 a36, v72\n"
"v_accvgpr_write_b32 a37, v74\n"
"v_accvgpr_write_b32 a38, v76\n"
"v_accvgpr_write_b32 a39, v78\n"
"v_cvt_pk_bf16_f32 v80, v80, v81\n"
"v_cvt_pk_bf16_f32 v82, v82, v83\n"
"v_mfma_f32_16x16x32_bf16 v[224:227], a[244:247], a[36:39], v[224:227]\n"
"v_cvt_pk_bf16_f32 v84, v84, v85\n"
"v_cvt_pk_bf16_f32 v86, v86, v87\n"
"v_accvgpr_write_b32 a40, v80\n"
"v_accvgpr_write_b32 a41, v82\n"
"v_accvgpr_write_b32 a42, v84\n"
"v_accvgpr_write_b32 a43, v86\n"
"v_cvt_pk_bf16_f32 v88, v88, v89\n"
"v_cvt_pk_bf16_f32 v90, v90, v91\n"
"v_mfma_f32_16x16x32_bf16 v[224:227], a[248:251], a[40:43], v[224:227]\n"
"v_cvt_pk_bf16_f32 v92, v92, v93\n"
"v_cvt_pk_bf16_f32 v94, v94, v95\n"
"v_accvgpr_write_b32 a44, v88\n"
"v_accvgpr_write_b32 a45, v90\n"
"v_accvgpr_write_b32 a46, v92\n"
"v_accvgpr_write_b32 a47, v94\n"
"s_barrier\n"
"s_nop 0\n"
"v_mfma_f32_16x16x32_bf16 v[224:227], a[252:255], a[44:47], v[224:227]\n"
"v_mfma_f32_16x16x32_bf16 v[192:195], a[96:99], a[32:35], v[224:227]\n"
"v_add_f32 v241, v241, v204\n"
"v_mfma_f32_16x16x32_bf16 v[192:195], a[100:103], a[36:39], v[192:195]\n"
"v_exp_f32 v205, v205\n"
"v_mfma_f32_16x16x32_bf16 v[192:195], a[104:107], a[40:43], v[192:195]\n"
"v_add_f32 v241, v241, v205\n"
"v_cvt_pk_bf16_f32 v214, v204, v205\n"
"v_mfma_f32_16x16x32_bf16 v[192:195], a[108:111], a[44:47], v[192:195]\n"
"v_exp_f32 v206, v206\n"
"v_mfma_f32_16x16x32_bf16 v[196:199], a[112:115], a[32:35], v[224:227]\n"
"v_add_f32 v241, v241, v206\n"
"v_mfma_f32_16x16x32_bf16 v[196:199], a[116:119], a[36:39], v[196:199]\n"
"v_exp_f32 v207, v207\n"
"v_mfma_f32_16x16x32_bf16 v[196:199], a[120:123], a[40:43], v[196:199]\n"
"v_add_f32 v241, v241, v207\n"
"v_cvt_pk_bf16_f32 v215, v206, v207\n"
"v_mfma_f32_16x16x32_bf16 v[196:199], a[124:127], a[44:47], v[196:199]\n"
"v_exp_f32 v192, v192\n"
"v_mfma_f32_16x16x32_bf16 v[32:35], a[192:195], v[212:215], v[32:35]\n"
"v_mov_b32 v92, s98\n"
"v_mov_b32 v242, v192\n"
"v_mfma_f32_16x16x32_bf16 v[36:39], a[196:199], v[212:215], v[36:39]\n"
"v_exp_f32 v193, v193\n"
"v_mfma_f32_16x16x32_bf16 v[40:43], a[200:203], v[212:215], v[40:43]\n"
"v_add_f32 v242, v242, v193\n"
"v_cvt_pk_bf16_f32 v208, v192, v193\n"
"v_mfma_f32_16x16x32_bf16 v[44:47], a[204:207], v[212:215], v[44:47]\n"
"v_exp_f32 v194, v194\n"
"v_mfma_f32_16x16x32_bf16 v[48:51], a[208:211], v[212:215], v[48:51]\n"
"v_mov_b32 v93, s98\n"
"v_add_f32 v242, v242, v194\n"
"v_mfma_f32_16x16x32_bf16 v[52:55], a[212:215], v[212:215], v[52:55]\n"
"v_exp_f32 v195, v195\n"
"v_mfma_f32_16x16x32_bf16 v[56:59], a[216:219], v[212:215], v[56:59]\n"
"v_add_f32 v242, v242, v195\n"
"v_cvt_pk_bf16_f32 v209, v194, v195\n"
"v_mfma_f32_16x16x32_bf16 v[60:63], a[220:223], v[212:215], v[60:63]\n"
"v_exp_f32 v196, v196\n"
"v_mfma_f32_16x16x32_bf16 v[200:203], a[128:131], a[32:35], v[224:227]\n"
"v_mov_b32 v94, s98\n"
"v_add_f32 v242, v242, v196\n"
"v_mfma_f32_16x16x32_bf16 v[200:203], a[132:135], a[36:39], v[200:203]\n"
"v_exp_f32 v197, v197\n"
"v_mfma_f32_16x16x32_bf16 v[200:203], a[136:139], a[40:43], v[200:203]\n"
"v_add_f32 v242, v242, v197\n"
"v_cvt_pk_bf16_f32 v210, v196, v197\n"
"v_mfma_f32_16x16x32_bf16 v[200:203], a[140:143], a[44:47], v[200:203]\n"
"v_exp_f32 v198, v198\n"
"v_mfma_f32_16x16x32_bf16 v[204:207], a[144:147], a[32:35], v[224:227]\n"
"v_mov_b32 v95, s98\n"
"v_add_f32 v242, v242, v198\n"
"v_mfma_f32_16x16x32_bf16 v[204:207], a[148:151], a[36:39], v[204:207]\n"
"v_exp_f32 v199, v199\n"
"v_mfma_f32_16x16x32_bf16 v[204:207], a[152:155], a[40:43], v[204:207]\n"
"v_add_f32 v242, v242, v199\n"
"v_cvt_pk_bf16_f32 v211, v198, v199\n"
"v_mfma_f32_16x16x32_bf16 v[204:207], a[156:159], a[44:47], v[204:207]\n"
"s_waitcnt vmcnt(4)\n"
"v_exp_f32 v200, v200\n"
"v_mfma_f32_16x16x32_bf16 v[64:67], a[160:163], v[208:211], v[92:95]\n"
"ds_read_b128 a[56:59], v254 offset:13312\n"
"v_add_f32 v242, v242, v200\n"
"v_mfma_f32_16x16x32_bf16 v[68:71], a[164:167], v[208:211], v[92:95]\n"
"ds_read_b128 a[60:63], v254 offset:13824\n"
"v_exp_f32 v201, v201\n"
"v_mfma_f32_16x16x32_bf16 v[72:75], a[168:171], v[208:211], v[92:95]\n"
"ds_read_b128 v[104:107], v254 offset:12288\n"
"v_add_f32 v242, v242, v201\n"
"v_cvt_pk_bf16_f32 v212, v200, v201\n"
"v_mfma_f32_16x16x32_bf16 v[76:79], a[172:175], v[208:211], v[92:95]\n"
"ds_read_b128 v[108:111], v254 offset:12800\n"
"v_exp_f32 v202, v202\n"
"v_mfma_f32_16x16x32_bf16 v[80:83], a[176:179], v[208:211], v[92:95]\n"
"v_add_f32 v242, v242, v202\n"
"v_mfma_f32_16x16x32_bf16 v[84:87], a[180:183], v[208:211], v[92:95]\n"
"v_exp_f32 v203, v203\n"
"v_mfma_f32_16x16x32_bf16 v[88:91], a[184:187], v[208:211], v[92:95]\n"
"v_add_f32 v242, v242, v203\n"
"v_cvt_pk_bf16_f32 v213, v202, v203\n"
"v_mfma_f32_16x16x32_bf16 v[92:95], a[188:191], v[208:211], v[92:95]\n"
"s_waitcnt lgkmcnt(3)\n"
"v_exp_f32 v204, v204\n"
"v_mfma_f32_32x32x16_bf16 v[112:127], a[224:227], a[56:59], 0\n"
"s_waitcnt lgkmcnt(1)\n"
"v_lshlrev_b32 v96, 16, v104\n"
"v_and_b32 v97, s64, v104\n"
"v_lshlrev_b32 v98, 16, v105\n"
"v_and_b32 v99, s64, v105\n"
"v_mfma_f32_32x32x16_bf16 v[112:127], a[228:231], a[56:59], v[112:127]\n"
"v_lshlrev_b32 v100, 16, v106\n"
"v_and_b32 v101, s64, v106\n"
"v_lshlrev_b32 v102, 16, v107\n"
"v_and_b32 v103, s64, v107\n"
"v_mfma_f32_32x32x16_bf16 v[112:127], a[232:235], a[60:63], v[112:127]\n"
"s_waitcnt lgkmcnt(0)\n"
"v_lshlrev_b32 v104, 16, v108\n"
"v_and_b32 v105, s64, v108\n"
"v_lshlrev_b32 v106, 16, v109\n"
"v_and_b32 v107, s64, v109\n"
"v_mfma_f32_32x32x16_bf16 v[112:127], a[236:239], a[60:63], v[112:127]\n"
"v_lshlrev_b32 v108, 16, v110\n"
"v_and_b32 v109, s64, v110\n"
"v_lshlrev_b32 v110, 16, v111\n"
"v_and_b32 v111, s64, v111\n"
"v_pk_mul_f32 v[96:97], v[96:97], s[52:53]\n"
"v_pk_mul_f32 v[98:99], v[98:99], s[52:53]\n"
"v_pk_mul_f32 v[100:101], v[100:101], s[52:53]\n"
"v_pk_mul_f32 v[102:103], v[102:103], s[52:53]\n"
"v_pk_mul_f32 v[104:105], v[104:105], s[52:53]\n"
"v_pk_mul_f32 v[106:107], v[106:107], s[52:53]\n"
"v_pk_mul_f32 v[108:109], v[108:109], s[52:53]\n"
"v_pk_mul_f32 v[110:111], v[110:111], s[52:53]\n"
"v_cvt_pk_bf16_f32 v96, v96, v97\n"
"v_cvt_pk_bf16_f32 v98, v98, v99\n"
"v_cvt_pk_bf16_f32 v100, v100, v101\n"
"v_cvt_pk_bf16_f32 v102, v102, v103\n"
"v_accvgpr_write_b32 a48, v96\n"
"v_accvgpr_write_b32 a49, v98\n"
"v_accvgpr_write_b32 a50, v100\n"
"v_accvgpr_write_b32 a51, v102\n"
"v_cvt_pk_bf16_f32 v104, v104, v105\n"
"v_cvt_pk_bf16_f32 v106, v106, v107\n"
"v_mfma_f32_16x16x32_bf16 v[228:231], a[240:243], a[48:51], v[228:231]\n"
"v_cvt_pk_bf16_f32 v108, v108, v109\n"
"v_cvt_pk_bf16_f32 v110, v110, v111\n"
"v_accvgpr_write_b32 a52, v104\n"
"v_accvgpr_write_b32 a53, v106\n"
"v_accvgpr_write_b32 a54, v108\n"
"v_accvgpr_write_b32 a55, v110\n"
"v_cvt_pk_bf16_f32 v112, v112, v113\n"
"v_cvt_pk_bf16_f32 v114, v114, v115\n"
"v_mfma_f32_16x16x32_bf16 v[228:231], a[244:247], a[52:55], v[228:231]\n"
"v_cvt_pk_bf16_f32 v116, v116, v117\n"
"v_cvt_pk_bf16_f32 v118, v118, v119\n"
"v_accvgpr_write_b32 a56, v112\n"
"v_accvgpr_write_b32 a57, v114\n"
"v_accvgpr_write_b32 a58, v116\n"
"v_accvgpr_write_b32 a59, v118\n"
"v_cvt_pk_bf16_f32 v120, v120, v121\n"
"v_cvt_pk_bf16_f32 v122, v122, v123\n"
"v_mfma_f32_16x16x32_bf16 v[228:231], a[248:251], a[56:59], v[228:231]\n"
"v_cvt_pk_bf16_f32 v124, v124, v125\n"
"v_cvt_pk_bf16_f32 v126, v126, v127\n"
"v_accvgpr_write_b32 a60, v120\n"
"v_accvgpr_write_b32 a61, v122\n"
"v_accvgpr_write_b32 a62, v124\n"
"v_accvgpr_write_b32 a63, v126\n"
"s_nop 1\n"
"v_mfma_f32_16x16x32_bf16 v[228:231], a[252:255], a[60:63], v[228:231]\n"
"v_mfma_f32_16x16x32_bf16 v[192:195], a[96:99], a[48:51], v[228:231]\n"
"s_add_u32 m0, s66, 16384\n"
"s_add_u32 s59, s67, 16384\n"
"buffer_load_dwordx4 v252, s[36:39], s59 offen nt lds\n"
"v_add_f32 v242, v242, v204\n"
"v_mfma_f32_16x16x32_bf16 v[192:195], a[100:103], a[52:55], v[192:195]\n"
"s_mov_b32 m0, 57344\n"
"s_mov_b32 s59, 24576\n"
"buffer_load_dwordx4 v252, s[40:43], s59 offen nt lds\n"
"v_exp_f32 v205, v205\n"
"v_mfma_f32_16x16x32_bf16 v[192:195], a[104:107], a[56:59], v[192:195]\n"
"v_add_f32 v242, v242, v205\n"
"v_cvt_pk_bf16_f32 v214, v204, v205\n"
"v_mfma_f32_16x16x32_bf16 v[192:195], a[108:111], a[60:63], v[192:195]\n"
"s_mov_b32 m0, 57344\n"
"s_mov_b32 s59, 24576\n"
"buffer_load_dwordx4 v252, s[40:43], s59 offen offset:2048 nt lds\n"
"v_exp_f32 v206, v206\n"
"v_mfma_f32_16x16x32_bf16 v[196:199], a[112:115], a[48:51], v[228:231]\n"
"s_add_u32 m0, s66, 16384\n"
"s_add_u32 s59, s67, 16384\n"
"buffer_load_dwordx4 v253, s[36:39], s59 offen offset:2048 nt lds\n"
"v_add_f32 v242, v242, v206\n"
"v_mfma_f32_16x16x32_bf16 v[196:199], a[116:119], a[52:55], v[196:199]\n"
"s_mov_b32 m0, 58368\n"
"s_mov_b32 s59, 24704\n"
"buffer_load_dwordx4 v252, s[40:43], s59 offen nt lds\n"
"v_exp_f32 v207, v207\n"
"v_mfma_f32_16x16x32_bf16 v[196:199], a[120:123], a[56:59], v[196:199]\n"
"v_add_f32 v242, v242, v207\n"
"v_cvt_pk_bf16_f32 v215, v206, v207\n"
"v_mfma_f32_16x16x32_bf16 v[196:199], a[124:127], a[60:63], v[196:199]\n"
"s_mov_b32 m0, 58368\n"
"s_mov_b32 s59, 24704\n"
"buffer_load_dwordx4 v252, s[40:43], s59 offen offset:2048 nt lds\n"
"v_exp_f32 v192, v192\n"
"v_mfma_f32_16x16x32_bf16 v[64:67], a[192:195], v[212:215], v[64:67]\n"
"s_add_u32 m0, s66, 49152\n"
"s_add_u32 s59, s67, 16384\n"
"buffer_load_dwordx4 v252, s[40:43], s59 offen nt lds\n"
"v_mov_b32 v124, s98\n"
"v_mov_b32 v243, v192\n"
"v_mfma_f32_16x16x32_bf16 v[68:71], a[196:199], v[212:215], v[68:71]\n"
"s_mov_b32 m0, 61440\n"
"s_mov_b32 s59, 28672\n"
"buffer_load_dwordx4 v252, s[40:43], s59 offen nt lds\n"
"v_exp_f32 v193, v193\n"
"v_mfma_f32_16x16x32_bf16 v[72:75], a[200:203], v[212:215], v[72:75]\n"
"v_add_f32 v243, v243, v193\n"
"v_cvt_pk_bf16_f32 v208, v192, v193\n"
"v_mfma_f32_16x16x32_bf16 v[76:79], a[204:207], v[212:215], v[76:79]\n"
"s_mov_b32 m0, 61440\n"
"s_mov_b32 s59, 28672\n"
"buffer_load_dwordx4 v252, s[40:43], s59 offen offset:2048 nt lds\n"
"v_exp_f32 v194, v194\n"
"v_mfma_f32_16x16x32_bf16 v[80:83], a[208:211], v[212:215], v[80:83]\n"
"s_add_u32 m0, s66, 49152\n"
"s_add_u32 s59, s67, 16384\n"
"buffer_load_dwordx4 v252, s[40:43], s59 offen offset:2048 nt lds\n"
"v_mov_b32 v125, s98\n"
"v_add_f32 v243, v243, v194\n"
"v_mfma_f32_16x16x32_bf16 v[84:87], a[212:215], v[212:215], v[84:87]\n"
"s_mov_b32 m0, 62464\n"
"s_mov_b32 s59, 28800\n"
"buffer_load_dwordx4 v252, s[40:43], s59 offen nt lds\n"
"v_exp_f32 v195, v195\n"
"v_mfma_f32_16x16x32_bf16 v[88:91], a[216:219], v[212:215], v[88:91]\n"
"v_add_f32 v243, v243, v195\n"
"v_cvt_pk_bf16_f32 v209, v194, v195\n"
"v_mfma_f32_16x16x32_bf16 v[92:95], a[220:223], v[212:215], v[92:95]\n"
"s_mov_b32 m0, 62464\n"
"s_mov_b32 s59, 28800\n"
"buffer_load_dwordx4 v252, s[40:43], s59 offen offset:2048 nt lds\n"
"v_exp_f32 v196, v196\n"
"v_mfma_f32_16x16x32_bf16 v[200:203], a[128:131], a[48:51], v[228:231]\n"
"s_add_u32 m0, s66, 24576\n"
"s_add_u32 s59, s67, 24576\n"
"buffer_load_dwordx4 v252, s[36:39], s59 offen nt lds\n"
"v_mov_b32 v126, s98\n"
"v_add_f32 v243, v243, v196\n"
"v_mfma_f32_16x16x32_bf16 v[200:203], a[132:135], a[52:55], v[200:203]\n"
"s_mov_b32 m0, 0\n"
"s_mov_b32 s59, 32768\n"
"buffer_load_dwordx4 v252, s[36:39], s59 offen nt lds\n"
"v_exp_f32 v197, v197\n"
"v_mfma_f32_16x16x32_bf16 v[200:203], a[136:139], a[56:59], v[200:203]\n"
"v_add_f32 v243, v243, v197\n"
"v_cvt_pk_bf16_f32 v210, v196, v197\n"
"v_mfma_f32_16x16x32_bf16 v[200:203], a[140:143], a[60:63], v[200:203]\n"
"s_mov_b32 m0, 0\n"
"s_mov_b32 s59, 32768\n"
"buffer_load_dwordx4 v253, s[36:39], s59 offen offset:2048 nt lds\n"
"v_exp_f32 v198, v198\n"
"v_mfma_f32_16x16x32_bf16 v[204:207], a[144:147], a[48:51], v[228:231]\n"
"s_add_u32 m0, s66, 24576\n"
"s_add_u32 s59, s67, 24576\n"
"buffer_load_dwordx4 v253, s[36:39], s59 offen offset:2048 nt lds\n"
"v_mov_b32 v127, s98\n"
"v_add_f32 v243, v243, v198\n"
"v_mfma_f32_16x16x32_bf16 v[204:207], a[148:151], a[52:55], v[204:207]\n"
"s_mov_b32 m0, 1024\n"
"s_mov_b32 s59, 32896\n"
"buffer_load_dwordx4 v252, s[36:39], s59 offen nt lds\n"
"v_exp_f32 v199, v199\n"
"v_mfma_f32_16x16x32_bf16 v[204:207], a[152:155], a[56:59], v[204:207]\n"
"v_add_f32 v243, v243, v199\n"
"v_cvt_pk_bf16_f32 v211, v198, v199\n"
"v_mfma_f32_16x16x32_bf16 v[204:207], a[156:159], a[60:63], v[204:207]\n"
"s_waitcnt vmcnt(17)\n"
"s_mov_b32 m0, 1024\n"
"s_mov_b32 s59, 32896\n"
"buffer_load_dwordx4 v253, s[36:39], s59 offen offset:2048 nt lds\n"
"v_exp_f32 v200, v200\n"
"v_mfma_f32_16x16x32_bf16 v[96:99], a[160:163], v[208:211], v[124:127]\n"
"ds_read_b128 a[72:75], v254 offset:17408\n"
"v_add_f32 v243, v243, v200\n"
"v_mfma_f32_16x16x32_bf16 v[100:103], a[164:167], v[208:211], v[124:127]\n"
"s_mov_b32 m0, 4096\n"
"s_mov_b32 s59, 36864\n"
"buffer_load_dwordx4 v252, s[36:39], s59 offen nt lds\n"
"ds_read_b128 a[76:79], v254 offset:17920\n"
"v_exp_f32 v201, v201\n"
"v_mfma_f32_16x16x32_bf16 v[104:107], a[168:171], v[208:211], v[124:127]\n"
"ds_read_b128 v[136:139], v254 offset:16384\n"
"v_add_f32 v243, v243, v201\n"
"v_cvt_pk_bf16_f32 v212, v200, v201\n"
"v_mfma_f32_16x16x32_bf16 v[108:111], a[172:175], v[208:211], v[124:127]\n"
"s_mov_b32 m0, 4096\n"
"s_mov_b32 s59, 36864\n"
"buffer_load_dwordx4 v253, s[36:39], s59 offen offset:2048 nt lds\n"
"ds_read_b128 v[140:143], v254 offset:16896\n"
"v_exp_f32 v202, v202\n"
"v_mfma_f32_16x16x32_bf16 v[112:115], a[176:179], v[208:211], v[124:127]\n"
"v_add_f32 v243, v243, v202\n"
"v_mfma_f32_16x16x32_bf16 v[116:119], a[180:183], v[208:211], v[124:127]\n"
"s_mov_b32 m0, 5120\n"
"s_mov_b32 s59, 36992\n"
"buffer_load_dwordx4 v252, s[36:39], s59 offen nt lds\n"
"v_exp_f32 v203, v203\n"
"v_mfma_f32_16x16x32_bf16 v[120:123], a[184:187], v[208:211], v[124:127]\n"
"v_add_f32 v243, v243, v203\n"
"v_cvt_pk_bf16_f32 v213, v202, v203\n"
"v_mfma_f32_16x16x32_bf16 v[124:127], a[188:191], v[208:211], v[124:127]\n"
"s_waitcnt lgkmcnt(3)\n"
"s_mov_b32 m0, 5120\n"
"s_mov_b32 s59, 36992\n"
"buffer_load_dwordx4 v253, s[36:39], s59 offen offset:2048 nt lds\n"
"v_exp_f32 v204, v204\n"
"v_mfma_f32_32x32x16_bf16 v[144:159], a[224:227], a[72:75], 0\n"
"s_waitcnt lgkmcnt(1)\n"
"v_lshlrev_b32 v128, 16, v136\n"
"v_and_b32 v129, s64, v136\n"
"v_lshlrev_b32 v130, 16, v137\n"
"v_and_b32 v131, s64, v137\n"
"v_mfma_f32_32x32x16_bf16 v[144:159], a[228:231], a[72:75], v[144:159]\n"
"v_lshlrev_b32 v132, 16, v138\n"
"v_and_b32 v133, s64, v138\n"
"v_lshlrev_b32 v134, 16, v139\n"
"v_and_b32 v135, s64, v139\n"
"v_mfma_f32_32x32x16_bf16 v[144:159], a[232:235], a[76:79], v[144:159]\n"
"s_waitcnt lgkmcnt(0)\n"
"v_lshlrev_b32 v136, 16, v140\n"
"v_and_b32 v137, s64, v140\n"
"v_lshlrev_b32 v138, 16, v141\n"
"v_and_b32 v139, s64, v141\n"
"v_mfma_f32_32x32x16_bf16 v[144:159], a[236:239], a[76:79], v[144:159]\n"
"v_lshlrev_b32 v140, 16, v142\n"
"v_and_b32 v141, s64, v142\n"
"v_lshlrev_b32 v142, 16, v143\n"
"v_and_b32 v143, s64, v143\n"
"v_pk_mul_f32 v[128:129], v[128:129], s[52:53]\n"
"v_pk_mul_f32 v[130:131], v[130:131], s[52:53]\n"
"v_pk_mul_f32 v[132:133], v[132:133], s[52:53]\n"
"v_pk_mul_f32 v[134:135], v[134:135], s[52:53]\n"
"v_pk_mul_f32 v[136:137], v[136:137], s[52:53]\n"
"v_pk_mul_f32 v[138:139], v[138:139], s[52:53]\n"
"v_pk_mul_f32 v[140:141], v[140:141], s[52:53]\n"
"v_pk_mul_f32 v[142:143], v[142:143], s[52:53]\n"
"v_cvt_pk_bf16_f32 v128, v128, v129\n"
"v_cvt_pk_bf16_f32 v130, v130, v131\n"
"v_cvt_pk_bf16_f32 v132, v132, v133\n"
"v_cvt_pk_bf16_f32 v134, v134, v135\n"
"v_accvgpr_write_b32 a64, v128\n"
"v_accvgpr_write_b32 a65, v130\n"
"v_accvgpr_write_b32 a66, v132\n"
"v_accvgpr_write_b32 a67, v134\n"
"v_cvt_pk_bf16_f32 v136, v136, v137\n"
"v_cvt_pk_bf16_f32 v138, v138, v139\n"
"v_mfma_f32_16x16x32_bf16 v[232:235], a[240:243], a[64:67], v[232:235]\n"
"v_cvt_pk_bf16_f32 v140, v140, v141\n"
"v_cvt_pk_bf16_f32 v142, v142, v143\n"
"v_accvgpr_write_b32 a68, v136\n"
"v_accvgpr_write_b32 a69, v138\n"
"v_accvgpr_write_b32 a70, v140\n"
"v_accvgpr_write_b32 a71, v142\n"
"v_cvt_pk_bf16_f32 v144, v144, v145\n"
"v_cvt_pk_bf16_f32 v146, v146, v147\n"
"v_mfma_f32_16x16x32_bf16 v[232:235], a[244:247], a[68:71], v[232:235]\n"
"v_cvt_pk_bf16_f32 v148, v148, v149\n"
"v_cvt_pk_bf16_f32 v150, v150, v151\n"
"v_accvgpr_write_b32 a72, v144\n"
"v_accvgpr_write_b32 a73, v146\n"
"v_accvgpr_write_b32 a74, v148\n"
"v_accvgpr_write_b32 a75, v150\n"
"v_cvt_pk_bf16_f32 v152, v152, v153\n"
"v_cvt_pk_bf16_f32 v154, v154, v155\n"
"v_mfma_f32_16x16x32_bf16 v[232:235], a[248:251], a[72:75], v[232:235]\n"
"v_cvt_pk_bf16_f32 v156, v156, v157\n"
"v_cvt_pk_bf16_f32 v158, v158, v159\n"
"v_accvgpr_write_b32 a76, v152\n"
"v_accvgpr_write_b32 a77, v154\n"
"v_accvgpr_write_b32 a78, v156\n"
"v_accvgpr_write_b32 a79, v158\n"
"v_lshlrev_b32 v255, 4, v249\n"
"v_and_b32 v129, 15, v249\n"
"v_mul_u32_u24 v160, 272, v129\n"
"v_lshrrev_b32 v129, 4, v249\n"
"v_lshl_add_u32 v160, v129, 4, v160\n"
"v_add_u32 v160, s62, v160\n"
"v_mul_u32_u24 v161, 272, v129\n"
"v_add_u32 v161, s62, v161\n"
"v_bfe_u32 v129, v249, 2, 2\n"
"v_lshl_add_u32 v161, v129, 6, v161\n"
"v_and_b32 v129, 1, v249\n"
"v_lshl_add_u32 v161, v129, 5, v161\n"
"v_bfe_u32 v129, v249, 1, 1\n"
"v_lshl_add_u32 v161, v129, 3, v161\n"
"v_and_b32 v249, 48, v249\n"
"s_mov_b32 m0, 32768\n"
"s_mov_b32 s59, 32768\n"
"buffer_load_dwordx4 v252, s[40:43], s59 offen nt lds\n"
"buffer_load_dwordx4 v252, s[40:43], s59 offen offset:2048 nt lds\n"
"s_mov_b32 m0, 33792\n"
"s_mov_b32 s59, 32896\n"
"buffer_load_dwordx4 v252, s[40:43], s59 offen nt lds\n"
"buffer_load_dwordx4 v252, s[40:43], s59 offen offset:2048 nt lds\n"
"s_mov_b32 m0, 36864\n"
"s_mov_b32 s59, 36864\n"
"buffer_load_dwordx4 v252, s[40:43], s59 offen nt lds\n"
"buffer_load_dwordx4 v252, s[40:43], s59 offen offset:2048 nt lds\n"
"s_mov_b32 m0, 37888\n"
"s_mov_b32 s59, 36992\n"
"buffer_load_dwordx4 v252, s[40:43], s59 offen nt lds\n"
"buffer_load_dwordx4 v252, s[40:43], s59 offen offset:2048 nt lds\n"
"s_waitcnt vmcnt(26)\n"
"s_barrier\n"
"v_mfma_f32_16x16x32_bf16 v[232:235], a[252:255], a[76:79], v[232:235]\n"
"v_mfma_f32_16x16x32_bf16 v[192:195], a[96:99], a[64:67], v[232:235]\n"
"ds_read_b128 a[96:99], v246 offset:16384\n"
"v_add_f32 v243, v243, v204\n"
"v_mfma_f32_16x16x32_bf16 v[192:195], a[100:103], a[68:71], v[192:195]\n"
"v_exp_f32 v205, v205\n"
"v_mfma_f32_16x16x32_bf16 v[192:195], a[104:107], a[72:75], v[192:195]\n"
"ds_read_b128 a[100:103], v246 offset:16896\n"
"v_add_f32 v243, v243, v205\n"
"v_cvt_pk_bf16_f32 v214, v204, v205\n"
"v_mfma_f32_16x16x32_bf16 v[192:195], a[108:111], a[76:79], v[192:195]\n"
"s_mov_b32 m0, 8192\n"
"s_mov_b32 s59, 40960\n"
"buffer_load_dwordx4 v252, s[36:39], s59 offen nt lds\n"
"v_exp_f32 v206, v206\n"
"v_mfma_f32_16x16x32_bf16 v[196:199], a[112:115], a[64:67], v[232:235]\n"
"ds_read_b128 a[104:107], v246 offset:17408\n"
"v_add_f32 v243, v243, v206\n"
"v_mfma_f32_16x16x32_bf16 v[196:199], a[116:119], a[68:71], v[196:199]\n"
"s_mov_b32 m0, 8192\n"
"s_mov_b32 s59, 40960\n"
"buffer_load_dwordx4 v253, s[36:39], s59 offen offset:2048 nt lds\n"
"v_exp_f32 v207, v207\n"
"v_mfma_f32_16x16x32_bf16 v[196:199], a[120:123], a[72:75], v[196:199]\n"
"ds_read_b128 a[108:111], v246 offset:17920\n"
"v_add_f32 v243, v243, v207\n"
"v_cvt_pk_bf16_f32 v215, v206, v207\n"
"v_mfma_f32_16x16x32_bf16 v[196:199], a[124:127], a[76:79], v[196:199]\n"
"s_add_u32 m0, s58, -4096\n"
"s_add_u32 s59, s57, -4096\n"
"buffer_load_dwordx4 v252, s[44:47], s59 offen lds\n"
"v_exp_f32 v192, v192\n"
"v_mfma_f32_16x16x32_bf16 v[96:99], a[192:195], v[212:215], v[96:99]\n"
"ds_read_b128 a[112:115], v246 offset:20480\n"
"v_mov_b32 v156, s98\n"
"v_mov_b32 v244, v192\n"
"v_mfma_f32_16x16x32_bf16 v[100:103], a[196:199], v[212:215], v[100:103]\n"
"s_add_u32 m0, s58, -4096\n"
"s_add_u32 s59, s57, -4096\n"
"buffer_load_dwordx4 v253, s[44:47], s59 offen offset:2048 lds\n"
"v_exp_f32 v193, v193\n"
"v_mfma_f32_16x16x32_bf16 v[104:107], a[200:203], v[212:215], v[104:107]\n"
"ds_read_b128 a[116:119], v246 offset:20992\n"
"v_add_f32 v244, v244, v193\n"
"v_cvt_pk_bf16_f32 v208, v192, v193\n"
"v_mfma_f32_16x16x32_bf16 v[108:111], a[204:207], v[212:215], v[108:111]\n"
"s_add_u32 m0, s58, -3072\n"
"s_add_u32 s59, s57, -3968\n"
"buffer_load_dwordx4 v252, s[44:47], s59 offen lds\n"
"v_exp_f32 v194, v194\n"
"v_mfma_f32_16x16x32_bf16 v[112:115], a[208:211], v[212:215], v[112:115]\n"
"ds_read_b128 a[120:123], v246 offset:21504\n"
"v_mov_b32 v157, s98\n"
"v_add_f32 v244, v244, v194\n"
"v_mfma_f32_16x16x32_bf16 v[116:119], a[212:215], v[212:215], v[116:119]\n"
"s_add_u32 m0, s58, -3072\n"
"s_add_u32 s59, s57, -3968\n"
"buffer_load_dwordx4 v253, s[44:47], s59 offen offset:2048 lds\n"
"v_exp_f32 v195, v195\n"
"v_mfma_f32_16x16x32_bf16 v[120:123], a[216:219], v[212:215], v[120:123]\n"
"ds_read_b128 a[124:127], v246 offset:22016\n"
"v_add_f32 v244, v244, v195\n"
"v_cvt_pk_bf16_f32 v209, v194, v195\n"
"v_mfma_f32_16x16x32_bf16 v[124:127], a[220:223], v[212:215], v[124:127]\n"
"s_mov_b32 m0, 9216\n"
"s_mov_b32 s59, 41088\n"
"buffer_load_dwordx4 v252, s[36:39], s59 offen nt lds\n"
"v_exp_f32 v196, v196\n"
"v_mfma_f32_16x16x32_bf16 v[200:203], a[128:131], a[64:67], v[232:235]\n"
"v_mov_b32 v158, s98\n"
"v_add_f32 v244, v244, v196\n"
"v_mfma_f32_16x16x32_bf16 v[200:203], a[132:135], a[68:71], v[200:203]\n"
"s_mov_b32 m0, 9216\n"
"s_mov_b32 s59, 41088\n"
"buffer_load_dwordx4 v253, s[36:39], s59 offen offset:2048 nt lds\n"
"v_exp_f32 v197, v197\n"
"v_mfma_f32_16x16x32_bf16 v[200:203], a[136:139], a[72:75], v[200:203]\n"
"v_add_f32 v244, v244, v197\n"
"v_cvt_pk_bf16_f32 v210, v196, v197\n"
"v_mfma_f32_16x16x32_bf16 v[200:203], a[140:143], a[76:79], v[200:203]\n"
"s_mov_b32 m0, 12288\n"
"s_mov_b32 s59, 45056\n"
"buffer_load_dwordx4 v252, s[36:39], s59 offen nt lds\n"
"v_exp_f32 v198, v198\n"
"v_mfma_f32_16x16x32_bf16 v[204:207], a[144:147], a[64:67], v[232:235]\n"
"v_mov_b32 v159, s98\n"
"v_add_f32 v244, v244, v198\n"
"v_mfma_f32_16x16x32_bf16 v[204:207], a[148:151], a[68:71], v[204:207]\n"
"s_waitcnt vmcnt(23)\n"
"s_mov_b32 m0, 12288\n"
"s_mov_b32 s59, 45056\n"
"buffer_load_dwordx4 v253, s[36:39], s59 offen offset:2048 nt lds\n"
"v_exp_f32 v199, v199\n"
"v_mfma_f32_16x16x32_bf16 v[204:207], a[152:155], a[72:75], v[204:207]\n"
"s_barrier\n"
"v_add_f32 v244, v244, v199\n"
"v_cvt_pk_bf16_f32 v211, v198, v199\n"
"v_mfma_f32_16x16x32_bf16 v[204:207], a[156:159], a[76:79], v[204:207]\n"
"v_exp_f32 v200, v200\n"
"v_mfma_f32_16x16x32_bf16 v[128:131], a[160:163], v[208:211], v[156:159]\n"
"ds_read_b64_tr_b16 a[160:161], v247 offset:49152\n"
"v_add_f32 v244, v244, v200\n"
"v_mfma_f32_16x16x32_bf16 v[132:135], a[164:167], v[208:211], v[156:159]\n"
"s_mov_b32 m0, 13312\n"
"s_mov_b32 s59, 45184\n"
"ds_read_b64_tr_b16 a[162:163], v247 offset:53248\n"
"buffer_load_dwordx4 v252, s[36:39], s59 offen nt lds\n"
"v_exp_f32 v201, v201\n"
"v_mfma_f32_16x16x32_bf16 v[136:139], a[168:171], v[208:211], v[156:159]\n"
"ds_read_b64_tr_b16 a[164:165], v248 offset:49152\n"
"v_add_f32 v244, v244, v201\n"
"v_cvt_pk_bf16_f32 v212, v200, v201\n"
"v_mfma_f32_16x16x32_bf16 v[140:143], a[172:175], v[208:211], v[156:159]\n"
"s_mov_b32 m0, 13312\n"
"s_mov_b32 s59, 45184\n"
"ds_read_b64_tr_b16 a[166:167], v248 offset:53248\n"
"buffer_load_dwordx4 v253, s[36:39], s59 offen offset:2048 nt lds\n"
"v_exp_f32 v202, v202\n"
"v_mfma_f32_16x16x32_bf16 v[144:147], a[176:179], v[208:211], v[156:159]\n"
"ds_read_b64_tr_b16 a[168:169], v247 offset:49664\n"
"v_add_f32 v244, v244, v202\n"
"v_mfma_f32_16x16x32_bf16 v[148:151], a[180:183], v[208:211], v[156:159]\n"
"s_mov_b32 m0, 40960\n"
"s_mov_b32 s59, 40960\n"
"ds_read_b64_tr_b16 a[170:171], v247 offset:53760\n"
"buffer_load_dwordx4 v252, s[40:43], s59 offen nt lds\n"
"v_exp_f32 v203, v203\n"
"v_mfma_f32_16x16x32_bf16 v[152:155], a[184:187], v[208:211], v[156:159]\n"
"ds_read_b64_tr_b16 a[172:173], v248 offset:49664\n"
"v_add_f32 v244, v244, v203\n"
"v_cvt_pk_bf16_f32 v213, v202, v203\n"
"v_mfma_f32_16x16x32_bf16 v[156:159], a[188:191], v[208:211], v[156:159]\n"
"s_waitcnt lgkmcnt(5)\n"
"s_mov_b32 m0, 40960\n"
"s_mov_b32 s59, 40960\n"
"ds_read_b64_tr_b16 a[174:175], v248 offset:53760\n"
"buffer_load_dwordx4 v252, s[40:43], s59 offen offset:2048 nt lds\n"
"v_exp_f32 v204, v204\n"
"v_mfma_f32_16x16x32_bf16 v[192:195], a[96:99], a[0:3], v[216:219]\n"
"ds_read_b64_tr_b16 a[176:177], v247 offset:50176\n"
"v_add_f32 v244, v244, v204\n"
"v_mfma_f32_16x16x32_bf16 v[192:195], a[100:103], a[4:7], v[192:195]\n"
"s_mov_b32 m0, 41984\n"
"s_mov_b32 s59, 41088\n"
"buffer_load_dwordx4 v252, s[40:43], s59 offen nt lds\n"
"v_exp_f32 v205, v205\n"
"v_mfma_f32_16x16x32_bf16 v[192:195], a[104:107], a[8:11], v[192:195]\n"
"ds_read_b64_tr_b16 a[178:179], v247 offset:54272\n"
"v_add_f32 v244, v244, v205\n"
"v_cvt_pk_bf16_f32 v214, v204, v205\n"
"v_mfma_f32_16x16x32_bf16 v[192:195], a[108:111], a[12:15], v[192:195]\n"
"s_mov_b32 m0, 41984\n"
"s_mov_b32 s59, 41088\n"
"buffer_load_dwordx4 v252, s[40:43], s59 offen offset:2048 nt lds\n"
"v_exp_f32 v206, v206\n"
"v_mfma_f32_16x16x32_bf16 v[196:199], a[112:115], a[0:3], v[216:219]\n"
"ds_read_b64_tr_b16 a[180:181], v248 offset:50176\n"
"v_add_f32 v244, v244, v206\n"
"v_mfma_f32_16x16x32_bf16 v[196:199], a[116:119], a[4:7], v[196:199]\n"
"s_mov_b32 m0, 45056\n"
"s_mov_b32 s59, 45056\n"
"buffer_load_dwordx4 v252, s[40:43], s59 offen nt lds\n"
"v_exp_f32 v207, v207\n"
"v_mfma_f32_16x16x32_bf16 v[196:199], a[120:123], a[8:11], v[196:199]\n"
"ds_read_b64_tr_b16 a[182:183], v248 offset:54272\n"
"v_add_f32 v244, v244, v207\n"
"v_cvt_pk_bf16_f32 v215, v206, v207\n"
"v_mfma_f32_16x16x32_bf16 v[196:199], a[124:127], a[12:15], v[196:199]\n"
"s_mov_b32 m0, 45056\n"
"s_mov_b32 s59, 45056\n"
"buffer_load_dwordx4 v252, s[40:43], s59 offen offset:2048 nt lds\n"
"v_exp_f32 v192, v192\n"
"v_mfma_f32_16x16x32_bf16 v[128:131], a[192:195], v[212:215], v[128:131]\n"
"ds_read_b64_tr_b16 a[184:185], v247 offset:50688\n"
"v_add_f32 v240, v240, v192\n"
"v_mfma_f32_16x16x32_bf16 v[132:135], a[196:199], v[212:215], v[132:135]\n"
"s_mov_b32 m0, 46080\n"
"s_mov_b32 s59, 45184\n"
"buffer_load_dwordx4 v252, s[40:43], s59 offen nt lds\n"
"v_exp_f32 v193, v193\n"
"v_mfma_f32_16x16x32_bf16 v[136:139], a[200:203], v[212:215], v[136:139]\n"
"ds_read_b64_tr_b16 a[186:187], v247 offset:54784\n"
"v_add_f32 v240, v240, v193\n"
"v_cvt_pk_bf16_f32 v208, v192, v193\n"
"v_mfma_f32_16x16x32_bf16 v[140:143], a[204:207], v[212:215], v[140:143]\n"
"s_mov_b32 m0, 46080\n"
"s_mov_b32 s59, 45184\n"
"buffer_load_dwordx4 v252, s[40:43], s59 offen offset:2048 nt lds\n"
"v_exp_f32 v194, v194\n"
"v_mfma_f32_16x16x32_bf16 v[144:147], a[208:211], v[212:215], v[144:147]\n"
"ds_read_b64_tr_b16 a[188:189], v248 offset:50688\n"
"v_add_f32 v240, v240, v194\n"
"v_mfma_f32_16x16x32_bf16 v[148:151], a[212:215], v[212:215], v[148:151]\n"
"v_exp_f32 v195, v195\n"
"v_mfma_f32_16x16x32_bf16 v[152:155], a[216:219], v[212:215], v[152:155]\n"
"ds_read_b64_tr_b16 a[190:191], v248 offset:54784\n"
"v_add_f32 v240, v240, v195\n"
"v_cvt_pk_bf16_f32 v209, v194, v195\n"
"v_mfma_f32_16x16x32_bf16 v[156:159], a[220:223], v[212:215], v[156:159]\n"
"LMB0%=:\n"
"v_exp_f32 v196, v196\n"
"v_mfma_f32_16x16x32_bf16 v[200:203], a[96:99], a[16:19], v[220:223]\n"
"ds_read_b128 a[128:131], v246 offset:24576\n"
"v_add_f32 v240, v240, v196\n"
"v_mfma_f32_16x16x32_bf16 v[200:203], a[100:103], a[20:23], v[200:203]\n"
"v_exp_f32 v197, v197\n"
"v_mfma_f32_16x16x32_bf16 v[200:203], a[104:107], a[24:27], v[200:203]\n"
"v_add_f32 v240, v240, v197\n"
"v_cvt_pk_bf16_f32 v210, v196, v197\n"
"v_mfma_f32_16x16x32_bf16 v[200:203], a[108:111], a[28:31], v[200:203]\n"
"v_exp_f32 v198, v198\n"
"v_mfma_f32_16x16x32_bf16 v[204:207], a[112:115], a[16:19], v[220:223]\n"
"ds_read_b128 a[132:135], v246 offset:25088\n"
"v_add_f32 v240, v240, v198\n"
"v_mfma_f32_16x16x32_bf16 v[204:207], a[116:119], a[20:23], v[204:207]\n"
"s_waitcnt lgkmcnt(2)\n"
"v_exp_f32 v199, v199\n"
"v_mfma_f32_16x16x32_bf16 v[204:207], a[120:123], a[24:27], v[204:207]\n"
"s_waitcnt vmcnt(38)\n"
"v_add_f32 v240, v240, v199\n"
"v_cvt_pk_bf16_f32 v211, v198, v199\n"
"v_mfma_f32_16x16x32_bf16 v[204:207], a[124:127], a[28:31], v[204:207]\n"
"s_barrier\n"
"v_exp_f32 v200, v200\n"
"v_mfma_f32_16x16x32_bf16 v[0:3], a[160:163], v[208:211], v[0:3]\n"
"s_add_u32 s56, s56, 8192\n"
"ds_read_b128 a[136:139], v246 offset:25600\n"
"v_add_f32 v241, v241, v200\n"
"v_mfma_f32_16x16x32_bf16 v[4:7], a[164:167], v[208:211], v[4:7]\n"
"v_exp_f32 v201, v201\n"
"v_mfma_f32_16x16x32_bf16 v[8:11], a[168:171], v[208:211], v[8:11]\n"
"s_mov_b32 m0, 16384\n"
"ds_read_b128 a[140:143], v246 offset:26112\n"
"v_add_f32 v241, v241, v201\n"
"v_cvt_pk_bf16_f32 v212, v200, v201\n"
"v_mfma_f32_16x16x32_bf16 v[12:15], a[172:175], v[208:211], v[12:15]\n"
"s_add_u32 s59, s56, 0\n"
"buffer_load_dwordx4 v252, s[36:39], s59 offen lds\n"
"v_exp_f32 v202, v202\n"
"v_mfma_f32_16x16x32_bf16 v[16:19], a[176:179], v[208:211], v[16:19]\n"
"ds_read_b128 a[144:147], v246 offset:28672\n"
"v_add_f32 v241, v241, v202\n"
"v_mfma_f32_16x16x32_bf16 v[20:23], a[180:183], v[208:211], v[20:23]\n"
"buffer_load_dwordx4 v253, s[36:39], s59 offen offset:2048 lds\n"
"v_exp_f32 v203, v203\n"
"v_mfma_f32_16x16x32_bf16 v[24:27], a[184:187], v[208:211], v[24:27]\n"
"v_add_f32 v241, v241, v203\n"
"v_cvt_pk_bf16_f32 v213, v202, v203\n"
"v_mfma_f32_16x16x32_bf16 v[28:31], a[188:191], v[208:211], v[28:31]\n"
"v_exp_f32 v204, v204\n"
"v_mfma_f32_16x16x32_bf16 v[192:195], a[96:99], a[32:35], v[224:227]\n"
"s_add_u32 m0, s58, 0\n"
"ds_read_b128 a[148:151], v246 offset:29184\n"
"v_add_f32 v241, v241, v204\n"
"v_mfma_f32_16x16x32_bf16 v[192:195], a[100:103], a[36:39], v[192:195]\n"
"s_add_u32 s59, s57, 0\n"
"buffer_load_dwordx4 v252, s[44:47], s59 offen lds\n"
"v_exp_f32 v205, v205\n"
"v_mfma_f32_16x16x32_bf16 v[192:195], a[104:107], a[40:43], v[192:195]\n"
"v_add_f32 v241, v241, v205\n"
"v_cvt_pk_bf16_f32 v214, v204, v205\n"
"v_mfma_f32_16x16x32_bf16 v[192:195], a[108:111], a[44:47], v[192:195]\n"
"buffer_load_dwordx4 v253, s[44:47], s59 offen offset:2048 lds\n"
"v_exp_f32 v206, v206\n"
"v_mfma_f32_16x16x32_bf16 v[196:199], a[112:115], a[32:35], v[224:227]\n"
"ds_read_b128 a[152:155], v246 offset:29696\n"
"v_add_f32 v241, v241, v206\n"
"v_mfma_f32_16x16x32_bf16 v[196:199], a[116:119], a[36:39], v[196:199]\n"
"v_exp_f32 v207, v207\n"
"v_mfma_f32_16x16x32_bf16 v[196:199], a[120:123], a[40:43], v[196:199]\n"
"s_add_u32 m0, s58, 1024\n"
"v_add_f32 v241, v241, v207\n"
"v_cvt_pk_bf16_f32 v215, v206, v207\n"
"v_mfma_f32_16x16x32_bf16 v[196:199], a[124:127], a[44:47], v[196:199]\n"
"s_add_u32 s59, s57, 128\n"
"buffer_load_dwordx4 v252, s[44:47], s59 offen lds\n"
"v_exp_f32 v192, v192\n"
"v_mfma_f32_16x16x32_bf16 v[32:35], a[160:163], v[212:215], v[32:35]\n"
"ds_read_b128 a[156:159], v246 offset:30208\n"
"v_add_f32 v242, v242, v192\n"
"v_mfma_f32_16x16x32_bf16 v[36:39], a[164:167], v[212:215], v[36:39]\n"
"buffer_load_dwordx4 v253, s[44:47], s59 offen offset:2048 lds\n"
"v_exp_f32 v193, v193\n"
"v_mfma_f32_16x16x32_bf16 v[40:43], a[168:171], v[212:215], v[40:43]\n"
"v_add_f32 v242, v242, v193\n"
"v_cvt_pk_bf16_f32 v208, v192, v193\n"
"v_mfma_f32_16x16x32_bf16 v[44:47], a[172:175], v[212:215], v[44:47]\n"
"v_exp_f32 v194, v194\n"
"v_mfma_f32_16x16x32_bf16 v[48:51], a[176:179], v[212:215], v[48:51]\n"
"s_mov_b32 m0, 17408\n"
"ds_read_b64_tr_b16 a[192:193], v247 offset:57344\n"
"v_add_f32 v242, v242, v194\n"
"v_mfma_f32_16x16x32_bf16 v[52:55], a[180:183], v[212:215], v[52:55]\n"
"s_add_u32 s59, s56, 128\n"
"buffer_load_dwordx4 v252, s[36:39], s59 offen lds\n"
"v_exp_f32 v195, v195\n"
"v_mfma_f32_16x16x32_bf16 v[56:59], a[184:187], v[212:215], v[56:59]\n"
"ds_read_b64_tr_b16 a[194:195], v247 offset:61440\n"
"v_add_f32 v242, v242, v195\n"
"v_cvt_pk_bf16_f32 v209, v194, v195\n"
"v_mfma_f32_16x16x32_bf16 v[60:63], a[188:191], v[212:215], v[60:63]\n"
"buffer_load_dwordx4 v253, s[36:39], s59 offen offset:2048 lds\n"
"v_exp_f32 v196, v196\n"
"v_mfma_f32_16x16x32_bf16 v[200:203], a[96:99], a[48:51], v[228:231]\n"
"ds_read_b64_tr_b16 a[196:197], v248 offset:57344\n"
"v_add_f32 v242, v242, v196\n"
"v_mfma_f32_16x16x32_bf16 v[200:203], a[100:103], a[52:55], v[200:203]\n"
"v_exp_f32 v197, v197\n"
"v_mfma_f32_16x16x32_bf16 v[200:203], a[104:107], a[56:59], v[200:203]\n"
"s_mov_b32 m0, 20480\n"
"v_add_f32 v242, v242, v197\n"
"v_cvt_pk_bf16_f32 v210, v196, v197\n"
"v_mfma_f32_16x16x32_bf16 v[200:203], a[108:111], a[60:63], v[200:203]\n"
"s_add_u32 s59, s56, 4096\n"
"buffer_load_dwordx4 v252, s[36:39], s59 offen lds\n"
"v_exp_f32 v198, v198\n"
"v_mfma_f32_16x16x32_bf16 v[204:207], a[112:115], a[48:51], v[228:231]\n"
"ds_read_b64_tr_b16 a[198:199], v248 offset:61440\n"
"v_add_f32 v242, v242, v198\n"
"v_mfma_f32_16x16x32_bf16 v[204:207], a[116:119], a[52:55], v[204:207]\n"
"buffer_load_dwordx4 v253, s[36:39], s59 offen offset:2048 lds\n"
"v_exp_f32 v199, v199\n"
"v_mfma_f32_16x16x32_bf16 v[204:207], a[120:123], a[56:59], v[204:207]\n"
"v_add_f32 v242, v242, v199\n"
"v_cvt_pk_bf16_f32 v211, v198, v199\n"
"v_mfma_f32_16x16x32_bf16 v[204:207], a[124:127], a[60:63], v[204:207]\n"
"v_exp_f32 v200, v200\n"
"v_mfma_f32_16x16x32_bf16 v[64:67], a[160:163], v[208:211], v[64:67]\n"
"s_mov_b32 m0, 21504\n"
"ds_read_b64_tr_b16 a[200:201], v247 offset:57856\n"
"v_add_f32 v243, v243, v200\n"
"v_mfma_f32_16x16x32_bf16 v[68:71], a[164:167], v[208:211], v[68:71]\n"
"s_add_u32 s59, s56, 4224\n"
"buffer_load_dwordx4 v252, s[36:39], s59 offen lds\n"
"v_exp_f32 v201, v201\n"
"v_mfma_f32_16x16x32_bf16 v[72:75], a[168:171], v[208:211], v[72:75]\n"
"v_add_f32 v243, v243, v201\n"
"v_cvt_pk_bf16_f32 v212, v200, v201\n"
"v_mfma_f32_16x16x32_bf16 v[76:79], a[172:175], v[208:211], v[76:79]\n"
"v_exp_f32 v202, v202\n"
"v_mfma_f32_16x16x32_bf16 v[80:83], a[176:179], v[208:211], v[80:83]\n"
"ds_read_b64_tr_b16 a[202:203], v247 offset:61952\n"
"v_add_f32 v243, v243, v202\n"
"v_mfma_f32_16x16x32_bf16 v[84:87], a[180:183], v[208:211], v[84:87]\n"
"buffer_load_dwordx4 v253, s[36:39], s59 offen offset:2048 lds\n"
"v_exp_f32 v203, v203\n"
"v_mfma_f32_16x16x32_bf16 v[88:91], a[184:187], v[208:211], v[88:91]\n"
"s_mov_b32 m0, 49152\n"
"v_add_f32 v243, v243, v203\n"
"v_cvt_pk_bf16_f32 v213, v202, v203\n"
"v_mfma_f32_16x16x32_bf16 v[92:95], a[188:191], v[208:211], v[92:95]\n"
"s_add_u32 s59, s56, 0\n"
"buffer_load_dwordx4 v252, s[40:43], s59 offen lds\n"
"v_exp_f32 v204, v204\n"
"v_mfma_f32_16x16x32_bf16 v[192:195], a[96:99], a[64:67], v[232:235]\n"
"ds_read_b64_tr_b16 a[204:205], v248 offset:57856\n"
"v_add_f32 v243, v243, v204\n"
"v_mfma_f32_16x16x32_bf16 v[192:195], a[100:103], a[68:71], v[192:195]\n"
"v_exp_f32 v205, v205\n"
"v_mfma_f32_16x16x32_bf16 v[192:195], a[104:107], a[72:75], v[192:195]\n"
"ds_read_b64_tr_b16 a[206:207], v248 offset:61952\n"
"v_add_f32 v243, v243, v205\n"
"v_cvt_pk_bf16_f32 v214, v204, v205\n"
"v_mfma_f32_16x16x32_bf16 v[192:195], a[108:111], a[76:79], v[192:195]\n"
"buffer_load_dwordx4 v252, s[40:43], s59 offen offset:2048 lds\n"
"v_exp_f32 v206, v206\n"
"v_mfma_f32_16x16x32_bf16 v[196:199], a[112:115], a[64:67], v[232:235]\n"
"s_mov_b32 m0, 50176\n"
"ds_read_b64_tr_b16 a[208:209], v247 offset:58368\n"
"v_add_f32 v243, v243, v206\n"
"v_mfma_f32_16x16x32_bf16 v[196:199], a[116:119], a[68:71], v[196:199]\n"
"s_add_u32 s59, s56, 128\n"
"buffer_load_dwordx4 v252, s[40:43], s59 offen lds\n"
"v_exp_f32 v207, v207\n"
"v_mfma_f32_16x16x32_bf16 v[196:199], a[120:123], a[72:75], v[196:199]\n"
"v_add_f32 v243, v243, v207\n"
"v_cvt_pk_bf16_f32 v215, v206, v207\n"
"v_mfma_f32_16x16x32_bf16 v[196:199], a[124:127], a[76:79], v[196:199]\n"
"v_exp_f32 v192, v192\n"
"v_mfma_f32_16x16x32_bf16 v[96:99], a[160:163], v[212:215], v[96:99]\n"
"ds_read_b64_tr_b16 a[210:211], v247 offset:62464\n"
"v_add_f32 v244, v244, v192\n"
"v_mfma_f32_16x16x32_bf16 v[100:103], a[164:167], v[212:215], v[100:103]\n"
"buffer_load_dwordx4 v252, s[40:43], s59 offen offset:2048 lds\n"
"v_exp_f32 v193, v193\n"
"v_mfma_f32_16x16x32_bf16 v[104:107], a[168:171], v[212:215], v[104:107]\n"
"s_mov_b32 m0, 53248\n"
"v_add_f32 v244, v244, v193\n"
"v_cvt_pk_bf16_f32 v208, v192, v193\n"
"v_mfma_f32_16x16x32_bf16 v[108:111], a[172:175], v[212:215], v[108:111]\n"
"s_add_u32 s59, s56, 4096\n"
"buffer_load_dwordx4 v252, s[40:43], s59 offen lds\n"
"v_exp_f32 v194, v194\n"
"v_mfma_f32_16x16x32_bf16 v[112:115], a[176:179], v[212:215], v[112:115]\n"
"ds_read_b64_tr_b16 a[212:213], v248 offset:58368\n"
"v_add_f32 v244, v244, v194\n"
"v_mfma_f32_16x16x32_bf16 v[116:119], a[180:183], v[212:215], v[116:119]\n"
"v_exp_f32 v195, v195\n"
"v_mfma_f32_16x16x32_bf16 v[120:123], a[184:187], v[212:215], v[120:123]\n"
"v_add_f32 v244, v244, v195\n"
"v_cvt_pk_bf16_f32 v209, v194, v195\n"
"v_mfma_f32_16x16x32_bf16 v[124:127], a[188:191], v[212:215], v[124:127]\n"
"s_waitcnt lgkmcnt(2)\n"
"buffer_load_dwordx4 v252, s[40:43], s59 offen offset:2048 lds\n"
"v_exp_f32 v196, v196\n"
"v_mfma_f32_16x16x32_bf16 v[200:203], a[128:131], a[0:3], v[216:219]\n"
"s_mov_b32 m0, 54272\n"
"ds_read_b64_tr_b16 a[214:215], v248 offset:62464\n"
"v_add_f32 v244, v244, v196\n"
"v_mfma_f32_16x16x32_bf16 v[200:203], a[132:135], a[4:7], v[200:203]\n"
"s_add_u32 s59, s56, 4224\n"
"buffer_load_dwordx4 v252, s[40:43], s59 offen lds\n"
"v_exp_f32 v197, v197\n"
"v_mfma_f32_16x16x32_bf16 v[200:203], a[136:139], a[8:11], v[200:203]\n"
"v_add_f32 v244, v244, v197\n"
"v_cvt_pk_bf16_f32 v210, v196, v197\n"
"v_mfma_f32_16x16x32_bf16 v[200:203], a[140:143], a[12:15], v[200:203]\n"
"v_exp_f32 v198, v198\n"
"v_mfma_f32_16x16x32_bf16 v[204:207], a[144:147], a[0:3], v[216:219]\n"
"ds_read_b64_tr_b16 a[216:217], v247 offset:58880\n"
"v_add_f32 v244, v244, v198\n"
"v_mfma_f32_16x16x32_bf16 v[204:207], a[148:151], a[4:7], v[204:207]\n"
"buffer_load_dwordx4 v252, s[40:43], s59 offen offset:2048 lds\n"
"v_exp_f32 v199, v199\n"
"v_mfma_f32_16x16x32_bf16 v[204:207], a[152:155], a[8:11], v[204:207]\n"
"s_add_u32 s58, s58, s69\n"
"ds_read_b64_tr_b16 a[218:219], v247 offset:62976\n"
"v_add_f32 v244, v244, v199\n"
"v_cvt_pk_bf16_f32 v211, v198, v199\n"
"v_mfma_f32_16x16x32_bf16 v[204:207], a[156:159], a[12:15], v[204:207]\n"
"s_add_u32 s57, s57, 4096\n"
"v_exp_f32 v200, v200\n"
"v_mfma_f32_16x16x32_bf16 v[128:131], a[160:163], v[208:211], v[128:131]\n"
"ds_read_b64_tr_b16 a[220:221], v248 offset:58880\n"
"v_add_f32 v240, v240, v200\n"
"v_mfma_f32_16x16x32_bf16 v[132:135], a[164:167], v[208:211], v[132:135]\n"
"v_exp_f32 v201, v201\n"
"v_mfma_f32_16x16x32_bf16 v[136:139], a[168:171], v[208:211], v[136:139]\n"
"v_add_f32 v240, v240, v201\n"
"v_cvt_pk_bf16_f32 v212, v200, v201\n"
"v_mfma_f32_16x16x32_bf16 v[140:143], a[172:175], v[208:211], v[140:143]\n"
"v_exp_f32 v202, v202\n"
"v_mfma_f32_16x16x32_bf16 v[144:147], a[176:179], v[208:211], v[144:147]\n"
"ds_read_b64_tr_b16 a[222:223], v248 offset:62976\n"
"v_add_f32 v240, v240, v202\n"
"v_mfma_f32_16x16x32_bf16 v[148:151], a[180:183], v[208:211], v[148:151]\n"
"v_exp_f32 v203, v203\n"
"v_mfma_f32_16x16x32_bf16 v[152:155], a[184:187], v[208:211], v[152:155]\n"
"v_add_f32 v240, v240, v203\n"
"v_cvt_pk_bf16_f32 v213, v202, v203\n"
"v_mfma_f32_16x16x32_bf16 v[156:159], a[188:191], v[208:211], v[156:159]\n"
"LMB1%=:\n"
"v_exp_f32 v204, v204\n"
"v_mfma_f32_16x16x32_bf16 v[192:195], a[128:131], a[16:19], v[220:223]\n"
"ds_read_b128 a[96:99], v246 offset:0\n"
"v_add_f32 v240, v240, v204\n"
"v_mfma_f32_16x16x32_bf16 v[192:195], a[132:135], a[20:23], v[192:195]\n"
"v_exp_f32 v205, v205\n"
"v_mfma_f32_16x16x32_bf16 v[192:195], a[136:139], a[24:27], v[192:195]\n"
"v_add_f32 v240, v240, v205\n"
"v_cvt_pk_bf16_f32 v214, v204, v205\n"
"v_mfma_f32_16x16x32_bf16 v[192:195], a[140:143], a[28:31], v[192:195]\n"
"v_exp_f32 v206, v206\n"
"v_mfma_f32_16x16x32_bf16 v[196:199], a[144:147], a[16:19], v[220:223]\n"
"ds_read_b128 a[100:103], v246 offset:512\n"
"v_add_f32 v240, v240, v206\n"
"v_mfma_f32_16x16x32_bf16 v[196:199], a[148:151], a[20:23], v[196:199]\n"
"s_waitcnt lgkmcnt(2)\n"
"v_exp_f32 v207, v207\n"
"v_mfma_f32_16x16x32_bf16 v[196:199], a[152:155], a[24:27], v[196:199]\n"
"s_waitcnt vmcnt(38)\n"
"v_add_f32 v240, v240, v207\n"
"v_cvt_pk_bf16_f32 v215, v206, v207\n"
"v_mfma_f32_16x16x32_bf16 v[196:199], a[156:159], a[28:31], v[196:199]\n"
"s_barrier\n"
"v_exp_f32 v192, v192\n"
"v_mfma_f32_16x16x32_bf16 v[0:3], a[192:195], v[212:215], v[0:3]\n"
"s_add_u32 s56, s56, 8192\n"
"ds_read_b128 a[104:107], v246 offset:1024\n"
"v_add_f32 v241, v241, v192\n"
"v_mfma_f32_16x16x32_bf16 v[4:7], a[196:199], v[212:215], v[4:7]\n"
"v_exp_f32 v193, v193\n"
"v_mfma_f32_16x16x32_bf16 v[8:11], a[200:203], v[212:215], v[8:11]\n"
"s_mov_b32 m0, 24576\n"
"ds_read_b128 a[108:111], v246 offset:1536\n"
"v_add_f32 v241, v241, v193\n"
"v_cvt_pk_bf16_f32 v208, v192, v193\n"
"v_mfma_f32_16x16x32_bf16 v[12:15], a[204:207], v[212:215], v[12:15]\n"
"s_add_u32 s59, s56, 0\n"
"buffer_load_dwordx4 v252, s[36:39], s59 offen lds\n"
"v_exp_f32 v194, v194\n"
"v_mfma_f32_16x16x32_bf16 v[16:19], a[208:211], v[212:215], v[16:19]\n"
"ds_read_b128 a[112:115], v246 offset:4096\n"
"v_add_f32 v241, v241, v194\n"
"v_mfma_f32_16x16x32_bf16 v[20:23], a[212:215], v[212:215], v[20:23]\n"
"buffer_load_dwordx4 v253, s[36:39], s59 offen offset:2048 lds\n"
"v_exp_f32 v195, v195\n"
"v_mfma_f32_16x16x32_bf16 v[24:27], a[216:219], v[212:215], v[24:27]\n"
"v_add_f32 v241, v241, v195\n"
"v_cvt_pk_bf16_f32 v209, v194, v195\n"
"v_mfma_f32_16x16x32_bf16 v[28:31], a[220:223], v[212:215], v[28:31]\n"
"v_exp_f32 v196, v196\n"
"v_mfma_f32_16x16x32_bf16 v[200:203], a[128:131], a[32:35], v[224:227]\n"
"s_add_u32 m0, s58, 0\n"
"ds_read_b128 a[116:119], v246 offset:4608\n"
"v_add_f32 v241, v241, v196\n"
"v_mfma_f32_16x16x32_bf16 v[200:203], a[132:135], a[36:39], v[200:203]\n"
"s_add_u32 s59, s57, 0\n"
"buffer_load_dwordx4 v252, s[44:47], s59 offen lds\n"
"v_exp_f32 v197, v197\n"
"v_mfma_f32_16x16x32_bf16 v[200:203], a[136:139], a[40:43], v[200:203]\n"
"v_add_f32 v241, v241, v197\n"
"v_cvt_pk_bf16_f32 v210, v196, v197\n"
"v_mfma_f32_16x16x32_bf16 v[200:203], a[140:143], a[44:47], v[200:203]\n"
"buffer_load_dwordx4 v253, s[44:47], s59 offen offset:2048 lds\n"
"v_exp_f32 v198, v198\n"
"v_mfma_f32_16x16x32_bf16 v[204:207], a[144:147], a[32:35], v[224:227]\n"
"ds_read_b128 a[120:123], v246 offset:5120\n"
"v_add_f32 v241, v241, v198\n"
"v_mfma_f32_16x16x32_bf16 v[204:207], a[148:151], a[36:39], v[204:207]\n"
"v_exp_f32 v199, v199\n"
"v_mfma_f32_16x16x32_bf16 v[204:207], a[152:155], a[40:43], v[204:207]\n"
"s_add_u32 m0, s58, 1024\n"
"v_add_f32 v241, v241, v199\n"
"v_cvt_pk_bf16_f32 v211, v198, v199\n"
"v_mfma_f32_16x16x32_bf16 v[204:207], a[156:159], a[44:47], v[204:207]\n"
"s_add_u32 s59, s57, 128\n"
"buffer_load_dwordx4 v252, s[44:47], s59 offen lds\n"
"v_exp_f32 v200, v200\n"
"v_mfma_f32_16x16x32_bf16 v[32:35], a[192:195], v[208:211], v[32:35]\n"
"ds_read_b128 a[124:127], v246 offset:5632\n"
"v_add_f32 v242, v242, v200\n"
"v_mfma_f32_16x16x32_bf16 v[36:39], a[196:199], v[208:211], v[36:39]\n"
"buffer_load_dwordx4 v253, s[44:47], s59 offen offset:2048 lds\n"
"v_exp_f32 v201, v201\n"
"v_mfma_f32_16x16x32_bf16 v[40:43], a[200:203], v[208:211], v[40:43]\n"
"v_add_f32 v242, v242, v201\n"
"v_cvt_pk_bf16_f32 v212, v200, v201\n"
"v_mfma_f32_16x16x32_bf16 v[44:47], a[204:207], v[208:211], v[44:47]\n"
"v_exp_f32 v202, v202\n"
"v_mfma_f32_16x16x32_bf16 v[48:51], a[208:211], v[208:211], v[48:51]\n"
"s_mov_b32 m0, 25600\n"
"ds_read_b64_tr_b16 a[160:161], v247 offset:32768\n"
"v_add_f32 v242, v242, v202\n"
"v_mfma_f32_16x16x32_bf16 v[52:55], a[212:215], v[208:211], v[52:55]\n"
"s_add_u32 s59, s56, 128\n"
"buffer_load_dwordx4 v252, s[36:39], s59 offen lds\n"
"v_exp_f32 v203, v203\n"
"v_mfma_f32_16x16x32_bf16 v[56:59], a[216:219], v[208:211], v[56:59]\n"
"ds_read_b64_tr_b16 a[162:163], v247 offset:36864\n"
"v_add_f32 v242, v242, v203\n"
"v_cvt_pk_bf16_f32 v213, v202, v203\n"
"v_mfma_f32_16x16x32_bf16 v[60:63], a[220:223], v[208:211], v[60:63]\n"
"buffer_load_dwordx4 v253, s[36:39], s59 offen offset:2048 lds\n"
"v_exp_f32 v204, v204\n"
"v_mfma_f32_16x16x32_bf16 v[192:195], a[128:131], a[48:51], v[228:231]\n"
"ds_read_b64_tr_b16 a[164:165], v248 offset:32768\n"
"v_add_f32 v242, v242, v204\n"
"v_mfma_f32_16x16x32_bf16 v[192:195], a[132:135], a[52:55], v[192:195]\n"
"v_exp_f32 v205, v205\n"
"v_mfma_f32_16x16x32_bf16 v[192:195], a[136:139], a[56:59], v[192:195]\n"
"s_mov_b32 m0, 28672\n"
"v_add_f32 v242, v242, v205\n"
"v_cvt_pk_bf16_f32 v214, v204, v205\n"
"v_mfma_f32_16x16x32_bf16 v[192:195], a[140:143], a[60:63], v[192:195]\n"
"s_add_u32 s59, s56, 4096\n"
"buffer_load_dwordx4 v252, s[36:39], s59 offen lds\n"
"v_exp_f32 v206, v206\n"
"v_mfma_f32_16x16x32_bf16 v[196:199], a[144:147], a[48:51], v[228:231]\n"
"ds_read_b64_tr_b16 a[166:167], v248 offset:36864\n"
"v_add_f32 v242, v242, v206\n"
"v_mfma_f32_16x16x32_bf16 v[196:199], a[148:151], a[52:55], v[196:199]\n"
"buffer_load_dwordx4 v253, s[36:39], s59 offen offset:2048 lds\n"
"v_exp_f32 v207, v207\n"
"v_mfma_f32_16x16x32_bf16 v[196:199], a[152:155], a[56:59], v[196:199]\n"
"v_add_f32 v242, v242, v207\n"
"v_cvt_pk_bf16_f32 v215, v206, v207\n"
"v_mfma_f32_16x16x32_bf16 v[196:199], a[156:159], a[60:63], v[196:199]\n"
"v_exp_f32 v192, v192\n"
"v_mfma_f32_16x16x32_bf16 v[64:67], a[192:195], v[212:215], v[64:67]\n"
"s_mov_b32 m0, 29696\n"
"ds_read_b64_tr_b16 a[168:169], v247 offset:33280\n"
"v_add_f32 v243, v243, v192\n"
"v_mfma_f32_16x16x32_bf16 v[68:71], a[196:199], v[212:215], v[68:71]\n"
"s_add_u32 s59, s56, 4224\n"
"buffer_load_dwordx4 v252, s[36:39], s59 offen lds\n"
"v_exp_f32 v193, v193\n"
"v_mfma_f32_16x16x32_bf16 v[72:75], a[200:203], v[212:215], v[72:75]\n"
"v_add_f32 v243, v243, v193\n"
"v_cvt_pk_bf16_f32 v208, v192, v193\n"
"v_mfma_f32_16x16x32_bf16 v[76:79], a[204:207], v[212:215], v[76:79]\n"
"v_exp_f32 v194, v194\n"
"v_mfma_f32_16x16x32_bf16 v[80:83], a[208:211], v[212:215], v[80:83]\n"
"ds_read_b64_tr_b16 a[170:171], v247 offset:37376\n"
"v_add_f32 v243, v243, v194\n"
"v_mfma_f32_16x16x32_bf16 v[84:87], a[212:215], v[212:215], v[84:87]\n"
"buffer_load_dwordx4 v253, s[36:39], s59 offen offset:2048 lds\n"
"v_exp_f32 v195, v195\n"
"v_mfma_f32_16x16x32_bf16 v[88:91], a[216:219], v[212:215], v[88:91]\n"
"s_mov_b32 m0, 57344\n"
"v_add_f32 v243, v243, v195\n"
"v_cvt_pk_bf16_f32 v209, v194, v195\n"
"v_mfma_f32_16x16x32_bf16 v[92:95], a[220:223], v[212:215], v[92:95]\n"
"s_add_u32 s59, s56, 0\n"
"buffer_load_dwordx4 v252, s[40:43], s59 offen lds\n"
"v_exp_f32 v196, v196\n"
"v_mfma_f32_16x16x32_bf16 v[200:203], a[128:131], a[64:67], v[232:235]\n"
"ds_read_b64_tr_b16 a[172:173], v248 offset:33280\n"
"v_add_f32 v243, v243, v196\n"
"v_mfma_f32_16x16x32_bf16 v[200:203], a[132:135], a[68:71], v[200:203]\n"
"v_exp_f32 v197, v197\n"
"v_mfma_f32_16x16x32_bf16 v[200:203], a[136:139], a[72:75], v[200:203]\n"
"ds_read_b64_tr_b16 a[174:175], v248 offset:37376\n"
"v_add_f32 v243, v243, v197\n"
"v_cvt_pk_bf16_f32 v210, v196, v197\n"
"v_mfma_f32_16x16x32_bf16 v[200:203], a[140:143], a[76:79], v[200:203]\n"
"buffer_load_dwordx4 v252, s[40:43], s59 offen offset:2048 lds\n"
"v_exp_f32 v198, v198\n"
"v_mfma_f32_16x16x32_bf16 v[204:207], a[144:147], a[64:67], v[232:235]\n"
"s_mov_b32 m0, 58368\n"
"ds_read_b64_tr_b16 a[176:177], v247 offset:33792\n"
"v_add_f32 v243, v243, v198\n"
"v_mfma_f32_16x16x32_bf16 v[204:207], a[148:151], a[68:71], v[204:207]\n"
"s_add_u32 s59, s56, 128\n"
"buffer_load_dwordx4 v252, s[40:43], s59 offen lds\n"
"v_exp_f32 v199, v199\n"
"v_mfma_f32_16x16x32_bf16 v[204:207], a[152:155], a[72:75], v[204:207]\n"
"v_add_f32 v243, v243, v199\n"
"v_cvt_pk_bf16_f32 v211, v198, v199\n"
"v_mfma_f32_16x16x32_bf16 v[204:207], a[156:159], a[76:79], v[204:207]\n"
"v_exp_f32 v200, v200\n"
"v_mfma_f32_16x16x32_bf16 v[96:99], a[192:195], v[208:211], v[96:99]\n"
"ds_read_b64_tr_b16 a[178:179], v247 offset:37888\n"
"v_add_f32 v244, v244, v200\n"
"v_mfma_f32_16x16x32_bf16 v[100:103], a[196:199], v[208:211], v[100:103]\n"
"buffer_load_dwordx4 v252, s[40:43], s59 offen offset:2048 lds\n"
"v_exp_f32 v201, v201\n"
"v_mfma_f32_16x16x32_bf16 v[104:107], a[200:203], v[208:211], v[104:107]\n"
"s_mov_b32 m0, 61440\n"
"v_add_f32 v244, v244, v201\n"
"v_cvt_pk_bf16_f32 v212, v200, v201\n"
"v_mfma_f32_16x16x32_bf16 v[108:111], a[204:207], v[208:211], v[108:111]\n"
"s_add_u32 s59, s56, 4096\n"
"buffer_load_dwordx4 v252, s[40:43], s59 offen lds\n"
"v_exp_f32 v202, v202\n"
"v_mfma_f32_16x16x32_bf16 v[112:115], a[208:211], v[208:211], v[112:115]\n"
"ds_read_b64_tr_b16 a[180:181], v248 offset:33792\n"
"v_add_f32 v244, v244, v202\n"
"v_mfma_f32_16x16x32_bf16 v[116:119], a[212:215], v[208:211], v[116:119]\n"
"v_exp_f32 v203, v203\n"
"v_mfma_f32_16x16x32_bf16 v[120:123], a[216:219], v[208:211], v[120:123]\n"
"v_add_f32 v244, v244, v203\n"
"v_cvt_pk_bf16_f32 v213, v202, v203\n"
"v_mfma_f32_16x16x32_bf16 v[124:127], a[220:223], v[208:211], v[124:127]\n"
"s_waitcnt lgkmcnt(2)\n"
"buffer_load_dwordx4 v252, s[40:43], s59 offen offset:2048 lds\n"
"v_exp_f32 v204, v204\n"
"v_mfma_f32_16x16x32_bf16 v[192:195], a[96:99], a[0:3], v[216:219]\n"
"s_mov_b32 m0, 62464\n"
"ds_read_b64_tr_b16 a[182:183], v248 offset:37888\n"
"v_add_f32 v244, v244, v204\n"
"v_mfma_f32_16x16x32_bf16 v[192:195], a[100:103], a[4:7], v[192:195]\n"
"s_add_u32 s59, s56, 4224\n"
"buffer_load_dwordx4 v252, s[40:43], s59 offen lds\n"
"v_exp_f32 v205, v205\n"
"v_mfma_f32_16x16x32_bf16 v[192:195], a[104:107], a[8:11], v[192:195]\n"
"v_add_f32 v244, v244, v205\n"
"v_cvt_pk_bf16_f32 v214, v204, v205\n"
"v_mfma_f32_16x16x32_bf16 v[192:195], a[108:111], a[12:15], v[192:195]\n"
"v_exp_f32 v206, v206\n"
"v_mfma_f32_16x16x32_bf16 v[196:199], a[112:115], a[0:3], v[216:219]\n"
"ds_read_b64_tr_b16 a[184:185], v247 offset:34304\n"
"v_add_f32 v244, v244, v206\n"
"v_mfma_f32_16x16x32_bf16 v[196:199], a[116:119], a[4:7], v[196:199]\n"
"buffer_load_dwordx4 v252, s[40:43], s59 offen offset:2048 lds\n"
"v_exp_f32 v207, v207\n"
"v_mfma_f32_16x16x32_bf16 v[196:199], a[120:123], a[8:11], v[196:199]\n"
"s_add_u32 s58, s58, s69\n"
"ds_read_b64_tr_b16 a[186:187], v247 offset:38400\n"
"v_add_f32 v244, v244, v207\n"
"v_cvt_pk_bf16_f32 v215, v206, v207\n"
"v_mfma_f32_16x16x32_bf16 v[196:199], a[124:127], a[12:15], v[196:199]\n"
"s_add_u32 s57, s57, 4096\n"
"v_exp_f32 v192, v192\n"
"v_mfma_f32_16x16x32_bf16 v[128:131], a[192:195], v[212:215], v[128:131]\n"
"ds_read_b64_tr_b16 a[188:189], v248 offset:34304\n"
"v_add_f32 v240, v240, v192\n"
"v_mfma_f32_16x16x32_bf16 v[132:135], a[196:199], v[212:215], v[132:135]\n"
"v_exp_f32 v193, v193\n"
"v_mfma_f32_16x16x32_bf16 v[136:139], a[200:203], v[212:215], v[136:139]\n"
"v_add_f32 v240, v240, v193\n"
"v_cvt_pk_bf16_f32 v208, v192, v193\n"
"v_mfma_f32_16x16x32_bf16 v[140:143], a[204:207], v[212:215], v[140:143]\n"
"v_exp_f32 v194, v194\n"
"v_mfma_f32_16x16x32_bf16 v[144:147], a[208:211], v[212:215], v[144:147]\n"
"ds_read_b64_tr_b16 a[190:191], v248 offset:38400\n"
"v_add_f32 v240, v240, v194\n"
"v_mfma_f32_16x16x32_bf16 v[148:151], a[212:215], v[212:215], v[148:151]\n"
"v_exp_f32 v195, v195\n"
"v_mfma_f32_16x16x32_bf16 v[152:155], a[216:219], v[212:215], v[152:155]\n"
"v_add_f32 v240, v240, v195\n"
"v_cvt_pk_bf16_f32 v209, v194, v195\n"
"v_mfma_f32_16x16x32_bf16 v[156:159], a[220:223], v[212:215], v[156:159]\n"
"LMB2%=:\n"
"v_exp_f32 v196, v196\n"
"v_mfma_f32_16x16x32_bf16 v[200:203], a[96:99], a[16:19], v[220:223]\n"
"ds_read_b128 a[128:131], v246 offset:8192\n"
"v_add_f32 v240, v240, v196\n"
"v_mfma_f32_16x16x32_bf16 v[200:203], a[100:103], a[20:23], v[200:203]\n"
"s_add_u32 s54, s54, 1\n"
"v_exp_f32 v197, v197\n"
"v_mfma_f32_16x16x32_bf16 v[200:203], a[104:107], a[24:27], v[200:203]\n"
"v_add_f32 v240, v240, v197\n"
"v_cvt_pk_bf16_f32 v210, v196, v197\n"
"v_mfma_f32_16x16x32_bf16 v[200:203], a[108:111], a[28:31], v[200:203]\n"
"v_exp_f32 v198, v198\n"
"v_mfma_f32_16x16x32_bf16 v[204:207], a[112:115], a[16:19], v[220:223]\n"
"ds_read_b128 a[132:135], v246 offset:8704\n"
"v_add_f32 v240, v240, v198\n"
"v_mfma_f32_16x16x32_bf16 v[204:207], a[116:119], a[20:23], v[204:207]\n"
"s_waitcnt lgkmcnt(2)\n"
"v_exp_f32 v199, v199\n"
"v_mfma_f32_16x16x32_bf16 v[204:207], a[120:123], a[24:27], v[204:207]\n"
"s_waitcnt vmcnt(38)\n"
"v_add_f32 v240, v240, v199\n"
"v_cvt_pk_bf16_f32 v211, v198, v199\n"
"v_mfma_f32_16x16x32_bf16 v[204:207], a[124:127], a[28:31], v[204:207]\n"
"s_barrier\n"
"v_exp_f32 v200, v200\n"
"v_mfma_f32_16x16x32_bf16 v[0:3], a[160:163], v[208:211], v[0:3]\n"
"s_add_u32 s56, s56, 8192\n"
"ds_read_b128 a[136:139], v246 offset:9216\n"
"v_add_f32 v241, v241, v200\n"
"v_mfma_f32_16x16x32_bf16 v[4:7], a[164:167], v[208:211], v[4:7]\n"
"global_load_dword v163, v162, s[96:97]\n"
"v_exp_f32 v201, v201\n"
"v_mfma_f32_16x16x32_bf16 v[8:11], a[168:171], v[208:211], v[8:11]\n"
"s_mov_b32 m0, 0\n"
"ds_read_b128 a[140:143], v246 offset:9728\n"
"v_add_f32 v241, v241, v201\n"
"v_cvt_pk_bf16_f32 v212, v200, v201\n"
"v_mfma_f32_16x16x32_bf16 v[12:15], a[172:175], v[208:211], v[12:15]\n"
"s_add_u32 s59, s56, 0\n"
"buffer_load_dwordx4 v252, s[36:39], s59 offen lds\n"
"v_exp_f32 v202, v202\n"
"v_mfma_f32_16x16x32_bf16 v[16:19], a[176:179], v[208:211], v[16:19]\n"
"ds_read_b128 a[144:147], v246 offset:12288\n"
"v_add_f32 v241, v241, v202\n"
"v_mfma_f32_16x16x32_bf16 v[20:23], a[180:183], v[208:211], v[20:23]\n"
"buffer_load_dwordx4 v253, s[36:39], s59 offen offset:2048 lds\n"
"v_exp_f32 v203, v203\n"
"v_mfma_f32_16x16x32_bf16 v[24:27], a[184:187], v[208:211], v[24:27]\n"
"v_add_f32 v241, v241, v203\n"
"v_cvt_pk_bf16_f32 v213, v202, v203\n"
"v_mfma_f32_16x16x32_bf16 v[28:31], a[188:191], v[208:211], v[28:31]\n"
"v_exp_f32 v204, v204\n"
"v_mfma_f32_16x16x32_bf16 v[192:195], a[96:99], a[32:35], v[224:227]\n"
"s_add_u32 m0, s58, 0\n"
"ds_read_b128 a[148:151], v246 offset:12800\n"
"v_add_f32 v241, v241, v204\n"
"v_mfma_f32_16x16x32_bf16 v[192:195], a[100:103], a[36:39], v[192:195]\n"
"s_add_u32 s59, s57, 0\n"
"buffer_load_dwordx4 v252, s[44:47], s59 offen lds\n"
"v_exp_f32 v205, v205\n"
"v_mfma_f32_16x16x32_bf16 v[192:195], a[104:107], a[40:43], v[192:195]\n"
"v_add_f32 v241, v241, v205\n"
"v_cvt_pk_bf16_f32 v214, v204, v205\n"
"v_mfma_f32_16x16x32_bf16 v[192:195], a[108:111], a[44:47], v[192:195]\n"
"buffer_load_dwordx4 v253, s[44:47], s59 offen offset:2048 lds\n"
"v_exp_f32 v206, v206\n"
"v_mfma_f32_16x16x32_bf16 v[196:199], a[112:115], a[32:35], v[224:227]\n"
"ds_read_b128 a[152:155], v246 offset:13312\n"
"v_add_f32 v241, v241, v206\n"
"v_mfma_f32_16x16x32_bf16 v[196:199], a[116:119], a[36:39], v[196:199]\n"
"v_exp_f32 v207, v207\n"
"v_mfma_f32_16x16x32_bf16 v[196:199], a[120:123], a[40:43], v[196:199]\n"
"s_add_u32 m0, s58, 1024\n"
"v_add_f32 v241, v241, v207\n"
"v_cvt_pk_bf16_f32 v215, v206, v207\n"
"v_mfma_f32_16x16x32_bf16 v[196:199], a[124:127], a[44:47], v[196:199]\n"
"s_add_u32 s59, s57, 128\n"
"buffer_load_dwordx4 v252, s[44:47], s59 offen lds\n"
"v_exp_f32 v192, v192\n"
"v_mfma_f32_16x16x32_bf16 v[32:35], a[160:163], v[212:215], v[32:35]\n"
"ds_read_b128 a[156:159], v246 offset:13824\n"
"v_add_f32 v242, v242, v192\n"
"v_mfma_f32_16x16x32_bf16 v[36:39], a[164:167], v[212:215], v[36:39]\n"
"buffer_load_dwordx4 v253, s[44:47], s59 offen offset:2048 lds\n"
"v_exp_f32 v193, v193\n"
"v_mfma_f32_16x16x32_bf16 v[40:43], a[168:171], v[212:215], v[40:43]\n"
"Lhop6%=:\n"
"s_cbranch_execz Lhopret%=\n"
"v_add_f32 v242, v242, v193\n"
"v_cvt_pk_bf16_f32 v208, v192, v193\n"
"v_mfma_f32_16x16x32_bf16 v[44:47], a[172:175], v[212:215], v[44:47]\n"
"v_exp_f32 v194, v194\n"
"v_mfma_f32_16x16x32_bf16 v[48:51], a[176:179], v[212:215], v[48:51]\n"
"s_mov_b32 m0, 1024\n"
"ds_read_b64_tr_b16 a[192:193], v247 offset:40960\n"
"v_add_f32 v242, v242, v194\n"
"v_mfma_f32_16x16x32_bf16 v[52:55], a[180:183], v[212:215], v[52:55]\n"
"s_add_u32 s59, s56, 128\n"
"buffer_load_dwordx4 v252, s[36:39], s59 offen lds\n"
"v_exp_f32 v195, v195\n"
"v_mfma_f32_16x16x32_bf16 v[56:59], a[184:187], v[212:215], v[56:59]\n"
"ds_read_b64_tr_b16 a[194:195], v247 offset:45056\n"
"v_add_f32 v242, v242, v195\n"
"v_cvt_pk_bf16_f32 v209, v194, v195\n"
"v_mfma_f32_16x16x32_bf16 v[60:63], a[188:191], v[212:215], v[60:63]\n"
"buffer_load_dwordx4 v253, s[36:39], s59 offen offset:2048 lds\n"
"v_exp_f32 v196, v196\n"
"v_mfma_f32_16x16x32_bf16 v[200:203], a[96:99], a[48:51], v[228:231]\n"
"ds_read_b64_tr_b16 a[196:197], v248 offset:40960\n"
"v_add_f32 v242, v242, v196\n"
"v_mfma_f32_16x16x32_bf16 v[200:203], a[100:103], a[52:55], v[200:203]\n"
"v_exp_f32 v197, v197\n"
"v_mfma_f32_16x16x32_bf16 v[200:203], a[104:107], a[56:59], v[200:203]\n"
"s_mov_b32 m0, 4096\n"
"v_add_f32 v242, v242, v197\n"
"v_cvt_pk_bf16_f32 v210, v196, v197\n"
"v_mfma_f32_16x16x32_bf16 v[200:203], a[108:111], a[60:63], v[200:203]\n"
"s_add_u32 s59, s56, 4096\n"
"buffer_load_dwordx4 v252, s[36:39], s59 offen lds\n"
"v_exp_f32 v198, v198\n"
"v_mfma_f32_16x16x32_bf16 v[204:207], a[112:115], a[48:51], v[228:231]\n"
"ds_read_b64_tr_b16 a[198:199], v248 offset:45056\n"
"v_add_f32 v242, v242, v198\n"
"v_mfma_f32_16x16x32_bf16 v[204:207], a[116:119], a[52:55], v[204:207]\n"
"buffer_load_dwordx4 v253, s[36:39], s59 offen offset:2048 lds\n"
"v_exp_f32 v199, v199\n"
"v_mfma_f32_16x16x32_bf16 v[204:207], a[120:123], a[56:59], v[204:207]\n"
"v_add_f32 v242, v242, v199\n"
"v_cvt_pk_bf16_f32 v211, v198, v199\n"
"v_mfma_f32_16x16x32_bf16 v[204:207], a[124:127], a[60:63], v[204:207]\n"
"v_exp_f32 v200, v200\n"
"v_mfma_f32_16x16x32_bf16 v[64:67], a[160:163], v[208:211], v[64:67]\n"
"s_mov_b32 m0, 5120\n"
"ds_read_b64_tr_b16 a[200:201], v247 offset:41472\n"
"v_add_f32 v243, v243, v200\n"
"v_mfma_f32_16x16x32_bf16 v[68:71], a[164:167], v[208:211], v[68:71]\n"
"s_add_u32 s59, s56, 4224\n"
"buffer_load_dwordx4 v252, s[36:39], s59 offen lds\n"
"v_exp_f32 v201, v201\n"
"v_mfma_f32_16x16x32_bf16 v[72:75], a[168:171], v[208:211], v[72:75]\n"
"v_add_f32 v243, v243, v201\n"
"v_cvt_pk_bf16_f32 v212, v200, v201\n"
"v_mfma_f32_16x16x32_bf16 v[76:79], a[172:175], v[208:211], v[76:79]\n"
"v_exp_f32 v202, v202\n"
"v_mfma_f32_16x16x32_bf16 v[80:83], a[176:179], v[208:211], v[80:83]\n"
"ds_read_b64_tr_b16 a[202:203], v247 offset:45568\n"
"v_add_f32 v243, v243, v202\n"
"v_mfma_f32_16x16x32_bf16 v[84:87], a[180:183], v[208:211], v[84:87]\n"
"buffer_load_dwordx4 v253, s[36:39], s59 offen offset:2048 lds\n"
"v_exp_f32 v203, v203\n"
"v_mfma_f32_16x16x32_bf16 v[88:91], a[184:187], v[208:211], v[88:91]\n"
"s_mov_b32 m0, 32768\n"
"v_add_f32 v243, v243, v203\n"
"v_cvt_pk_bf16_f32 v213, v202, v203\n"
"v_mfma_f32_16x16x32_bf16 v[92:95], a[188:191], v[208:211], v[92:95]\n"
"s_add_u32 s59, s56, 0\n"
"buffer_load_dwordx4 v252, s[40:43], s59 offen lds\n"
"v_exp_f32 v204, v204\n"
"v_mfma_f32_16x16x32_bf16 v[192:195], a[96:99], a[64:67], v[232:235]\n"
"ds_read_b64_tr_b16 a[204:205], v248 offset:41472\n"
"v_add_f32 v243, v243, v204\n"
"v_mfma_f32_16x16x32_bf16 v[192:195], a[100:103], a[68:71], v[192:195]\n"
"v_exp_f32 v205, v205\n"
"v_mfma_f32_16x16x32_bf16 v[192:195], a[104:107], a[72:75], v[192:195]\n"
"ds_read_b64_tr_b16 a[206:207], v248 offset:45568\n"
"v_add_f32 v243, v243, v205\n"
"v_cvt_pk_bf16_f32 v214, v204, v205\n"
"v_mfma_f32_16x16x32_bf16 v[192:195], a[108:111], a[76:79], v[192:195]\n"
"buffer_load_dwordx4 v252, s[40:43], s59 offen offset:2048 lds\n"
"v_exp_f32 v206, v206\n"
"v_mfma_f32_16x16x32_bf16 v[196:199], a[112:115], a[64:67], v[232:235]\n"
"s_mov_b32 m0, 33792\n"
"ds_read_b64_tr_b16 a[208:209], v247 offset:41984\n"
"v_add_f32 v243, v243, v206\n"
"v_mfma_f32_16x16x32_bf16 v[196:199], a[116:119], a[68:71], v[196:199]\n"
"s_add_u32 s59, s56, 128\n"
"buffer_load_dwordx4 v252, s[40:43], s59 offen lds\n"
"v_exp_f32 v207, v207\n"
"v_mfma_f32_16x16x32_bf16 v[196:199], a[120:123], a[72:75], v[196:199]\n"
"v_add_f32 v243, v243, v207\n"
"v_cvt_pk_bf16_f32 v215, v206, v207\n"
"v_mfma_f32_16x16x32_bf16 v[196:199], a[124:127], a[76:79], v[196:199]\n"
"v_exp_f32 v192, v192\n"
"v_mfma_f32_16x16x32_bf16 v[96:99], a[160:163], v[212:215], v[96:99]\n"
"ds_read_b64_tr_b16 a[210:211], v247 offset:46080\n"
"v_add_f32 v244, v244, v192\n"
"v_mfma_f32_16x16x32_bf16 v[100:103], a[164:167], v[212:215], v[100:103]\n"
"buffer_load_dwordx4 v252, s[40:43], s59 offen offset:2048 lds\n"
"v_exp_f32 v193, v193\n"
"v_mfma_f32_16x16x32_bf16 v[104:107], a[168:171], v[212:215], v[104:107]\n"
"s_mov_b32 m0, 36864\n"
"v_add_f32 v244, v244, v193\n"
"v_cvt_pk_bf16_f32 v208, v192, v193\n"
"v_mfma_f32_16x16x32_bf16 v[108:111], a[172:175], v[212:215], v[108:111]\n"
"s_add_u32 s59, s56, 4096\n"
"buffer_load_dwordx4 v252, s[40:43], s59 offen lds\n"
"v_exp_f32 v194, v194\n"
"v_mfma_f32_16x16x32_bf16 v[112:115], a[176:179], v[212:215], v[112:115]\n"
"ds_read_b64_tr_b16 a[212:213], v248 offset:41984\n"
"v_add_f32 v244, v244, v194\n"
"v_mfma_f32_16x16x32_bf16 v[116:119], a[180:183], v[212:215], v[116:119]\n"
"v_exp_f32 v195, v195\n"
"v_mfma_f32_16x16x32_bf16 v[120:123], a[184:187], v[212:215], v[120:123]\n"
"v_add_f32 v244, v244, v195\n"
"v_cvt_pk_bf16_f32 v209, v194, v195\n"
"v_mfma_f32_16x16x32_bf16 v[124:127], a[188:191], v[212:215], v[124:127]\n"
"s_waitcnt lgkmcnt(2)\n"
"buffer_load_dwordx4 v252, s[40:43], s59 offen offset:2048 lds\n"
"v_exp_f32 v196, v196\n"
"v_mfma_f32_16x16x32_bf16 v[200:203], a[128:131], a[0:3], v[216:219]\n"
"s_mov_b32 m0, 37888\n"
"ds_read_b64_tr_b16 a[214:215], v248 offset:46080\n"
"v_add_f32 v244, v244, v196\n"
"v_mfma_f32_16x16x32_bf16 v[200:203], a[132:135], a[4:7], v[200:203]\n"
"s_add_u32 s59, s56, 4224\n"
"buffer_load_dwordx4 v252, s[40:43], s59 offen lds\n"
"v_exp_f32 v197, v197\n"
"v_mfma_f32_16x16x32_bf16 v[200:203], a[136:139], a[8:11], v[200:203]\n"
"v_add_f32 v244, v244, v197\n"
"v_cvt_pk_bf16_f32 v210, v196, v197\n"
"v_mfma_f32_16x16x32_bf16 v[200:203], a[140:143], a[12:15], v[200:203]\n"
"v_exp_f32 v198, v198\n"
"v_mfma_f32_16x16x32_bf16 v[204:207], a[144:147], a[0:3], v[216:219]\n"
"ds_read_b64_tr_b16 a[216:217], v247 offset:42496\n"
"v_add_f32 v244, v244, v198\n"
"v_mfma_f32_16x16x32_bf16 v[204:207], a[148:151], a[4:7], v[204:207]\n"
"buffer_load_dwordx4 v252, s[40:43], s59 offen offset:2048 lds\n"
"v_exp_f32 v199, v199\n"
"v_mfma_f32_16x16x32_bf16 v[204:207], a[152:155], a[8:11], v[204:207]\n"
"s_add_u32 s58, s58, s69\n"
"ds_read_b64_tr_b16 a[218:219], v247 offset:46592\n"
"v_add_f32 v244, v244, v199\n"
"v_cvt_pk_bf16_f32 v211, v198, v199\n"
"v_mfma_f32_16x16x32_bf16 v[204:207], a[156:159], a[12:15], v[204:207]\n"
"s_add_u32 s57, s57, 4096\n"
"v_exp_f32 v200, v200\n"
"v_mfma_f32_16x16x32_bf16 v[128:131], a[160:163], v[208:211], v[128:131]\n"
"ds_read_b64_tr_b16 a[220:221], v248 offset:42496\n"
"v_add_f32 v240, v240, v200\n"
"v_mfma_f32_16x16x32_bf16 v[132:135], a[164:167], v[208:211], v[132:135]\n"
"v_exp_f32 v201, v201\n"
"v_mfma_f32_16x16x32_bf16 v[136:139], a[168:171], v[208:211], v[136:139]\n"
"v_add_f32 v240, v240, v201\n"
"v_cvt_pk_bf16_f32 v212, v200, v201\n"
"v_mfma_f32_16x16x32_bf16 v[140:143], a[172:175], v[208:211], v[140:143]\n"
"v_exp_f32 v202, v202\n"
"v_mfma_f32_16x16x32_bf16 v[144:147], a[176:179], v[208:211], v[144:147]\n"
"ds_read_b64_tr_b16 a[222:223], v248 offset:46592\n"
"v_add_f32 v240, v240, v202\n"
"v_mfma_f32_16x16x32_bf16 v[148:151], a[180:183], v[208:211], v[148:151]\n"
"v_exp_f32 v203, v203\n"
"v_mfma_f32_16x16x32_bf16 v[152:155], a[184:187], v[208:211], v[152:155]\n"
"v_add_f32 v240, v240, v203\n"
"v_cvt_pk_bf16_f32 v213, v202, v203\n"
"v_mfma_f32_16x16x32_bf16 v[156:159], a[188:191], v[208:211], v[156:159]\n"
"LMB3%=:\n"
"v_exp_f32 v204, v204\n"
"v_mfma_f32_16x16x32_bf16 v[192:195], a[128:131], a[16:19], v[220:223]\n"
"ds_read_b128 a[96:99], v246 offset:16384\n"
"v_add_f32 v240, v240, v204\n"
"v_mfma_f32_16x16x32_bf16 v[192:195], a[132:135], a[20:23], v[192:195]\n"
"v_exp_f32 v205, v205\n"
"v_mfma_f32_16x16x32_bf16 v[192:195], a[136:139], a[24:27], v[192:195]\n"
"v_add_f32 v240, v240, v205\n"
"v_cvt_pk_bf16_f32 v214, v204, v205\n"
"v_mfma_f32_16x16x32_bf16 v[192:195], a[140:143], a[28:31], v[192:195]\n"
"v_exp_f32 v206, v206\n"
"v_mfma_f32_16x16x32_bf16 v[196:199], a[144:147], a[16:19], v[220:223]\n"
"ds_read_b128 a[100:103], v246 offset:16896\n"
"v_add_f32 v240, v240, v206\n"
"v_mfma_f32_16x16x32_bf16 v[196:199], a[148:151], a[20:23], v[196:199]\n"
"s_waitcnt lgkmcnt(2)\n"
"v_exp_f32 v207, v207\n"
"v_mfma_f32_16x16x32_bf16 v[196:199], a[152:155], a[24:27], v[196:199]\n"
"s_waitcnt vmcnt(39)\n"
"v_add_f32 v240, v240, v207\n"
"v_cvt_pk_bf16_f32 v215, v206, v207\n"
"v_mfma_f32_16x16x32_bf16 v[196:199], a[156:159], a[28:31], v[196:199]\n"
"s_barrier\n"
"v_exp_f32 v192, v192\n"
"v_mfma_f32_16x16x32_bf16 v[0:3], a[192:195], v[212:215], v[0:3]\n"
"s_add_u32 s56, s56, 8192\n"
"ds_read_b128 a[104:107], v246 offset:17408\n"
"v_add_f32 v241, v241, v192\n"
"v_mfma_f32_16x16x32_bf16 v[4:7], a[196:199], v[212:215], v[4:7]\n"
"v_exp_f32 v193, v193\n"
"v_mfma_f32_16x16x32_bf16 v[8:11], a[200:203], v[212:215], v[8:11]\n"
"s_mov_b32 m0, 8192\n"
"ds_read_b128 a[108:111], v246 offset:17920\n"
"v_add_f32 v241, v241, v193\n"
"v_cvt_pk_bf16_f32 v208, v192, v193\n"
"v_mfma_f32_16x16x32_bf16 v[12:15], a[204:207], v[212:215], v[12:15]\n"
"s_add_u32 s59, s56, 0\n"
"buffer_load_dwordx4 v252, s[36:39], s59 offen lds\n"
"v_exp_f32 v194, v194\n"
"v_mfma_f32_16x16x32_bf16 v[16:19], a[208:211], v[212:215], v[16:19]\n"
"ds_read_b128 a[112:115], v246 offset:20480\n"
"v_add_f32 v241, v241, v194\n"
"v_mfma_f32_16x16x32_bf16 v[20:23], a[212:215], v[212:215], v[20:23]\n"
"buffer_load_dwordx4 v253, s[36:39], s59 offen offset:2048 lds\n"
"v_exp_f32 v195, v195\n"
"v_mfma_f32_16x16x32_bf16 v[24:27], a[216:219], v[212:215], v[24:27]\n"
"v_add_f32 v241, v241, v195\n"
"v_cvt_pk_bf16_f32 v209, v194, v195\n"
"v_mfma_f32_16x16x32_bf16 v[28:31], a[220:223], v[212:215], v[28:31]\n"
"v_exp_f32 v196, v196\n"
"v_mfma_f32_16x16x32_bf16 v[200:203], a[128:131], a[32:35], v[224:227]\n"
"s_add_u32 m0, s58, 0\n"
"ds_read_b128 a[116:119], v246 offset:20992\n"
"v_add_f32 v241, v241, v196\n"
"v_mfma_f32_16x16x32_bf16 v[200:203], a[132:135], a[36:39], v[200:203]\n"
"s_add_u32 s59, s57, 0\n"
"buffer_load_dwordx4 v252, s[44:47], s59 offen lds\n"
"v_exp_f32 v197, v197\n"
"v_mfma_f32_16x16x32_bf16 v[200:203], a[136:139], a[40:43], v[200:203]\n"
"v_add_f32 v241, v241, v197\n"
"v_cvt_pk_bf16_f32 v210, v196, v197\n"
"v_mfma_f32_16x16x32_bf16 v[200:203], a[140:143], a[44:47], v[200:203]\n"
"buffer_load_dwordx4 v253, s[44:47], s59 offen offset:2048 lds\n"
"v_exp_f32 v198, v198\n"
"v_mfma_f32_16x16x32_bf16 v[204:207], a[144:147], a[32:35], v[224:227]\n"
"ds_read_b128 a[120:123], v246 offset:21504\n"
"v_add_f32 v241, v241, v198\n"
"v_mfma_f32_16x16x32_bf16 v[204:207], a[148:151], a[36:39], v[204:207]\n"
"v_exp_f32 v199, v199\n"
"v_mfma_f32_16x16x32_bf16 v[204:207], a[152:155], a[40:43], v[204:207]\n"
"s_add_u32 m0, s58, 1024\n"
"v_add_f32 v241, v241, v199\n"
"v_cvt_pk_bf16_f32 v211, v198, v199\n"
"v_mfma_f32_16x16x32_bf16 v[204:207], a[156:159], a[44:47], v[204:207]\n"
"s_add_u32 s59, s57, 128\n"
"buffer_load_dwordx4 v252, s[44:47], s59 offen lds\n"
"v_exp_f32 v200, v200\n"
"v_mfma_f32_16x16x32_bf16 v[32:35], a[192:195], v[208:211], v[32:35]\n"
"ds_read_b128 a[124:127], v246 offset:22016\n"
"v_add_f32 v242, v242, v200\n"
"v_mfma_f32_16x16x32_bf16 v[36:39], a[196:199], v[208:211], v[36:39]\n"
"buffer_load_dwordx4 v253, s[44:47], s59 offen offset:2048 lds\n"
"v_exp_f32 v201, v201\n"
"v_mfma_f32_16x16x32_bf16 v[40:43], a[200:203], v[208:211], v[40:43]\n"
"v_add_f32 v242, v242, v201\n"
"v_cvt_pk_bf16_f32 v212, v200, v201\n"
"v_mfma_f32_16x16x32_bf16 v[44:47], a[204:207], v[208:211], v[44:47]\n"
"v_exp_f32 v202, v202\n"
"v_mfma_f32_16x16x32_bf16 v[48:51], a[208:211], v[208:211], v[48:51]\n"
"s_mov_b32 m0, 9216\n"
"ds_read_b64_tr_b16 a[160:161], v247 offset:49152\n"
"v_add_f32 v242, v242, v202\n"
"v_mfma_f32_16x16x32_bf16 v[52:55], a[212:215], v[208:211], v[52:55]\n"
"s_add_u32 s59, s56, 128\n"
"buffer_load_dwordx4 v252, s[36:39], s59 offen lds\n"
"v_exp_f32 v203, v203\n"
"v_mfma_f32_16x16x32_bf16 v[56:59], a[216:219], v[208:211], v[56:59]\n"
"ds_read_b64_tr_b16 a[162:163], v247 offset:53248\n"
"v_add_f32 v242, v242, v203\n"
"v_cvt_pk_bf16_f32 v213, v202, v203\n"
"v_mfma_f32_16x16x32_bf16 v[60:63], a[220:223], v[208:211], v[60:63]\n"
"buffer_load_dwordx4 v253, s[36:39], s59 offen offset:2048 lds\n"
"v_exp_f32 v204, v204\n"
"v_mfma_f32_16x16x32_bf16 v[192:195], a[128:131], a[48:51], v[228:231]\n"
"ds_read_b64_tr_b16 a[164:165], v248 offset:49152\n"
"v_add_f32 v242, v242, v204\n"
"v_mfma_f32_16x16x32_bf16 v[192:195], a[132:135], a[52:55], v[192:195]\n"
"v_exp_f32 v205, v205\n"
"v_mfma_f32_16x16x32_bf16 v[192:195], a[136:139], a[56:59], v[192:195]\n"
"s_mov_b32 m0, 12288\n"
"v_add_f32 v242, v242, v205\n"
"v_cvt_pk_bf16_f32 v214, v204, v205\n"
"v_mfma_f32_16x16x32_bf16 v[192:195], a[140:143], a[60:63], v[192:195]\n"
"s_add_u32 s59, s56, 4096\n"
"buffer_load_dwordx4 v252, s[36:39], s59 offen lds\n"
"v_exp_f32 v206, v206\n"
"v_mfma_f32_16x16x32_bf16 v[196:199], a[144:147], a[48:51], v[228:231]\n"
"ds_read_b64_tr_b16 a[166:167], v248 offset:53248\n"
"v_add_f32 v242, v242, v206\n"
"v_mfma_f32_16x16x32_bf16 v[196:199], a[148:151], a[52:55], v[196:199]\n"
"buffer_load_dwordx4 v253, s[36:39], s59 offen offset:2048 lds\n"
"v_exp_f32 v207, v207\n"
"v_mfma_f32_16x16x32_bf16 v[196:199], a[152:155], a[56:59], v[196:199]\n"
"v_add_f32 v242, v242, v207\n"
"v_cvt_pk_bf16_f32 v215, v206, v207\n"
"v_mfma_f32_16x16x32_bf16 v[196:199], a[156:159], a[60:63], v[196:199]\n"
"v_exp_f32 v192, v192\n"
"v_mfma_f32_16x16x32_bf16 v[64:67], a[192:195], v[212:215], v[64:67]\n"
"s_mov_b32 m0, 13312\n"
"ds_read_b64_tr_b16 a[168:169], v247 offset:49664\n"
"v_add_f32 v243, v243, v192\n"
"v_mfma_f32_16x16x32_bf16 v[68:71], a[196:199], v[212:215], v[68:71]\n"
"s_add_u32 s59, s56, 4224\n"
"buffer_load_dwordx4 v252, s[36:39], s59 offen lds\n"
"v_exp_f32 v193, v193\n"
"v_mfma_f32_16x16x32_bf16 v[72:75], a[200:203], v[212:215], v[72:75]\n"
"v_add_f32 v243, v243, v193\n"
"v_cvt_pk_bf16_f32 v208, v192, v193\n"
"v_mfma_f32_16x16x32_bf16 v[76:79], a[204:207], v[212:215], v[76:79]\n"
"v_exp_f32 v194, v194\n"
"v_mfma_f32_16x16x32_bf16 v[80:83], a[208:211], v[212:215], v[80:83]\n"
"ds_read_b64_tr_b16 a[170:171], v247 offset:53760\n"
"v_add_f32 v243, v243, v194\n"
"v_mfma_f32_16x16x32_bf16 v[84:87], a[212:215], v[212:215], v[84:87]\n"
"buffer_load_dwordx4 v253, s[36:39], s59 offen offset:2048 lds\n"
"v_exp_f32 v195, v195\n"
"v_mfma_f32_16x16x32_bf16 v[88:91], a[216:219], v[212:215], v[88:91]\n"
"s_mov_b32 m0, 40960\n"
"v_add_f32 v243, v243, v195\n"
"v_cvt_pk_bf16_f32 v209, v194, v195\n"
"v_mfma_f32_16x16x32_bf16 v[92:95], a[220:223], v[212:215], v[92:95]\n"
"s_add_u32 s59, s56, 0\n"
"buffer_load_dwordx4 v252, s[40:43], s59 offen lds\n"
"v_exp_f32 v196, v196\n"
"v_mfma_f32_16x16x32_bf16 v[200:203], a[128:131], a[64:67], v[232:235]\n"
"ds_read_b64_tr_b16 a[172:173], v248 offset:49664\n"
"v_add_f32 v243, v243, v196\n"
"v_mfma_f32_16x16x32_bf16 v[200:203], a[132:135], a[68:71], v[200:203]\n"
"v_exp_f32 v197, v197\n"
"v_mfma_f32_16x16x32_bf16 v[200:203], a[136:139], a[72:75], v[200:203]\n"
"ds_read_b64_tr_b16 a[174:175], v248 offset:53760\n"
"v_add_f32 v243, v243, v197\n"
"v_cvt_pk_bf16_f32 v210, v196, v197\n"
"v_mfma_f32_16x16x32_bf16 v[200:203], a[140:143], a[76:79], v[200:203]\n"
"buffer_load_dwordx4 v252, s[40:43], s59 offen offset:2048 lds\n"
"v_exp_f32 v198, v198\n"
"v_mfma_f32_16x16x32_bf16 v[204:207], a[144:147], a[64:67], v[232:235]\n"
"s_mov_b32 m0, 41984\n"
"ds_read_b64_tr_b16 a[176:177], v247 offset:50176\n"
"v_add_f32 v243, v243, v198\n"
"v_mfma_f32_16x16x32_bf16 v[204:207], a[148:151], a[68:71], v[204:207]\n"
"s_add_u32 s59, s56, 128\n"
"buffer_load_dwordx4 v252, s[40:43], s59 offen lds\n"
"v_exp_f32 v199, v199\n"
"v_mfma_f32_16x16x32_bf16 v[204:207], a[152:155], a[72:75], v[204:207]\n"
"v_add_f32 v243, v243, v199\n"
"v_cvt_pk_bf16_f32 v211, v198, v199\n"
"v_mfma_f32_16x16x32_bf16 v[204:207], a[156:159], a[76:79], v[204:207]\n"
"v_exp_f32 v200, v200\n"
"v_mfma_f32_16x16x32_bf16 v[96:99], a[192:195], v[208:211], v[96:99]\n"
"ds_read_b64_tr_b16 a[178:179], v247 offset:54272\n"
"v_add_f32 v244, v244, v200\n"
"v_mfma_f32_16x16x32_bf16 v[100:103], a[196:199], v[208:211], v[100:103]\n"
"buffer_load_dwordx4 v252, s[40:43], s59 offen offset:2048 lds\n"
"v_exp_f32 v201, v201\n"
"v_mfma_f32_16x16x32_bf16 v[104:107], a[200:203], v[208:211], v[104:107]\n"
"s_mov_b32 m0, 45056\n"
"v_add_f32 v244, v244, v201\n"
"v_cvt_pk_bf16_f32 v212, v200, v201\n"
"v_mfma_f32_16x16x32_bf16 v[108:111], a[204:207], v[208:211], v[108:111]\n"
"s_add_u32 s59, s56, 4096\n"
"buffer_load_dwordx4 v252, s[40:43], s59 offen lds\n"
"v_exp_f32 v202, v202\n"
"v_mfma_f32_16x16x32_bf16 v[112:115], a[208:211], v[208:211], v[112:115]\n"
"ds_read_b64_tr_b16 a[180:181], v248 offset:50176\n"
"v_add_f32 v244, v244, v202\n"
"v_mfma_f32_16x16x32_bf16 v[116:119], a[212:215], v[208:211], v[116:119]\n"
"v_exp_f32 v203, v203\n"
"v_mfma_f32_16x16x32_bf16 v[120:123], a[216:219], v[208:211], v[120:123]\n"
"v_add_f32 v244, v244, v203\n"
"v_cvt_pk_bf16_f32 v213, v202, v203\n"
"v_mfma_f32_16x16x32_bf16 v[124:127], a[220:223], v[208:211], v[124:127]\n"
"s_waitcnt lgkmcnt(2)\n"
"buffer_load_dwordx4 v252, s[40:43], s59 offen offset:2048 lds\n"
"v_exp_f32 v204, v204\n"
"v_mfma_f32_16x16x32_bf16 v[192:195], a[96:99], a[0:3], v[216:219]\n"
"s_mov_b32 m0, 46080\n"
"ds_read_b64_tr_b16 a[182:183], v248 offset:54272\n"
"v_add_f32 v244, v244, v204\n"
"v_mfma_f32_16x16x32_bf16 v[192:195], a[100:103], a[4:7], v[192:195]\n"
"s_add_u32 s59, s56, 4224\n"
"buffer_load_dwordx4 v252, s[40:43], s59 offen lds\n"
"v_exp_f32 v205, v205\n"
"v_mfma_f32_16x16x32_bf16 v[192:195], a[104:107], a[8:11], v[192:195]\n"
"v_add_f32 v244, v244, v205\n"
"v_cvt_pk_bf16_f32 v214, v204, v205\n"
"v_mfma_f32_16x16x32_bf16 v[192:195], a[108:111], a[12:15], v[192:195]\n"
"v_exp_f32 v206, v206\n"
"v_mfma_f32_16x16x32_bf16 v[196:199], a[112:115], a[0:3], v[216:219]\n"
"ds_read_b64_tr_b16 a[184:185], v247 offset:50688\n"
"v_add_f32 v244, v244, v206\n"
"v_mfma_f32_16x16x32_bf16 v[196:199], a[116:119], a[4:7], v[196:199]\n"
"buffer_load_dwordx4 v252, s[40:43], s59 offen offset:2048 lds\n"
"v_exp_f32 v207, v207\n"
"v_mfma_f32_16x16x32_bf16 v[196:199], a[120:123], a[8:11], v[196:199]\n"
"s_add_u32 s58, s58, s69\n"
"ds_read_b64_tr_b16 a[186:187], v247 offset:54784\n"
"v_add_f32 v244, v244, v207\n"
"v_cvt_pk_bf16_f32 v215, v206, v207\n"
"v_mfma_f32_16x16x32_bf16 v[196:199], a[124:127], a[12:15], v[196:199]\n"
"s_add_u32 s57, s57, 4096\n"
"v_exp_f32 v192, v192\n"
"v_mfma_f32_16x16x32_bf16 v[128:131], a[192:195], v[212:215], v[128:131]\n"
"ds_read_b64_tr_b16 a[188:189], v248 offset:50688\n"
"v_add_f32 v240, v240, v192\n"
"v_mfma_f32_16x16x32_bf16 v[132:135], a[196:199], v[212:215], v[132:135]\n"
"v_exp_f32 v193, v193\n"
"v_mfma_f32_16x16x32_bf16 v[136:139], a[200:203], v[212:215], v[136:139]\n"
"v_add_f32 v240, v240, v193\n"
"v_cvt_pk_bf16_f32 v208, v192, v193\n"
"v_mfma_f32_16x16x32_bf16 v[140:143], a[204:207], v[212:215], v[140:143]\n"
"v_exp_f32 v194, v194\n"
"v_mfma_f32_16x16x32_bf16 v[144:147], a[208:211], v[212:215], v[144:147]\n"
"s_cmp_eq_u32 s54, 5\n"
"ds_read_b64_tr_b16 a[190:191], v248 offset:54784\n"
"v_add_f32 v240, v240, v194\n"
"v_mfma_f32_16x16x32_bf16 v[148:151], a[212:215], v[212:215], v[148:151]\n"
"s_cselect_b64 vcc, -1, 0\n"
"v_exp_f32 v195, v195\n"
"v_mfma_f32_16x16x32_bf16 v[152:155], a[216:219], v[212:215], v[152:155]\n"
"s_cmp_lt_u32 s54, 5\n"
"v_add_f32 v240, v240, v195\n"
"v_cvt_pk_bf16_f32 v209, v194, v195\n"
"v_mfma_f32_16x16x32_bf16 v[156:159], a[220:223], v[212:215], v[156:159]\n"
"s_cbranch_scc1 LMB0%=\n"
"s_cbranch_vccnz LSEAMB%=\n"
"v_exp_f32 v196, v196\n"
"v_mfma_f32_16x16x32_bf16 v[200:203], a[96:99], a[16:19], v[220:223]\n"
"ds_read_b128 a[128:131], v246 offset:24576\n"
"v_add_f32 v240, v240, v196\n"
"v_mfma_f32_16x16x32_bf16 v[200:203], a[100:103], a[20:23], v[200:203]\n"
"v_exp_f32 v197, v197\n"
"v_mfma_f32_16x16x32_bf16 v[200:203], a[104:107], a[24:27], v[200:203]\n"
"s_cmp_lt_u32 s54, 10\n"
"v_add_f32 v240, v240, v197\n"
"v_cvt_pk_bf16_f32 v210, v196, v197\n"
"v_mfma_f32_16x16x32_bf16 v[200:203], a[108:111], a[28:31], v[200:203]\n"
"s_cselect_b32 s60, 4352, 0\n"
"v_exp_f32 v198, v198\n"
"v_mfma_f32_16x16x32_bf16 v[204:207], a[112:115], a[16:19], v[220:223]\n"
"ds_read_b128 a[132:135], v246 offset:25088\n"
"v_add_f32 v240, v240, v198\n"
"v_add_u32 v161, s60, v161\n"
"v_mfma_f32_16x16x32_bf16 v[204:207], a[116:119], a[20:23], v[204:207]\n"
"s_waitcnt lgkmcnt(2)\n"
"v_exp_f32 v199, v199\n"
"v_mfma_f32_16x16x32_bf16 v[204:207], a[120:123], a[24:27], v[204:207]\n"
"s_waitcnt vmcnt(38)\n"
"v_add_f32 v240, v240, v199\n"
"v_cvt_pk_bf16_f32 v211, v198, v199\n"
"v_mfma_f32_16x16x32_bf16 v[204:207], a[124:127], a[28:31], v[204:207]\n"
"s_barrier\n"
"v_exp_f32 v200, v200\n"
"v_mfma_f32_16x16x32_bf16 v[0:3], a[160:163], v[208:211], v[0:3]\n"
"s_add_u32 s56, s56, 8192\n"
"ds_read_b128 a[136:139], v246 offset:25600\n"
"v_add_f32 v241, v241, v200\n"
"v_add_u32 v164, 2176, v161\n"
"v_mfma_f32_16x16x32_bf16 v[4:7], a[164:167], v[208:211], v[4:7]\n"
"v_exp_f32 v201, v201\n"
"v_mfma_f32_16x16x32_bf16 v[8:11], a[168:171], v[208:211], v[8:11]\n"
"s_mov_b32 m0, 16384\n"
"ds_read_b128 a[140:143], v246 offset:26112\n"
"v_add_f32 v241, v241, v201\n"
"v_cvt_pk_bf16_f32 v212, v200, v201\n"
"v_mfma_f32_16x16x32_bf16 v[12:15], a[172:175], v[208:211], v[12:15]\n"
"s_add_u32 s59, s56, 0\n"
"buffer_load_dwordx4 v252, s[36:39], s59 offen lds\n"
"v_exp_f32 v202, v202\n"
"v_mfma_f32_16x16x32_bf16 v[16:19], a[176:179], v[208:211], v[16:19]\n"
"ds_read_b128 a[144:147], v246 offset:28672\n"
"v_add_f32 v241, v241, v202\n"
"v_mfma_f32_16x16x32_bf16 v[20:23], a[180:183], v[208:211], v[20:23]\n"
"buffer_load_dwordx4 v253, s[36:39], s59 offen offset:2048 lds\n"
"v_exp_f32 v203, v203\n"
"v_mfma_f32_16x16x32_bf16 v[24:27], a[184:187], v[208:211], v[24:27]\n"
"v_add_f32 v241, v241, v203\n"
"v_cvt_pk_bf16_f32 v213, v202, v203\n"
"v_mfma_f32_16x16x32_bf16 v[28:31], a[188:191], v[208:211], v[28:31]\n"
"v_exp_f32 v204, v204\n"
"v_mfma_f32_16x16x32_bf16 v[192:195], a[96:99], a[32:35], v[224:227]\n"
"s_add_u32 m0, s58, 0\n"
"ds_read_b128 a[148:151], v246 offset:29184\n"
"v_add_f32 v241, v241, v204\n"
"v_mfma_f32_16x16x32_bf16 v[192:195], a[100:103], a[36:39], v[192:195]\n"
"s_add_u32 s59, s57, 0\n"
"buffer_load_dwordx4 v252, s[44:47], s59 offen lds\n"
"v_exp_f32 v205, v205\n"
"v_mfma_f32_16x16x32_bf16 v[192:195], a[104:107], a[40:43], v[192:195]\n"
"buffer_store_dwordx4 a[224:227], v255, s[48:51], s68 offen sc1\n"
"v_add_f32 v241, v241, v205\n"
"v_cvt_pk_bf16_f32 v214, v204, v205\n"
"v_mfma_f32_16x16x32_bf16 v[192:195], a[108:111], a[44:47], v[192:195]\n"
"buffer_load_dwordx4 v253, s[44:47], s59 offen offset:2048 lds\n"
"v_exp_f32 v206, v206\n"
"v_mfma_f32_16x16x32_bf16 v[196:199], a[112:115], a[32:35], v[224:227]\n"
"ds_read_b128 a[152:155], v246 offset:29696\n"
"v_add_f32 v241, v241, v206\n"
"v_mfma_f32_16x16x32_bf16 v[196:199], a[116:119], a[36:39], v[196:199]\n"
"v_exp_f32 v207, v207\n"
"v_mfma_f32_16x16x32_bf16 v[196:199], a[120:123], a[40:43], v[196:199]\n"
"s_add_u32 m0, s58, 1024\n"
"v_add_f32 v241, v241, v207\n"
"v_cvt_pk_bf16_f32 v215, v206, v207\n"
"v_mfma_f32_16x16x32_bf16 v[196:199], a[124:127], a[44:47], v[196:199]\n"
"s_add_u32 s59, s57, 128\n"
"buffer_load_dwordx4 v252, s[44:47], s59 offen lds\n"
"v_exp_f32 v192, v192\n"
"v_mfma_f32_16x16x32_bf16 v[32:35], a[160:163], v[212:215], v[32:35]\n"
"ds_read_b128 a[156:159], v246 offset:30208\n"
"v_add_f32 v242, v242, v192\n"
"v_mfma_f32_16x16x32_bf16 v[36:39], a[164:167], v[212:215], v[36:39]\n"
"buffer_load_dwordx4 v253, s[44:47], s59 offen offset:2048 lds\n"
"v_exp_f32 v193, v193\n"
"v_mfma_f32_16x16x32_bf16 v[40:43], a[168:171], v[212:215], v[40:43]\n"
"ds_read2_b64 a[224:227], v161 offset0:0 offset1:2\n"
"v_add_f32 v242, v242, v193\n"
"v_cvt_pk_bf16_f32 v208, v192, v193\n"
"v_mfma_f32_16x16x32_bf16 v[44:47], a[172:175], v[212:215], v[44:47]\n"
"v_exp_f32 v194, v194\n"
"v_mfma_f32_16x16x32_bf16 v[48:51], a[176:179], v[212:215], v[48:51]\n"
"s_mov_b32 m0, 17408\n"
"ds_read_b64_tr_b16 a[192:193], v247 offset:57344\n"
"v_add_f32 v242, v242, v194\n"
"v_mfma_f32_16x16x32_bf16 v[52:55], a[180:183], v[212:215], v[52:55]\n"
"s_add_u32 s59, s56, 128\n"
"buffer_load_dwordx4 v252, s[36:39], s59 offen lds\n"
"v_exp_f32 v195, v195\n"
"v_mfma_f32_16x16x32_bf16 v[56:59], a[184:187], v[212:215], v[56:59]\n"
"ds_read_b64_tr_b16 a[194:195], v247 offset:61440\n"
"v_add_f32 v242, v242, v195\n"
"v_cvt_pk_bf16_f32 v209, v194, v195\n"
"v_mfma_f32_16x16x32_bf16 v[60:63], a[188:191], v[212:215], v[60:63]\n"
"buffer_load_dwordx4 v253, s[36:39], s59 offen offset:2048 lds\n"
"v_exp_f32 v196, v196\n"
"v_mfma_f32_16x16x32_bf16 v[200:203], a[96:99], a[48:51], v[228:231]\n"
"ds_read_b64_tr_b16 a[196:197], v248 offset:57344\n"
"v_add_f32 v242, v242, v196\n"
"v_mfma_f32_16x16x32_bf16 v[200:203], a[100:103], a[52:55], v[200:203]\n"
"v_exp_f32 v197, v197\n"
"v_mfma_f32_16x16x32_bf16 v[200:203], a[104:107], a[56:59], v[200:203]\n"
"s_mov_b32 m0, 20480\n"
"buffer_store_dwordx4 a[228:231], v255, s[48:51], s68 offen offset:1024 sc1\n"
"v_add_f32 v242, v242, v197\n"
"v_cvt_pk_bf16_f32 v210, v196, v197\n"
"v_mfma_f32_16x16x32_bf16 v[200:203], a[108:111], a[60:63], v[200:203]\n"
"s_add_u32 s59, s56, 4096\n"
"buffer_load_dwordx4 v252, s[36:39], s59 offen lds\n"
"v_exp_f32 v198, v198\n"
"v_mfma_f32_16x16x32_bf16 v[204:207], a[112:115], a[48:51], v[228:231]\n"
"ds_read_b64_tr_b16 a[198:199], v248 offset:61440\n"
"v_add_f32 v242, v242, v198\n"
"v_mfma_f32_16x16x32_bf16 v[204:207], a[116:119], a[52:55], v[204:207]\n"
"buffer_load_dwordx4 v253, s[36:39], s59 offen offset:2048 lds\n"
"v_exp_f32 v199, v199\n"
"v_mfma_f32_16x16x32_bf16 v[204:207], a[120:123], a[56:59], v[204:207]\n"
"v_add_f32 v242, v242, v199\n"
"v_cvt_pk_bf16_f32 v211, v198, v199\n"
"v_mfma_f32_16x16x32_bf16 v[204:207], a[124:127], a[60:63], v[204:207]\n"
"v_exp_f32 v200, v200\n"
"v_mfma_f32_16x16x32_bf16 v[64:67], a[160:163], v[208:211], v[64:67]\n"
"s_mov_b32 m0, 21504\n"
"ds_read_b64_tr_b16 a[200:201], v247 offset:57856\n"
"v_add_f32 v243, v243, v200\n"
"v_mfma_f32_16x16x32_bf16 v[68:71], a[164:167], v[208:211], v[68:71]\n"
"s_add_u32 s59, s56, 4224\n"
"buffer_load_dwordx4 v252, s[36:39], s59 offen lds\n"
"v_exp_f32 v201, v201\n"
"v_mfma_f32_16x16x32_bf16 v[72:75], a[168:171], v[208:211], v[72:75]\n"
"ds_read2_b64 a[228:231], v161 offset0:136 offset1:138\n"
"v_add_f32 v243, v243, v201\n"
"v_cvt_pk_bf16_f32 v212, v200, v201\n"
"v_mfma_f32_16x16x32_bf16 v[76:79], a[172:175], v[208:211], v[76:79]\n"
"v_exp_f32 v202, v202\n"
"v_mfma_f32_16x16x32_bf16 v[80:83], a[176:179], v[208:211], v[80:83]\n"
"ds_read_b64_tr_b16 a[202:203], v247 offset:61952\n"
"v_add_f32 v243, v243, v202\n"
"v_mfma_f32_16x16x32_bf16 v[84:87], a[180:183], v[208:211], v[84:87]\n"
"buffer_load_dwordx4 v253, s[36:39], s59 offen offset:2048 lds\n"
"v_exp_f32 v203, v203\n"
"v_mfma_f32_16x16x32_bf16 v[88:91], a[184:187], v[208:211], v[88:91]\n"
"s_mov_b32 m0, 49152\n"
"v_add_f32 v243, v243, v203\n"
"v_cvt_pk_bf16_f32 v213, v202, v203\n"
"v_mfma_f32_16x16x32_bf16 v[92:95], a[188:191], v[208:211], v[92:95]\n"
"s_add_u32 s59, s56, 0\n"
"buffer_load_dwordx4 v252, s[40:43], s59 offen lds\n"
"v_exp_f32 v204, v204\n"
"v_mfma_f32_16x16x32_bf16 v[192:195], a[96:99], a[64:67], v[232:235]\n"
"ds_read_b64_tr_b16 a[204:205], v248 offset:57856\n"
"v_add_f32 v243, v243, v204\n"
"v_mfma_f32_16x16x32_bf16 v[192:195], a[100:103], a[68:71], v[192:195]\n"
"v_exp_f32 v205, v205\n"
"v_mfma_f32_16x16x32_bf16 v[192:195], a[104:107], a[72:75], v[192:195]\n"
"ds_read_b64_tr_b16 a[206:207], v248 offset:61952\n"
"v_add_f32 v243, v243, v205\n"
"v_cvt_pk_bf16_f32 v214, v204, v205\n"
"v_mfma_f32_16x16x32_bf16 v[192:195], a[108:111], a[76:79], v[192:195]\n"
"buffer_load_dwordx4 v252, s[40:43], s59 offen offset:2048 lds\n"
"v_exp_f32 v206, v206\n"
"v_mfma_f32_16x16x32_bf16 v[196:199], a[112:115], a[64:67], v[232:235]\n"
"s_mov_b32 m0, 50176\n"
"ds_read_b64_tr_b16 a[208:209], v247 offset:58368\n"
"v_add_f32 v243, v243, v206\n"
"v_mfma_f32_16x16x32_bf16 v[196:199], a[116:119], a[68:71], v[196:199]\n"
"s_add_u32 s59, s56, 128\n"
"buffer_load_dwordx4 v252, s[40:43], s59 offen lds\n"
"v_exp_f32 v207, v207\n"
"v_mfma_f32_16x16x32_bf16 v[196:199], a[120:123], a[72:75], v[196:199]\n"
"buffer_store_dwordx4 a[232:235], v255, s[48:51], s68 offen offset:2048 sc1\n"
"v_add_f32 v243, v243, v207\n"
"v_cvt_pk_bf16_f32 v215, v206, v207\n"
"v_mfma_f32_16x16x32_bf16 v[196:199], a[124:127], a[76:79], v[196:199]\n"
"v_exp_f32 v192, v192\n"
"v_mfma_f32_16x16x32_bf16 v[96:99], a[160:163], v[212:215], v[96:99]\n"
"ds_read_b64_tr_b16 a[210:211], v247 offset:62464\n"
"v_add_f32 v244, v244, v192\n"
"v_mfma_f32_16x16x32_bf16 v[100:103], a[164:167], v[212:215], v[100:103]\n"
"buffer_load_dwordx4 v252, s[40:43], s59 offen offset:2048 lds\n"
"v_exp_f32 v193, v193\n"
"v_mfma_f32_16x16x32_bf16 v[104:107], a[168:171], v[212:215], v[104:107]\n"
"s_mov_b32 m0, 53248\n"
"v_add_f32 v244, v244, v193\n"
"v_cvt_pk_bf16_f32 v208, v192, v193\n"
"v_mfma_f32_16x16x32_bf16 v[108:111], a[172:175], v[212:215], v[108:111]\n"
"s_add_u32 s59, s56, 4096\n"
"buffer_load_dwordx4 v252, s[40:43], s59 offen lds\n"
"v_exp_f32 v194, v194\n"
"v_mfma_f32_16x16x32_bf16 v[112:115], a[176:179], v[212:215], v[112:115]\n"
"Lhop7%=:\n"
"s_cbranch_execz Lhopret%=\n"
"ds_read_b64_tr_b16 a[212:213], v248 offset:58368\n"
"v_add_f32 v244, v244, v194\n"
"v_mfma_f32_16x16x32_bf16 v[116:119], a[180:183], v[212:215], v[116:119]\n"
"v_exp_f32 v195, v195\n"
"v_mfma_f32_16x16x32_bf16 v[120:123], a[184:187], v[212:215], v[120:123]\n"
"ds_read2_b64 a[232:235], v164 offset0:0 offset1:2\n"
"v_add_f32 v244, v244, v195\n"
"v_cvt_pk_bf16_f32 v209, v194, v195\n"
"v_mfma_f32_16x16x32_bf16 v[124:127], a[188:191], v[212:215], v[124:127]\n"
"s_waitcnt lgkmcnt(3)\n"
"buffer_load_dwordx4 v252, s[40:43], s59 offen offset:2048 lds\n"
"v_exp_f32 v196, v196\n"
"v_mfma_f32_16x16x32_bf16 v[200:203], a[128:131], a[0:3], v[216:219]\n"
"s_mov_b32 m0, 54272\n"
"ds_read_b64_tr_b16 a[214:215], v248 offset:62464\n"
"v_add_f32 v244, v244, v196\n"
"v_mfma_f32_16x16x32_bf16 v[200:203], a[132:135], a[4:7], v[200:203]\n"
"s_add_u32 s59, s56, 4224\n"
"buffer_load_dwordx4 v252, s[40:43], s59 offen lds\n"
"v_exp_f32 v197, v197\n"
"v_mfma_f32_16x16x32_bf16 v[200:203], a[136:139], a[8:11], v[200:203]\n"
"buffer_store_dwordx4 a[236:239], v255, s[48:51], s68 offen offset:3072 sc1\n"
"v_add_f32 v244, v244, v197\n"
"v_cvt_pk_bf16_f32 v210, v196, v197\n"
"v_mfma_f32_16x16x32_bf16 v[200:203], a[140:143], a[12:15], v[200:203]\n"
"v_exp_f32 v198, v198\n"
"v_mfma_f32_16x16x32_bf16 v[204:207], a[144:147], a[0:3], v[216:219]\n"
"ds_read_b64_tr_b16 a[216:217], v247 offset:58880\n"
"v_add_f32 v244, v244, v198\n"
"v_mfma_f32_16x16x32_bf16 v[204:207], a[148:151], a[4:7], v[204:207]\n"
"buffer_load_dwordx4 v252, s[40:43], s59 offen offset:2048 lds\n"
"v_exp_f32 v199, v199\n"
"v_mfma_f32_16x16x32_bf16 v[204:207], a[152:155], a[8:11], v[204:207]\n"
"s_add_u32 s58, s58, s69\n"
"ds_read_b64_tr_b16 a[218:219], v247 offset:62976\n"
"v_add_f32 v244, v244, v199\n"
"v_cvt_pk_bf16_f32 v211, v198, v199\n"
"v_mfma_f32_16x16x32_bf16 v[204:207], a[156:159], a[12:15], v[204:207]\n"
"s_add_u32 s57, s57, 4096\n"
"v_exp_f32 v200, v200\n"
"v_mfma_f32_16x16x32_bf16 v[128:131], a[160:163], v[208:211], v[128:131]\n"
"ds_read_b64_tr_b16 a[220:221], v248 offset:58880\n"
"v_add_f32 v240, v240, v200\n"
"v_mfma_f32_16x16x32_bf16 v[132:135], a[164:167], v[208:211], v[132:135]\n"
"v_exp_f32 v201, v201\n"
"v_mfma_f32_16x16x32_bf16 v[136:139], a[168:171], v[208:211], v[136:139]\n"
"ds_read2_b64 a[236:239], v164 offset0:136 offset1:138\n"
"v_add_f32 v240, v240, v201\n"
"v_cvt_pk_bf16_f32 v212, v200, v201\n"
"v_mfma_f32_16x16x32_bf16 v[140:143], a[172:175], v[208:211], v[140:143]\n"
"s_add_u32 s68, s68, 4096\n"
"v_exp_f32 v202, v202\n"
"v_mfma_f32_16x16x32_bf16 v[144:147], a[176:179], v[208:211], v[144:147]\n"
"ds_read_b64_tr_b16 a[222:223], v248 offset:62976\n"
"v_add_f32 v240, v240, v202\n"
"v_mfma_f32_16x16x32_bf16 v[148:151], a[180:183], v[208:211], v[148:151]\n"
"v_exp_f32 v203, v203\n"
"v_mfma_f32_16x16x32_bf16 v[152:155], a[184:187], v[208:211], v[152:155]\n"
"s_cmp_lt_u32 s54, 10\n"
"v_add_f32 v240, v240, v203\n"
"v_cvt_pk_bf16_f32 v213, v202, v203\n"
"v_mfma_f32_16x16x32_bf16 v[156:159], a[188:191], v[208:211], v[156:159]\n"
"s_cbranch_scc1 LMB1%=\n"
"v_exp_f32 v204, v204\n"
"v_mfma_f32_16x16x32_bf16 v[192:195], a[128:131], a[16:19], v[220:223]\n"
"ds_read_b128 a[96:99], v246 offset:0\n"
"v_add_f32 v240, v240, v204\n"
"v_mfma_f32_16x16x32_bf16 v[192:195], a[132:135], a[20:23], v[192:195]\n"
"v_exp_f32 v205, v205\n"
"v_mfma_f32_16x16x32_bf16 v[192:195], a[136:139], a[24:27], v[192:195]\n"
"v_add_f32 v240, v240, v205\n"
"v_cvt_pk_bf16_f32 v214, v204, v205\n"
"v_mfma_f32_16x16x32_bf16 v[192:195], a[140:143], a[28:31], v[192:195]\n"
"v_exp_f32 v206, v206\n"
"v_mfma_f32_16x16x32_bf16 v[196:199], a[144:147], a[16:19], v[220:223]\n"
"ds_read_b128 a[100:103], v246 offset:512\n"
"v_add_f32 v240, v240, v206\n"
"v_mfma_f32_16x16x32_bf16 v[196:199], a[148:151], a[20:23], v[196:199]\n"
"s_waitcnt lgkmcnt(2)\n"
"v_exp_f32 v207, v207\n"
"v_mfma_f32_16x16x32_bf16 v[196:199], a[152:155], a[24:27], v[196:199]\n"
"s_waitcnt vmcnt(28)\n"
"v_add_f32 v240, v240, v207\n"
"v_cvt_pk_bf16_f32 v215, v206, v207\n"
"v_mfma_f32_16x16x32_bf16 v[196:199], a[156:159], a[28:31], v[196:199]\n"
"s_barrier\n"
"v_exp_f32 v192, v192\n"
"v_mfma_f32_16x16x32_bf16 v[0:3], a[192:195], v[212:215], v[0:3]\n"
"ds_read_b128 a[104:107], v246 offset:1024\n"
"v_add_f32 v241, v241, v192\n"
"v_mfma_f32_16x16x32_bf16 v[4:7], a[196:199], v[212:215], v[4:7]\n"
"v_exp_f32 v193, v193\n"
"v_mfma_f32_16x16x32_bf16 v[8:11], a[200:203], v[212:215], v[8:11]\n"
"ds_read_b128 a[108:111], v246 offset:1536\n"
"v_add_f32 v241, v241, v193\n"
"v_cvt_pk_bf16_f32 v208, v192, v193\n"
"v_mfma_f32_16x16x32_bf16 v[12:15], a[204:207], v[212:215], v[12:15]\n"
"v_exp_f32 v194, v194\n"
"v_mfma_f32_16x16x32_bf16 v[16:19], a[208:211], v[212:215], v[16:19]\n"
"ds_read_b128 a[112:115], v246 offset:4096\n"
"v_add_f32 v241, v241, v194\n"
"v_mfma_f32_16x16x32_bf16 v[20:23], a[212:215], v[212:215], v[20:23]\n"
"v_exp_f32 v195, v195\n"
"v_mfma_f32_16x16x32_bf16 v[24:27], a[216:219], v[212:215], v[24:27]\n"
"v_add_f32 v241, v241, v195\n"
"v_cvt_pk_bf16_f32 v209, v194, v195\n"
"v_mfma_f32_16x16x32_bf16 v[28:31], a[220:223], v[212:215], v[28:31]\n"
"v_exp_f32 v196, v196\n"
"v_mfma_f32_16x16x32_bf16 v[200:203], a[128:131], a[32:35], v[224:227]\n"
"ds_read_b128 a[116:119], v246 offset:4608\n"
"v_add_f32 v241, v241, v196\n"
"v_mfma_f32_16x16x32_bf16 v[200:203], a[132:135], a[36:39], v[200:203]\n"
"v_exp_f32 v197, v197\n"
"v_mfma_f32_16x16x32_bf16 v[200:203], a[136:139], a[40:43], v[200:203]\n"
"v_add_f32 v241, v241, v197\n"
"v_cvt_pk_bf16_f32 v210, v196, v197\n"
"v_mfma_f32_16x16x32_bf16 v[200:203], a[140:143], a[44:47], v[200:203]\n"
"v_exp_f32 v198, v198\n"
"v_mfma_f32_16x16x32_bf16 v[204:207], a[144:147], a[32:35], v[224:227]\n"
"ds_read_b128 a[120:123], v246 offset:5120\n"
"v_add_f32 v241, v241, v198\n"
"v_mfma_f32_16x16x32_bf16 v[204:207], a[148:151], a[36:39], v[204:207]\n"
"v_exp_f32 v199, v199\n"
"v_mfma_f32_16x16x32_bf16 v[204:207], a[152:155], a[40:43], v[204:207]\n"
"v_add_f32 v241, v241, v199\n"
"v_cvt_pk_bf16_f32 v211, v198, v199\n"
"v_mfma_f32_16x16x32_bf16 v[204:207], a[156:159], a[44:47], v[204:207]\n"
"v_exp_f32 v200, v200\n"
"v_mfma_f32_16x16x32_bf16 v[32:35], a[192:195], v[208:211], v[32:35]\n"
"ds_read_b128 a[124:127], v246 offset:5632\n"
"v_add_f32 v242, v242, v200\n"
"v_mfma_f32_16x16x32_bf16 v[36:39], a[196:199], v[208:211], v[36:39]\n"
"v_exp_f32 v201, v201\n"
"v_mfma_f32_16x16x32_bf16 v[40:43], a[200:203], v[208:211], v[40:43]\n"
"v_add_f32 v242, v242, v201\n"
"v_cvt_pk_bf16_f32 v212, v200, v201\n"
"v_mfma_f32_16x16x32_bf16 v[44:47], a[204:207], v[208:211], v[44:47]\n"
"v_exp_f32 v202, v202\n"
"v_mfma_f32_16x16x32_bf16 v[48:51], a[208:211], v[208:211], v[48:51]\n"
"ds_read_b64_tr_b16 a[160:161], v247 offset:32768\n"
"v_add_f32 v242, v242, v202\n"
"v_mfma_f32_16x16x32_bf16 v[52:55], a[212:215], v[208:211], v[52:55]\n"
"v_exp_f32 v203, v203\n"
"v_mfma_f32_16x16x32_bf16 v[56:59], a[216:219], v[208:211], v[56:59]\n"
"ds_read_b64_tr_b16 a[162:163], v247 offset:36864\n"
"v_add_f32 v242, v242, v203\n"
"v_cvt_pk_bf16_f32 v213, v202, v203\n"
"v_mfma_f32_16x16x32_bf16 v[60:63], a[220:223], v[208:211], v[60:63]\n"
"v_exp_f32 v204, v204\n"
"v_mfma_f32_16x16x32_bf16 v[192:195], a[128:131], a[48:51], v[228:231]\n"
"ds_read_b64_tr_b16 a[164:165], v248 offset:32768\n"
"v_add_f32 v242, v242, v204\n"
"v_mfma_f32_16x16x32_bf16 v[192:195], a[132:135], a[52:55], v[192:195]\n"
"v_exp_f32 v205, v205\n"
"v_mfma_f32_16x16x32_bf16 v[192:195], a[136:139], a[56:59], v[192:195]\n"
"v_add_f32 v242, v242, v205\n"
"v_cvt_pk_bf16_f32 v214, v204, v205\n"
"v_mfma_f32_16x16x32_bf16 v[192:195], a[140:143], a[60:63], v[192:195]\n"
"v_exp_f32 v206, v206\n"
"v_mfma_f32_16x16x32_bf16 v[196:199], a[144:147], a[48:51], v[228:231]\n"
"ds_read_b64_tr_b16 a[166:167], v248 offset:36864\n"
"v_add_f32 v242, v242, v206\n"
"v_mfma_f32_16x16x32_bf16 v[196:199], a[148:151], a[52:55], v[196:199]\n"
"v_exp_f32 v207, v207\n"
"v_mfma_f32_16x16x32_bf16 v[196:199], a[152:155], a[56:59], v[196:199]\n"
"v_add_f32 v242, v242, v207\n"
"v_cvt_pk_bf16_f32 v215, v206, v207\n"
"v_mfma_f32_16x16x32_bf16 v[196:199], a[156:159], a[60:63], v[196:199]\n"
"v_exp_f32 v192, v192\n"
"v_mfma_f32_16x16x32_bf16 v[64:67], a[192:195], v[212:215], v[64:67]\n"
"ds_read_b64_tr_b16 a[168:169], v247 offset:33280\n"
"v_add_f32 v243, v243, v192\n"
"v_mfma_f32_16x16x32_bf16 v[68:71], a[196:199], v[212:215], v[68:71]\n"
"v_exp_f32 v193, v193\n"
"v_mfma_f32_16x16x32_bf16 v[72:75], a[200:203], v[212:215], v[72:75]\n"
"v_add_f32 v243, v243, v193\n"
"v_cvt_pk_bf16_f32 v208, v192, v193\n"
"v_mfma_f32_16x16x32_bf16 v[76:79], a[204:207], v[212:215], v[76:79]\n"
"v_exp_f32 v194, v194\n"
"v_mfma_f32_16x16x32_bf16 v[80:83], a[208:211], v[212:215], v[80:83]\n"
"ds_read_b64_tr_b16 a[170:171], v247 offset:37376\n"
"v_add_f32 v243, v243, v194\n"
"v_mfma_f32_16x16x32_bf16 v[84:87], a[212:215], v[212:215], v[84:87]\n"
"v_exp_f32 v195, v195\n"
"v_mfma_f32_16x16x32_bf16 v[88:91], a[216:219], v[212:215], v[88:91]\n"
"v_add_f32 v243, v243, v195\n"
"v_cvt_pk_bf16_f32 v209, v194, v195\n"
"v_mfma_f32_16x16x32_bf16 v[92:95], a[220:223], v[212:215], v[92:95]\n"
"v_exp_f32 v196, v196\n"
"v_mfma_f32_16x16x32_bf16 v[200:203], a[128:131], a[64:67], v[232:235]\n"
"ds_read_b64_tr_b16 a[172:173], v248 offset:33280\n"
"v_add_f32 v243, v243, v196\n"
"v_mfma_f32_16x16x32_bf16 v[200:203], a[132:135], a[68:71], v[200:203]\n"
"v_exp_f32 v197, v197\n"
"v_mfma_f32_16x16x32_bf16 v[200:203], a[136:139], a[72:75], v[200:203]\n"
"ds_read_b128 a[128:131], v246 offset:8192\n"
"ds_read_b64_tr_b16 a[174:175], v248 offset:37376\n"
"v_add_f32 v243, v243, v197\n"
"v_cvt_pk_bf16_f32 v210, v196, v197\n"
"v_mfma_f32_16x16x32_bf16 v[200:203], a[140:143], a[76:79], v[200:203]\n"
"v_exp_f32 v198, v198\n"
"v_mfma_f32_16x16x32_bf16 v[204:207], a[144:147], a[64:67], v[232:235]\n"
"ds_read_b64_tr_b16 a[176:177], v247 offset:33792\n"
"v_add_f32 v243, v243, v198\n"
"v_mfma_f32_16x16x32_bf16 v[204:207], a[148:151], a[68:71], v[204:207]\n"
"v_exp_f32 v199, v199\n"
"v_mfma_f32_16x16x32_bf16 v[204:207], a[152:155], a[72:75], v[204:207]\n"
"ds_read_b128 a[132:135], v246 offset:8704\n"
"v_add_f32 v243, v243, v199\n"
"v_cvt_pk_bf16_f32 v211, v198, v199\n"
"v_mfma_f32_16x16x32_bf16 v[204:207], a[156:159], a[76:79], v[204:207]\n"
"v_exp_f32 v200, v200\n"
"v_mfma_f32_16x16x32_bf16 v[96:99], a[192:195], v[208:211], v[96:99]\n"
"ds_read_b64_tr_b16 a[178:179], v247 offset:37888\n"
"v_add_f32 v244, v244, v200\n"
"v_mfma_f32_16x16x32_bf16 v[100:103], a[196:199], v[208:211], v[100:103]\n"
"v_exp_f32 v201, v201\n"
"v_mfma_f32_16x16x32_bf16 v[104:107], a[200:203], v[208:211], v[104:107]\n"
"ds_read_b128 a[136:139], v246 offset:9216\n"
"v_add_f32 v244, v244, v201\n"
"v_cvt_pk_bf16_f32 v212, v200, v201\n"
"v_mfma_f32_16x16x32_bf16 v[108:111], a[204:207], v[208:211], v[108:111]\n"
"v_exp_f32 v202, v202\n"
"v_mfma_f32_16x16x32_bf16 v[112:115], a[208:211], v[208:211], v[112:115]\n"
"ds_read_b64_tr_b16 a[180:181], v248 offset:33792\n"
"v_add_f32 v244, v244, v202\n"
"v_mfma_f32_16x16x32_bf16 v[116:119], a[212:215], v[208:211], v[116:119]\n"
"v_exp_f32 v203, v203\n"
"v_mfma_f32_16x16x32_bf16 v[120:123], a[216:219], v[208:211], v[120:123]\n"
"ds_read_b128 a[140:143], v246 offset:9728\n"
"v_add_f32 v244, v244, v203\n"
"v_cvt_pk_bf16_f32 v213, v202, v203\n"
"v_mfma_f32_16x16x32_bf16 v[124:127], a[220:223], v[208:211], v[124:127]\n"
"s_waitcnt lgkmcnt(5)\n"
"v_exp_f32 v204, v204\n"
"v_mfma_f32_16x16x32_bf16 v[192:195], a[96:99], a[0:3], v[216:219]\n"
"ds_read_b64_tr_b16 a[182:183], v248 offset:37888\n"
"v_add_f32 v244, v244, v204\n"
"v_mfma_f32_16x16x32_bf16 v[192:195], a[100:103], a[4:7], v[192:195]\n"
"v_exp_f32 v205, v205\n"
"v_mfma_f32_16x16x32_bf16 v[192:195], a[104:107], a[8:11], v[192:195]\n"
"ds_read_b128 a[144:147], v246 offset:12288\n"
"v_add_f32 v244, v244, v205\n"
"v_cvt_pk_bf16_f32 v214, v204, v205\n"
"v_mfma_f32_16x16x32_bf16 v[192:195], a[108:111], a[12:15], v[192:195]\n"
"v_exp_f32 v206, v206\n"
"v_mfma_f32_16x16x32_bf16 v[196:199], a[112:115], a[0:3], v[216:219]\n"
"ds_read_b64_tr_b16 a[184:185], v247 offset:34304\n"
"v_add_f32 v244, v244, v206\n"
"v_mfma_f32_16x16x32_bf16 v[196:199], a[116:119], a[4:7], v[196:199]\n"
"v_exp_f32 v207, v207\n"
"v_mfma_f32_16x16x32_bf16 v[196:199], a[120:123], a[8:11], v[196:199]\n"
"ds_read_b128 a[148:151], v246 offset:12800\n"
"ds_read_b64_tr_b16 a[186:187], v247 offset:38400\n"
"v_add_f32 v244, v244, v207\n"
"v_cvt_pk_bf16_f32 v215, v206, v207\n"
"v_mfma_f32_16x16x32_bf16 v[196:199], a[124:127], a[12:15], v[196:199]\n"
"v_exp_f32 v192, v192\n"
"v_mfma_f32_16x16x32_bf16 v[128:131], a[192:195], v[212:215], v[128:131]\n"
"ds_read_b64_tr_b16 a[188:189], v248 offset:34304\n"
"v_add_f32 v240, v240, v192\n"
"v_mfma_f32_16x16x32_bf16 v[132:135], a[196:199], v[212:215], v[132:135]\n"
"v_exp_f32 v193, v193\n"
"v_mfma_f32_16x16x32_bf16 v[136:139], a[200:203], v[212:215], v[136:139]\n"
"ds_read_b128 a[152:155], v246 offset:13312\n"
"v_add_f32 v240, v240, v193\n"
"v_cvt_pk_bf16_f32 v208, v192, v193\n"
"v_mfma_f32_16x16x32_bf16 v[140:143], a[204:207], v[212:215], v[140:143]\n"
"v_exp_f32 v194, v194\n"
"v_mfma_f32_16x16x32_bf16 v[144:147], a[208:211], v[212:215], v[144:147]\n"
"ds_read_b64_tr_b16 a[190:191], v248 offset:38400\n"
"v_add_f32 v240, v240, v194\n"
"v_mfma_f32_16x16x32_bf16 v[148:151], a[212:215], v[212:215], v[148:151]\n"
"s_waitcnt vmcnt(18)\n"
"v_exp_f32 v195, v195\n"
"v_mfma_f32_16x16x32_bf16 v[152:155], a[216:219], v[212:215], v[152:155]\n"
"ds_read_b128 a[156:159], v246 offset:13824\n"
"v_add_f32 v240, v240, v195\n"
"v_cvt_pk_bf16_f32 v209, v194, v195\n"
"v_mfma_f32_16x16x32_bf16 v[156:159], a[220:223], v[212:215], v[156:159]\n"
"s_barrier\n"
"v_exp_f32 v196, v196\n"
"v_mfma_f32_16x16x32_bf16 v[200:203], a[128:131], a[0:3], v[216:219]\n"
"s_waitcnt lgkmcnt(4)\n"
"ds_read_b64_tr_b16 a[192:193], v247 offset:40960\n"
"v_add_f32 v240, v240, v196\n"
"v_mfma_f32_16x16x32_bf16 v[200:203], a[132:135], a[4:7], v[200:203]\n"
"s_add_u32 s68, s61, 188416\n"
"ds_read_b64_tr_b16 a[194:195], v247 offset:45056\n"
"v_exp_f32 v197, v197\n"
"v_mfma_f32_16x16x32_bf16 v[200:203], a[136:139], a[8:11], v[200:203]\n"
"s_mov_b32 s50, 376832\n"
"ds_read_b64_tr_b16 a[196:197], v248 offset:40960\n"
"v_add_f32 v240, v240, v197\n"
"v_cvt_pk_bf16_f32 v210, v196, v197\n"
"v_mfma_f32_16x16x32_bf16 v[200:203], a[140:143], a[12:15], v[200:203]\n"
"ds_read_b64_tr_b16 a[198:199], v248 offset:45056\n"
"v_exp_f32 v198, v198\n"
"v_mfma_f32_16x16x32_bf16 v[204:207], a[144:147], a[0:3], v[216:219]\n"
"ds_read_b64_tr_b16 a[200:201], v247 offset:41472\n"
"v_add_f32 v240, v240, v198\n"
"v_mfma_f32_16x16x32_bf16 v[204:207], a[148:151], a[4:7], v[204:207]\n"
"s_waitcnt lgkmcnt(5)\n"
"ds_read_b64_tr_b16 a[202:203], v247 offset:45568\n"
"v_exp_f32 v199, v199\n"
"v_mfma_f32_16x16x32_bf16 v[204:207], a[152:155], a[8:11], v[204:207]\n"
"ds_read_b64_tr_b16 a[204:205], v248 offset:41472\n"
"v_add_f32 v240, v240, v199\n"
"v_cvt_pk_bf16_f32 v211, v198, v199\n"
"v_mfma_f32_16x16x32_bf16 v[204:207], a[156:159], a[12:15], v[204:207]\n"
"v_exp_f32 v200, v200\n"
"v_mfma_f32_16x16x32_bf16 v[0:3], a[160:163], v[208:211], v[0:3]\n"
"ds_read_b64_tr_b16 a[206:207], v248 offset:45568\n"
"v_add_f32 v240, v240, v200\n"
"v_mfma_f32_16x16x32_bf16 v[4:7], a[164:167], v[208:211], v[4:7]\n"
"v_exp_f32 v201, v201\n"
"v_mfma_f32_16x16x32_bf16 v[8:11], a[168:171], v[208:211], v[8:11]\n"
"ds_read_b64_tr_b16 a[208:209], v247 offset:41984\n"
"v_add_f32 v240, v240, v201\n"
"v_cvt_pk_bf16_f32 v212, v200, v201\n"
"v_mfma_f32_16x16x32_bf16 v[12:15], a[172:175], v[208:211], v[12:15]\n"
"v_exp_f32 v202, v202\n"
"v_mfma_f32_16x16x32_bf16 v[16:19], a[176:179], v[208:211], v[16:19]\n"
"ds_read_b64_tr_b16 a[210:211], v247 offset:46080\n"
"v_add_f32 v240, v240, v202\n"
"v_mfma_f32_16x16x32_bf16 v[20:23], a[180:183], v[208:211], v[20:23]\n"
"v_exp_f32 v203, v203\n"
"v_mfma_f32_16x16x32_bf16 v[24:27], a[184:187], v[208:211], v[24:27]\n"
"ds_read_b64_tr_b16 a[212:213], v248 offset:41984\n"
"v_add_f32 v240, v240, v203\n"
"v_cvt_pk_bf16_f32 v213, v202, v203\n"
"v_mfma_f32_16x16x32_bf16 v[28:31], a[188:191], v[208:211], v[28:31]\n"
"v_exp_f32 v204, v204\n"
"v_mfma_f32_16x16x32_bf16 v[192:195], a[96:99], a[16:19], v[220:223]\n"
"ds_read_b64_tr_b16 a[214:215], v248 offset:46080\n"
"v_add_f32 v240, v240, v204\n"
"v_mfma_f32_16x16x32_bf16 v[192:195], a[100:103], a[20:23], v[192:195]\n"
"v_exp_f32 v205, v205\n"
"v_mfma_f32_16x16x32_bf16 v[192:195], a[104:107], a[24:27], v[192:195]\n"
"ds_read_b64_tr_b16 a[216:217], v247 offset:42496\n"
"v_add_f32 v240, v240, v205\n"
"v_cvt_pk_bf16_f32 v214, v204, v205\n"
"v_mfma_f32_16x16x32_bf16 v[192:195], a[108:111], a[28:31], v[192:195]\n"
"v_exp_f32 v206, v206\n"
"v_mfma_f32_16x16x32_bf16 v[196:199], a[112:115], a[16:19], v[220:223]\n"
"ds_read_b64_tr_b16 a[218:219], v247 offset:46592\n"
"v_add_f32 v240, v240, v206\n"
"v_mfma_f32_16x16x32_bf16 v[196:199], a[116:119], a[20:23], v[196:199]\n"
"v_exp_f32 v207, v207\n"
"v_mfma_f32_16x16x32_bf16 v[196:199], a[120:123], a[24:27], v[196:199]\n"
"ds_read_b64_tr_b16 a[220:221], v248 offset:42496\n"
"v_add_f32 v240, v240, v207\n"
"v_cvt_pk_bf16_f32 v215, v206, v207\n"
"v_mfma_f32_16x16x32_bf16 v[196:199], a[124:127], a[28:31], v[196:199]\n"
"s_waitcnt lgkmcnt(5)\n"
"v_exp_f32 v192, v192\n"
"v_mfma_f32_16x16x32_bf16 v[0:3], a[192:195], v[212:215], v[0:3]\n"
"ds_read_b64_tr_b16 a[222:223], v248 offset:46592\n"
"v_add_f32 v241, v241, v192\n"
"v_mfma_f32_16x16x32_bf16 v[4:7], a[196:199], v[212:215], v[4:7]\n"
"v_exp_f32 v193, v193\n"
"v_mfma_f32_16x16x32_bf16 v[8:11], a[200:203], v[212:215], v[8:11]\n"
"v_add_f32 v241, v241, v193\n"
"v_cvt_pk_bf16_f32 v208, v192, v193\n"
"v_mfma_f32_16x16x32_bf16 v[12:15], a[204:207], v[212:215], v[12:15]\n"
"v_exp_f32 v194, v194\n"
"v_mfma_f32_16x16x32_bf16 v[16:19], a[208:211], v[212:215], v[16:19]\n"
"s_waitcnt lgkmcnt(3)\n"
"v_add_f32 v241, v241, v194\n"
"v_subrev_u32 v161, 17408, v161\n"
"v_mfma_f32_16x16x32_bf16 v[20:23], a[212:215], v[212:215], v[20:23]\n"
"s_waitcnt lgkmcnt(2)\n"
"v_exp_f32 v195, v195\n"
"v_mfma_f32_16x16x32_bf16 v[24:27], a[216:219], v[212:215], v[24:27]\n"
"s_waitcnt lgkmcnt(0)\n"
"v_add_f32 v241, v241, v195\n"
"v_cvt_pk_bf16_f32 v209, v194, v195\n"
"v_mfma_f32_16x16x32_bf16 v[28:31], a[220:223], v[212:215], v[28:31]\n"
"v_exp_f32 v196, v196\n"
"v_add_u32 v164, 2176, v161\n"
"v_mfma_f32_16x16x4_f32 v[216:219], v250, v240, 0\n"
"s_nop 9\n"
"v_rcp_f32 v218, v216\n"
"v_med3_f32 v217, v216, v251, s73\n"
"v_cmp_neq_f32 vcc, v217, v216\n"
"v_mul_f32 v219, s99, v218\n"
"v_pk_fma_f32 v[0:1], v[0:1], v[218:219], v[218:219] op_sel:[0,0,1] op_sel_hi:[1,0,1] neg_lo:[0,0,1] neg_hi:[0,0,1]\n"
"v_pk_fma_f32 v[2:3], v[2:3], v[218:219], v[218:219] op_sel:[0,0,1] op_sel_hi:[1,0,1] neg_lo:[0,0,1] neg_hi:[0,0,1]\n"
"v_pk_fma_f32 v[4:5], v[4:5], v[218:219], v[218:219] op_sel:[0,0,1] op_sel_hi:[1,0,1] neg_lo:[0,0,1] neg_hi:[0,0,1]\n"
"v_pk_fma_f32 v[6:7], v[6:7], v[218:219], v[218:219] op_sel:[0,0,1] op_sel_hi:[1,0,1] neg_lo:[0,0,1] neg_hi:[0,0,1]\n"
"v_pk_fma_f32 v[8:9], v[8:9], v[218:219], v[218:219] op_sel:[0,0,1] op_sel_hi:[1,0,1] neg_lo:[0,0,1] neg_hi:[0,0,1]\n"
"v_pk_fma_f32 v[10:11], v[10:11], v[218:219], v[218:219] op_sel:[0,0,1] op_sel_hi:[1,0,1] neg_lo:[0,0,1] neg_hi:[0,0,1]\n"
"v_pk_fma_f32 v[12:13], v[12:13], v[218:219], v[218:219] op_sel:[0,0,1] op_sel_hi:[1,0,1] neg_lo:[0,0,1] neg_hi:[0,0,1]\n"
"v_pk_fma_f32 v[14:15], v[14:15], v[218:219], v[218:219] op_sel:[0,0,1] op_sel_hi:[1,0,1] neg_lo:[0,0,1] neg_hi:[0,0,1]\n"
"v_pk_fma_f32 v[16:17], v[16:17], v[218:219], v[218:219] op_sel:[0,0,1] op_sel_hi:[1,0,1] neg_lo:[0,0,1] neg_hi:[0,0,1]\n"
"v_pk_fma_f32 v[18:19], v[18:19], v[218:219], v[218:219] op_sel:[0,0,1] op_sel_hi:[1,0,1] neg_lo:[0,0,1] neg_hi:[0,0,1]\n"
"v_pk_fma_f32 v[20:21], v[20:21], v[218:219], v[218:219] op_sel:[0,0,1] op_sel_hi:[1,0,1] neg_lo:[0,0,1] neg_hi:[0,0,1]\n"
"v_pk_fma_f32 v[22:23], v[22:23], v[218:219], v[218:219] op_sel:[0,0,1] op_sel_hi:[1,0,1] neg_lo:[0,0,1] neg_hi:[0,0,1]\n"
"v_pk_fma_f32 v[24:25], v[24:25], v[218:219], v[218:219] op_sel:[0,0,1] op_sel_hi:[1,0,1] neg_lo:[0,0,1] neg_hi:[0,0,1]\n"
"v_pk_fma_f32 v[26:27], v[26:27], v[218:219], v[218:219] op_sel:[0,0,1] op_sel_hi:[1,0,1] neg_lo:[0,0,1] neg_hi:[0,0,1]\n"
"v_pk_fma_f32 v[28:29], v[28:29], v[218:219], v[218:219] op_sel:[0,0,1] op_sel_hi:[1,0,1] neg_lo:[0,0,1] neg_hi:[0,0,1]\n"
"v_pk_fma_f32 v[30:31], v[30:31], v[218:219], v[218:219] op_sel:[0,0,1] op_sel_hi:[1,0,1] neg_lo:[0,0,1] neg_hi:[0,0,1]\n"
"s_cmp_lg_u64 vcc, 0\n"
"s_addc_u32 s55, s55, s55\n"
"v_cvt_pk_bf16_f32 v0, v0, v1\n"
"v_cvt_pk_bf16_f32 v1, v2, v3\n"
"v_cvt_pk_bf16_f32 v2, v4, v5\n"
"v_cvt_pk_bf16_f32 v3, v6, v7\n"
"v_cvt_pk_bf16_f32 v4, v8, v9\n"
"v_cvt_pk_bf16_f32 v5, v10, v11\n"
"v_cvt_pk_bf16_f32 v6, v12, v13\n"
"v_cvt_pk_bf16_f32 v7, v14, v15\n"
"v_cvt_pk_bf16_f32 v8, v16, v17\n"
"v_cvt_pk_bf16_f32 v9, v18, v19\n"
"v_cvt_pk_bf16_f32 v10, v20, v21\n"
"v_cvt_pk_bf16_f32 v11, v22, v23\n"
"v_cvt_pk_bf16_f32 v12, v24, v25\n"
"v_cvt_pk_bf16_f32 v13, v26, v27\n"
"v_cvt_pk_bf16_f32 v14, v28, v29\n"
"v_cvt_pk_bf16_f32 v15, v30, v31\n"
"v_mfma_f32_16x16x32_bf16 v[200:203], a[128:131], a[16:19], v[220:223]\n"
"ds_write_b128 v160, v[0:3] offset:0\n"
"v_add_f32 v241, v241, v196\n"
"v_mfma_f32_16x16x32_bf16 v[200:203], a[132:135], a[20:23], v[200:203]\n"
"v_exp_f32 v197, v197\n"
"v_mfma_f32_16x16x32_bf16 v[200:203], a[136:139], a[24:27], v[200:203]\n"
"ds_write_b128 v160, v[4:7] offset:64\n"
"v_add_f32 v241, v241, v197\n"
"v_cvt_pk_bf16_f32 v210, v196, v197\n"
"v_mfma_f32_16x16x32_bf16 v[200:203], a[140:143], a[28:31], v[200:203]\n"
"v_exp_f32 v198, v198\n"
"v_mfma_f32_16x16x32_bf16 v[204:207], a[144:147], a[16:19], v[220:223]\n"
"ds_write_b128 v160, v[8:11] offset:128\n"
"v_add_f32 v241, v241, v198\n"
"v_mfma_f32_16x16x32_bf16 v[204:207], a[148:151], a[20:23], v[204:207]\n"
"v_exp_f32 v199, v199\n"
"v_mfma_f32_16x16x32_bf16 v[204:207], a[152:155], a[24:27], v[204:207]\n"
"ds_write_b128 v160, v[12:15] offset:192\n"
"v_add_f32 v241, v241, v199\n"
"v_cvt_pk_bf16_f32 v211, v198, v199\n"
"v_mfma_f32_16x16x32_bf16 v[204:207], a[156:159], a[28:31], v[204:207]\n"
"v_exp_f32 v200, v200\n"
"v_mfma_f32_16x16x32_bf16 v[32:35], a[160:163], v[208:211], v[32:35]\n"
"ds_read2_b64 v[0:3], v161 offset0:0 offset1:2\n"
"v_add_f32 v241, v241, v200\n"
"v_mfma_f32_16x16x32_bf16 v[36:39], a[164:167], v[208:211], v[36:39]\n"
"ds_read2_b64 v[4:7], v161 offset0:136 offset1:138\n"
"v_exp_f32 v201, v201\n"
"v_mfma_f32_16x16x32_bf16 v[40:43], a[168:171], v[208:211], v[40:43]\n"
"ds_read2_b64 v[8:11], v164 offset0:0 offset1:2\n"
"v_add_f32 v241, v241, v201\n"
"v_cvt_pk_bf16_f32 v212, v200, v201\n"
"v_mfma_f32_16x16x32_bf16 v[44:47], a[172:175], v[208:211], v[44:47]\n"
"ds_read2_b64 v[12:15], v164 offset0:136 offset1:138\n"
"v_exp_f32 v202, v202\n"
"v_mfma_f32_16x16x32_bf16 v[48:51], a[176:179], v[208:211], v[48:51]\n"
"v_add_f32 v241, v241, v202\n"
"v_mfma_f32_16x16x32_bf16 v[52:55], a[180:183], v[208:211], v[52:55]\n"
"v_exp_f32 v203, v203\n"
"v_mfma_f32_16x16x32_bf16 v[56:59], a[184:187], v[208:211], v[56:59]\n"
"v_add_f32 v241, v241, v203\n"
"v_cvt_pk_bf16_f32 v213, v202, v203\n"
"v_mfma_f32_16x16x32_bf16 v[60:63], a[188:191], v[208:211], v[60:63]\n"
"v_exp_f32 v204, v204\n"
"v_mfma_f32_16x16x32_bf16 v[192:195], a[96:99], a[32:35], v[224:227]\n"
"v_add_f32 v241, v241, v204\n"
"v_mfma_f32_16x16x32_bf16 v[192:195], a[100:103], a[36:39], v[192:195]\n"
"v_exp_f32 v205, v205\n"
"v_mfma_f32_16x16x32_bf16 v[192:195], a[104:107], a[40:43], v[192:195]\n"
"v_add_f32 v241, v241, v205\n"
"v_cvt_pk_bf16_f32 v214, v204, v205\n"
"v_mfma_f32_16x16x32_bf16 v[192:195], a[108:111], a[44:47], v[192:195]\n"
"s_waitcnt lgkmcnt(0)\n"
"v_exp_f32 v206, v206\n"
"v_mfma_f32_16x16x32_bf16 v[196:199], a[112:115], a[32:35], v[224:227]\n"
"buffer_store_dwordx4 v[0:3], v255, s[48:51], s68 offen sc1\n"
"v_add_f32 v241, v241, v206\n"
"v_mfma_f32_16x16x32_bf16 v[196:199], a[116:119], a[36:39], v[196:199]\n"
"v_exp_f32 v207, v207\n"
"v_mfma_f32_16x16x32_bf16 v[196:199], a[120:123], a[40:43], v[196:199]\n"
"v_add_f32 v241, v241, v207\n"
"v_cvt_pk_bf16_f32 v215, v206, v207\n"
"v_mfma_f32_16x16x32_bf16 v[196:199], a[124:127], a[44:47], v[196:199]\n"
"v_exp_f32 v192, v192\n"
"v_mfma_f32_16x16x32_bf16 v[32:35], a[192:195], v[212:215], v[32:35]\n"
"buffer_store_dwordx4 v[4:7], v255, s[48:51], s68 offen offset:1024 sc1\n"
"v_add_f32 v242, v242, v192\n"
"v_mfma_f32_16x16x32_bf16 v[36:39], a[196:199], v[212:215], v[36:39]\n"
"v_exp_f32 v193, v193\n"
"v_mfma_f32_16x16x32_bf16 v[40:43], a[200:203], v[212:215], v[40:43]\n"
"v_add_f32 v242, v242, v193\n"
"v_cvt_pk_bf16_f32 v208, v192, v193\n"
"v_mfma_f32_16x16x32_bf16 v[44:47], a[204:207], v[212:215], v[44:47]\n"
"v_exp_f32 v194, v194\n"
"v_mfma_f32_16x16x32_bf16 v[48:51], a[208:211], v[212:215], v[48:51]\n"
"buffer_store_dwordx4 v[8:11], v255, s[48:51], s68 offen offset:2048 sc1\n"
"v_add_f32 v242, v242, v194\n"
"v_mfma_f32_16x16x32_bf16 v[52:55], a[212:215], v[212:215], v[52:55]\n"
"v_exp_f32 v195, v195\n"
"v_mfma_f32_16x16x32_bf16 v[56:59], a[216:219], v[212:215], v[56:59]\n"
"v_add_f32 v242, v242, v195\n"
"v_cvt_pk_bf16_f32 v209, v194, v195\n"
"v_mfma_f32_16x16x32_bf16 v[60:63], a[220:223], v[212:215], v[60:63]\n"
"v_exp_f32 v196, v196\n"
"v_mfma_f32_16x16x4_f32 v[220:223], v250, v241, 0\n"
"s_nop 9\n"
"v_rcp_f32 v222, v220\n"
"v_med3_f32 v221, v220, v251, s73\n"
"v_cmp_neq_f32 vcc, v221, v220\n"
"v_mul_f32 v223, s99, v222\n"
"v_pk_fma_f32 v[32:33], v[32:33], v[222:223], v[222:223] op_sel:[0,0,1] op_sel_hi:[1,0,1] neg_lo:[0,0,1] neg_hi:[0,0,1]\n"
"v_pk_fma_f32 v[34:35], v[34:35], v[222:223], v[222:223] op_sel:[0,0,1] op_sel_hi:[1,0,1] neg_lo:[0,0,1] neg_hi:[0,0,1]\n"
"v_pk_fma_f32 v[36:37], v[36:37], v[222:223], v[222:223] op_sel:[0,0,1] op_sel_hi:[1,0,1] neg_lo:[0,0,1] neg_hi:[0,0,1]\n"
"v_pk_fma_f32 v[38:39], v[38:39], v[222:223], v[222:223] op_sel:[0,0,1] op_sel_hi:[1,0,1] neg_lo:[0,0,1] neg_hi:[0,0,1]\n"
"v_pk_fma_f32 v[40:41], v[40:41], v[222:223], v[222:223] op_sel:[0,0,1] op_sel_hi:[1,0,1] neg_lo:[0,0,1] neg_hi:[0,0,1]\n"
"v_pk_fma_f32 v[42:43], v[42:43], v[222:223], v[222:223] op_sel:[0,0,1] op_sel_hi:[1,0,1] neg_lo:[0,0,1] neg_hi:[0,0,1]\n"
"v_pk_fma_f32 v[44:45], v[44:45], v[222:223], v[222:223] op_sel:[0,0,1] op_sel_hi:[1,0,1] neg_lo:[0,0,1] neg_hi:[0,0,1]\n"
"v_pk_fma_f32 v[46:47], v[46:47], v[222:223], v[222:223] op_sel:[0,0,1] op_sel_hi:[1,0,1] neg_lo:[0,0,1] neg_hi:[0,0,1]\n"
"v_pk_fma_f32 v[48:49], v[48:49], v[222:223], v[222:223] op_sel:[0,0,1] op_sel_hi:[1,0,1] neg_lo:[0,0,1] neg_hi:[0,0,1]\n"
"v_pk_fma_f32 v[50:51], v[50:51], v[222:223], v[222:223] op_sel:[0,0,1] op_sel_hi:[1,0,1] neg_lo:[0,0,1] neg_hi:[0,0,1]\n"
"v_pk_fma_f32 v[52:53], v[52:53], v[222:223], v[222:223] op_sel:[0,0,1] op_sel_hi:[1,0,1] neg_lo:[0,0,1] neg_hi:[0,0,1]\n"
"v_pk_fma_f32 v[54:55], v[54:55], v[222:223], v[222:223] op_sel:[0,0,1] op_sel_hi:[1,0,1] neg_lo:[0,0,1] neg_hi:[0,0,1]\n"
"v_pk_fma_f32 v[56:57], v[56:57], v[222:223], v[222:223] op_sel:[0,0,1] op_sel_hi:[1,0,1] neg_lo:[0,0,1] neg_hi:[0,0,1]\n"
"v_pk_fma_f32 v[58:59], v[58:59], v[222:223], v[222:223] op_sel:[0,0,1] op_sel_hi:[1,0,1] neg_lo:[0,0,1] neg_hi:[0,0,1]\n"
"v_pk_fma_f32 v[60:61], v[60:61], v[222:223], v[222:223] op_sel:[0,0,1] op_sel_hi:[1,0,1] neg_lo:[0,0,1] neg_hi:[0,0,1]\n"
"v_pk_fma_f32 v[62:63], v[62:63], v[222:223], v[222:223] op_sel:[0,0,1] op_sel_hi:[1,0,1] neg_lo:[0,0,1] neg_hi:[0,0,1]\n"
"s_cmp_lg_u64 vcc, 0\n"
"s_addc_u32 s55, s55, s55\n"
"v_cvt_pk_bf16_f32 v32, v32, v33\n"
"v_cvt_pk_bf16_f32 v33, v34, v35\n"
"v_cvt_pk_bf16_f32 v34, v36, v37\n"
"v_cvt_pk_bf16_f32 v35, v38, v39\n"
"v_cvt_pk_bf16_f32 v36, v40, v41\n"
"v_cvt_pk_bf16_f32 v37, v42, v43\n"
"v_cvt_pk_bf16_f32 v38, v44, v45\n"
"v_cvt_pk_bf16_f32 v39, v46, v47\n"
"v_cvt_pk_bf16_f32 v40, v48, v49\n"
"v_cvt_pk_bf16_f32 v41, v50, v51\n"
"v_cvt_pk_bf16_f32 v42, v52, v53\n"
"v_cvt_pk_bf16_f32 v43, v54, v55\n"
"v_cvt_pk_bf16_f32 v44, v56, v57\n"
"v_cvt_pk_bf16_f32 v45, v58, v59\n"
"v_cvt_pk_bf16_f32 v46, v60, v61\n"
"v_cvt_pk_bf16_f32 v47, v62, v63\n"
"v_mfma_f32_16x16x32_bf16 v[200:203], a[128:131], a[32:35], v[224:227]\n"
"ds_write_b128 v160, v[32:35] offset:0\n"
"buffer_store_dwordx4 v[12:15], v255, s[48:51], s68 offen offset:3072 sc1\n"
"v_add_f32 v242, v242, v196\n"
"v_mfma_f32_16x16x32_bf16 v[200:203], a[132:135], a[36:39], v[200:203]\n"
"s_add_u32 s68, s68, 4096\n"
"v_exp_f32 v197, v197\n"
"v_mfma_f32_16x16x32_bf16 v[200:203], a[136:139], a[40:43], v[200:203]\n"
"ds_write_b128 v160, v[36:39] offset:64\n"
"v_add_f32 v242, v242, v197\n"
"v_cvt_pk_bf16_f32 v210, v196, v197\n"
"v_mfma_f32_16x16x32_bf16 v[200:203], a[140:143], a[44:47], v[200:203]\n"
"v_exp_f32 v198, v198\n"
"v_mfma_f32_16x16x32_bf16 v[204:207], a[144:147], a[32:35], v[224:227]\n"
"ds_write_b128 v160, v[40:43] offset:128\n"
"v_add_f32 v242, v242, v198\n"
"v_mfma_f32_16x16x32_bf16 v[204:207], a[148:151], a[36:39], v[204:207]\n"
"v_exp_f32 v199, v199\n"
"v_mfma_f32_16x16x32_bf16 v[204:207], a[152:155], a[40:43], v[204:207]\n"
"ds_write_b128 v160, v[44:47] offset:192\n"
"v_add_f32 v242, v242, v199\n"
"v_cvt_pk_bf16_f32 v211, v198, v199\n"
"v_mfma_f32_16x16x32_bf16 v[204:207], a[156:159], a[44:47], v[204:207]\n"
"v_exp_f32 v200, v200\n"
"v_mfma_f32_16x16x32_bf16 v[64:67], a[160:163], v[208:211], v[64:67]\n"
"ds_read2_b64 v[32:35], v161 offset0:0 offset1:2\n"
"v_add_f32 v242, v242, v200\n"
"v_mfma_f32_16x16x32_bf16 v[68:71], a[164:167], v[208:211], v[68:71]\n"
"ds_read2_b64 v[36:39], v161 offset0:136 offset1:138\n"
"v_exp_f32 v201, v201\n"
"v_mfma_f32_16x16x32_bf16 v[72:75], a[168:171], v[208:211], v[72:75]\n"
"ds_read2_b64 v[40:43], v164 offset0:0 offset1:2\n"
"v_add_f32 v242, v242, v201\n"
"v_cvt_pk_bf16_f32 v212, v200, v201\n"
"v_mfma_f32_16x16x32_bf16 v[76:79], a[172:175], v[208:211], v[76:79]\n"
"ds_read2_b64 v[44:47], v164 offset0:136 offset1:138\n"
"v_exp_f32 v202, v202\n"
"v_mfma_f32_16x16x32_bf16 v[80:83], a[176:179], v[208:211], v[80:83]\n"
"v_add_f32 v242, v242, v202\n"
"v_mfma_f32_16x16x32_bf16 v[84:87], a[180:183], v[208:211], v[84:87]\n"
"v_exp_f32 v203, v203\n"
"v_mfma_f32_16x16x32_bf16 v[88:91], a[184:187], v[208:211], v[88:91]\n"
"v_add_f32 v242, v242, v203\n"
"v_cvt_pk_bf16_f32 v213, v202, v203\n"
"v_mfma_f32_16x16x32_bf16 v[92:95], a[188:191], v[208:211], v[92:95]\n"
"v_exp_f32 v204, v204\n"
"v_mfma_f32_16x16x32_bf16 v[192:195], a[96:99], a[48:51], v[228:231]\n"
"v_add_f32 v242, v242, v204\n"
"v_mfma_f32_16x16x32_bf16 v[192:195], a[100:103], a[52:55], v[192:195]\n"
"v_exp_f32 v205, v205\n"
"v_mfma_f32_16x16x32_bf16 v[192:195], a[104:107], a[56:59], v[192:195]\n"
"v_add_f32 v242, v242, v205\n"
"v_cvt_pk_bf16_f32 v214, v204, v205\n"
"v_mfma_f32_16x16x32_bf16 v[192:195], a[108:111], a[60:63], v[192:195]\n"
"s_waitcnt lgkmcnt(0)\n"
"v_exp_f32 v206, v206\n"
"v_mfma_f32_16x16x32_bf16 v[196:199], a[112:115], a[48:51], v[228:231]\n"
"buffer_store_dwordx4 v[32:35], v255, s[48:51], s68 offen sc1\n"
"v_add_f32 v242, v242, v206\n"
"v_mfma_f32_16x16x32_bf16 v[196:199], a[116:119], a[52:55], v[196:199]\n"
"v_exp_f32 v207, v207\n"
"v_mfma_f32_16x16x32_bf16 v[196:199], a[120:123], a[56:59], v[196:199]\n"
"v_add_f32 v242, v242, v207\n"
"v_cvt_pk_bf16_f32 v215, v206, v207\n"
"v_mfma_f32_16x16x32_bf16 v[196:199], a[124:127], a[60:63], v[196:199]\n"
"v_exp_f32 v192, v192\n"
"v_mfma_f32_16x16x32_bf16 v[64:67], a[192:195], v[212:215], v[64:67]\n"
"buffer_store_dwordx4 v[36:39], v255, s[48:51], s68 offen offset:1024 sc1\n"
"v_add_f32 v243, v243, v192\n"
"v_mfma_f32_16x16x32_bf16 v[68:71], a[196:199], v[212:215], v[68:71]\n"
"v_exp_f32 v193, v193\n"
"v_mfma_f32_16x16x32_bf16 v[72:75], a[200:203], v[212:215], v[72:75]\n"
"v_add_f32 v243, v243, v193\n"
"v_cvt_pk_bf16_f32 v208, v192, v193\n"
"v_mfma_f32_16x16x32_bf16 v[76:79], a[204:207], v[212:215], v[76:79]\n"
"Lhop8%=:\n"
"s_cbranch_execz Lhopret%=\n"
"v_exp_f32 v194, v194\n"
"v_mfma_f32_16x16x32_bf16 v[80:83], a[208:211], v[212:215], v[80:83]\n"
"buffer_store_dwordx4 v[40:43], v255, s[48:51], s68 offen offset:2048 sc1\n"
"v_add_f32 v243, v243, v194\n"
"v_mfma_f32_16x16x32_bf16 v[84:87], a[212:215], v[212:215], v[84:87]\n"
"v_exp_f32 v195, v195\n"
"v_mfma_f32_16x16x32_bf16 v[88:91], a[216:219], v[212:215], v[88:91]\n"
"v_add_f32 v243, v243, v195\n"
"v_cvt_pk_bf16_f32 v209, v194, v195\n"
"v_mfma_f32_16x16x32_bf16 v[92:95], a[220:223], v[212:215], v[92:95]\n"
"v_exp_f32 v196, v196\n"
"v_mfma_f32_16x16x4_f32 v[224:227], v250, v242, 0\n"
"s_nop 9\n"
"v_rcp_f32 v226, v224\n"
"v_med3_f32 v225, v224, v251, s73\n"
"v_cmp_neq_f32 vcc, v225, v224\n"
"v_mul_f32 v227, s99, v226\n"
"v_pk_fma_f32 v[64:65], v[64:65], v[226:227], v[226:227] op_sel:[0,0,1] op_sel_hi:[1,0,1] neg_lo:[0,0,1] neg_hi:[0,0,1]\n"
"v_pk_fma_f32 v[66:67], v[66:67], v[226:227], v[226:227] op_sel:[0,0,1] op_sel_hi:[1,0,1] neg_lo:[0,0,1] neg_hi:[0,0,1]\n"
"v_pk_fma_f32 v[68:69], v[68:69], v[226:227], v[226:227] op_sel:[0,0,1] op_sel_hi:[1,0,1] neg_lo:[0,0,1] neg_hi:[0,0,1]\n"
"v_pk_fma_f32 v[70:71], v[70:71], v[226:227], v[226:227] op_sel:[0,0,1] op_sel_hi:[1,0,1] neg_lo:[0,0,1] neg_hi:[0,0,1]\n"
"v_pk_fma_f32 v[72:73], v[72:73], v[226:227], v[226:227] op_sel:[0,0,1] op_sel_hi:[1,0,1] neg_lo:[0,0,1] neg_hi:[0,0,1]\n"
"v_pk_fma_f32 v[74:75], v[74:75], v[226:227], v[226:227] op_sel:[0,0,1] op_sel_hi:[1,0,1] neg_lo:[0,0,1] neg_hi:[0,0,1]\n"
"v_pk_fma_f32 v[76:77], v[76:77], v[226:227], v[226:227] op_sel:[0,0,1] op_sel_hi:[1,0,1] neg_lo:[0,0,1] neg_hi:[0,0,1]\n"
"v_pk_fma_f32 v[78:79], v[78:79], v[226:227], v[226:227] op_sel:[0,0,1] op_sel_hi:[1,0,1] neg_lo:[0,0,1] neg_hi:[0,0,1]\n"
"v_pk_fma_f32 v[80:81], v[80:81], v[226:227], v[226:227] op_sel:[0,0,1] op_sel_hi:[1,0,1] neg_lo:[0,0,1] neg_hi:[0,0,1]\n"
"v_pk_fma_f32 v[82:83], v[82:83], v[226:227], v[226:227] op_sel:[0,0,1] op_sel_hi:[1,0,1] neg_lo:[0,0,1] neg_hi:[0,0,1]\n"
"v_pk_fma_f32 v[84:85], v[84:85], v[226:227], v[226:227] op_sel:[0,0,1] op_sel_hi:[1,0,1] neg_lo:[0,0,1] neg_hi:[0,0,1]\n"
"v_pk_fma_f32 v[86:87], v[86:87], v[226:227], v[226:227] op_sel:[0,0,1] op_sel_hi:[1,0,1] neg_lo:[0,0,1] neg_hi:[0,0,1]\n"
"v_pk_fma_f32 v[88:89], v[88:89], v[226:227], v[226:227] op_sel:[0,0,1] op_sel_hi:[1,0,1] neg_lo:[0,0,1] neg_hi:[0,0,1]\n"
"v_pk_fma_f32 v[90:91], v[90:91], v[226:227], v[226:227] op_sel:[0,0,1] op_sel_hi:[1,0,1] neg_lo:[0,0,1] neg_hi:[0,0,1]\n"
"v_pk_fma_f32 v[92:93], v[92:93], v[226:227], v[226:227] op_sel:[0,0,1] op_sel_hi:[1,0,1] neg_lo:[0,0,1] neg_hi:[0,0,1]\n"
"v_pk_fma_f32 v[94:95], v[94:95], v[226:227], v[226:227] op_sel:[0,0,1] op_sel_hi:[1,0,1] neg_lo:[0,0,1] neg_hi:[0,0,1]\n"
"s_cmp_lg_u64 vcc, 0\n"
"s_addc_u32 s55, s55, s55\n"
"v_cvt_pk_bf16_f32 v64, v64, v65\n"
"v_cvt_pk_bf16_f32 v65, v66, v67\n"
"v_cvt_pk_bf16_f32 v66, v68, v69\n"
"v_cvt_pk_bf16_f32 v67, v70, v71\n"
"v_cvt_pk_bf16_f32 v68, v72, v73\n"
"v_cvt_pk_bf16_f32 v69, v74, v75\n"
"v_cvt_pk_bf16_f32 v70, v76, v77\n"
"v_cvt_pk_bf16_f32 v71, v78, v79\n"
"v_cvt_pk_bf16_f32 v72, v80, v81\n"
"v_cvt_pk_bf16_f32 v73, v82, v83\n"
"v_cvt_pk_bf16_f32 v74, v84, v85\n"
"v_cvt_pk_bf16_f32 v75, v86, v87\n"
"v_cvt_pk_bf16_f32 v76, v88, v89\n"
"v_cvt_pk_bf16_f32 v77, v90, v91\n"
"v_cvt_pk_bf16_f32 v78, v92, v93\n"
"v_cvt_pk_bf16_f32 v79, v94, v95\n"
"v_mfma_f32_16x16x32_bf16 v[200:203], a[128:131], a[48:51], v[228:231]\n"
"ds_write_b128 v160, v[64:67] offset:0\n"
"buffer_store_dwordx4 v[44:47], v255, s[48:51], s68 offen offset:3072 sc1\n"
"v_add_f32 v243, v243, v196\n"
"v_mfma_f32_16x16x32_bf16 v[200:203], a[132:135], a[52:55], v[200:203]\n"
"s_add_u32 s68, s68, 4096\n"
"v_exp_f32 v197, v197\n"
"v_mfma_f32_16x16x32_bf16 v[200:203], a[136:139], a[56:59], v[200:203]\n"
"ds_write_b128 v160, v[68:71] offset:64\n"
"v_add_f32 v243, v243, v197\n"
"v_cvt_pk_bf16_f32 v210, v196, v197\n"
"v_mfma_f32_16x16x32_bf16 v[200:203], a[140:143], a[60:63], v[200:203]\n"
"v_exp_f32 v198, v198\n"
"v_mfma_f32_16x16x32_bf16 v[204:207], a[144:147], a[48:51], v[228:231]\n"
"ds_write_b128 v160, v[72:75] offset:128\n"
"v_add_f32 v243, v243, v198\n"
"v_mfma_f32_16x16x32_bf16 v[204:207], a[148:151], a[52:55], v[204:207]\n"
"v_exp_f32 v199, v199\n"
"v_mfma_f32_16x16x32_bf16 v[204:207], a[152:155], a[56:59], v[204:207]\n"
"ds_write_b128 v160, v[76:79] offset:192\n"
"v_add_f32 v243, v243, v199\n"
"v_cvt_pk_bf16_f32 v211, v198, v199\n"
"v_mfma_f32_16x16x32_bf16 v[204:207], a[156:159], a[60:63], v[204:207]\n"
"v_exp_f32 v200, v200\n"
"v_mfma_f32_16x16x32_bf16 v[96:99], a[160:163], v[208:211], v[96:99]\n"
"ds_read2_b64 v[64:67], v161 offset0:0 offset1:2\n"
"v_add_f32 v243, v243, v200\n"
"v_mfma_f32_16x16x32_bf16 v[100:103], a[164:167], v[208:211], v[100:103]\n"
"ds_read2_b64 v[68:71], v161 offset0:136 offset1:138\n"
"v_exp_f32 v201, v201\n"
"v_mfma_f32_16x16x32_bf16 v[104:107], a[168:171], v[208:211], v[104:107]\n"
"ds_read2_b64 v[72:75], v164 offset0:0 offset1:2\n"
"v_add_f32 v243, v243, v201\n"
"v_cvt_pk_bf16_f32 v212, v200, v201\n"
"v_mfma_f32_16x16x32_bf16 v[108:111], a[172:175], v[208:211], v[108:111]\n"
"ds_read2_b64 v[76:79], v164 offset0:136 offset1:138\n"
"v_exp_f32 v202, v202\n"
"v_mfma_f32_16x16x32_bf16 v[112:115], a[176:179], v[208:211], v[112:115]\n"
"v_add_f32 v243, v243, v202\n"
"v_mfma_f32_16x16x32_bf16 v[116:119], a[180:183], v[208:211], v[116:119]\n"
"v_exp_f32 v203, v203\n"
"v_mfma_f32_16x16x32_bf16 v[120:123], a[184:187], v[208:211], v[120:123]\n"
"v_add_f32 v243, v243, v203\n"
"v_cvt_pk_bf16_f32 v213, v202, v203\n"
"v_mfma_f32_16x16x32_bf16 v[124:127], a[188:191], v[208:211], v[124:127]\n"
"v_exp_f32 v204, v204\n"
"v_mfma_f32_16x16x32_bf16 v[192:195], a[96:99], a[64:67], v[232:235]\n"
"v_add_f32 v243, v243, v204\n"
"v_mfma_f32_16x16x32_bf16 v[192:195], a[100:103], a[68:71], v[192:195]\n"
"v_exp_f32 v205, v205\n"
"v_mfma_f32_16x16x32_bf16 v[192:195], a[104:107], a[72:75], v[192:195]\n"
"v_add_f32 v243, v243, v205\n"
"v_cvt_pk_bf16_f32 v214, v204, v205\n"
"v_mfma_f32_16x16x32_bf16 v[192:195], a[108:111], a[76:79], v[192:195]\n"
"s_waitcnt lgkmcnt(0)\n"
"v_exp_f32 v206, v206\n"
"v_mfma_f32_16x16x32_bf16 v[196:199], a[112:115], a[64:67], v[232:235]\n"
"buffer_store_dwordx4 v[64:67], v255, s[48:51], s68 offen sc1\n"
"v_add_f32 v243, v243, v206\n"
"v_mfma_f32_16x16x32_bf16 v[196:199], a[116:119], a[68:71], v[196:199]\n"
"v_exp_f32 v207, v207\n"
"v_mfma_f32_16x16x32_bf16 v[196:199], a[120:123], a[72:75], v[196:199]\n"
"v_add_f32 v243, v243, v207\n"
"v_cvt_pk_bf16_f32 v215, v206, v207\n"
"v_mfma_f32_16x16x32_bf16 v[196:199], a[124:127], a[76:79], v[196:199]\n"
"v_exp_f32 v192, v192\n"
"v_mfma_f32_16x16x32_bf16 v[96:99], a[192:195], v[212:215], v[96:99]\n"
"buffer_store_dwordx4 v[68:71], v255, s[48:51], s68 offen offset:1024 sc1\n"
"v_add_f32 v244, v244, v192\n"
"v_mfma_f32_16x16x32_bf16 v[100:103], a[196:199], v[212:215], v[100:103]\n"
"v_exp_f32 v193, v193\n"
"v_mfma_f32_16x16x32_bf16 v[104:107], a[200:203], v[212:215], v[104:107]\n"
"v_add_f32 v244, v244, v193\n"
"v_cvt_pk_bf16_f32 v208, v192, v193\n"
"v_mfma_f32_16x16x32_bf16 v[108:111], a[204:207], v[212:215], v[108:111]\n"
"v_exp_f32 v194, v194\n"
"v_mfma_f32_16x16x32_bf16 v[112:115], a[208:211], v[212:215], v[112:115]\n"
"buffer_store_dwordx4 v[72:75], v255, s[48:51], s68 offen offset:2048 sc1\n"
"v_add_f32 v244, v244, v194\n"
"v_mfma_f32_16x16x32_bf16 v[116:119], a[212:215], v[212:215], v[116:119]\n"
"v_exp_f32 v195, v195\n"
"v_mfma_f32_16x16x32_bf16 v[120:123], a[216:219], v[212:215], v[120:123]\n"
"v_add_f32 v244, v244, v195\n"
"v_cvt_pk_bf16_f32 v209, v194, v195\n"
"v_mfma_f32_16x16x32_bf16 v[124:127], a[220:223], v[212:215], v[124:127]\n"
"v_exp_f32 v196, v196\n"
"v_mfma_f32_16x16x4_f32 v[228:231], v250, v243, 0\n"
"s_nop 9\n"
"v_rcp_f32 v230, v228\n"
"v_med3_f32 v229, v228, v251, s73\n"
"v_cmp_neq_f32 vcc, v229, v228\n"
"v_mul_f32 v231, s99, v230\n"
"v_pk_fma_f32 v[96:97], v[96:97], v[230:231], v[230:231] op_sel:[0,0,1] op_sel_hi:[1,0,1] neg_lo:[0,0,1] neg_hi:[0,0,1]\n"
"v_pk_fma_f32 v[98:99], v[98:99], v[230:231], v[230:231] op_sel:[0,0,1] op_sel_hi:[1,0,1] neg_lo:[0,0,1] neg_hi:[0,0,1]\n"
"v_pk_fma_f32 v[100:101], v[100:101], v[230:231], v[230:231] op_sel:[0,0,1] op_sel_hi:[1,0,1] neg_lo:[0,0,1] neg_hi:[0,0,1]\n"
"v_pk_fma_f32 v[102:103], v[102:103], v[230:231], v[230:231] op_sel:[0,0,1] op_sel_hi:[1,0,1] neg_lo:[0,0,1] neg_hi:[0,0,1]\n"
"v_pk_fma_f32 v[104:105], v[104:105], v[230:231], v[230:231] op_sel:[0,0,1] op_sel_hi:[1,0,1] neg_lo:[0,0,1] neg_hi:[0,0,1]\n"
"v_pk_fma_f32 v[106:107], v[106:107], v[230:231], v[230:231] op_sel:[0,0,1] op_sel_hi:[1,0,1] neg_lo:[0,0,1] neg_hi:[0,0,1]\n"
"v_pk_fma_f32 v[108:109], v[108:109], v[230:231], v[230:231] op_sel:[0,0,1] op_sel_hi:[1,0,1] neg_lo:[0,0,1] neg_hi:[0,0,1]\n"
"v_pk_fma_f32 v[110:111], v[110:111], v[230:231], v[230:231] op_sel:[0,0,1] op_sel_hi:[1,0,1] neg_lo:[0,0,1] neg_hi:[0,0,1]\n"
"v_pk_fma_f32 v[112:113], v[112:113], v[230:231], v[230:231] op_sel:[0,0,1] op_sel_hi:[1,0,1] neg_lo:[0,0,1] neg_hi:[0,0,1]\n"
"v_pk_fma_f32 v[114:115], v[114:115], v[230:231], v[230:231] op_sel:[0,0,1] op_sel_hi:[1,0,1] neg_lo:[0,0,1] neg_hi:[0,0,1]\n"
"v_pk_fma_f32 v[116:117], v[116:117], v[230:231], v[230:231] op_sel:[0,0,1] op_sel_hi:[1,0,1] neg_lo:[0,0,1] neg_hi:[0,0,1]\n"
"v_pk_fma_f32 v[118:119], v[118:119], v[230:231], v[230:231] op_sel:[0,0,1] op_sel_hi:[1,0,1] neg_lo:[0,0,1] neg_hi:[0,0,1]\n"
"v_pk_fma_f32 v[120:121], v[120:121], v[230:231], v[230:231] op_sel:[0,0,1] op_sel_hi:[1,0,1] neg_lo:[0,0,1] neg_hi:[0,0,1]\n"
"v_pk_fma_f32 v[122:123], v[122:123], v[230:231], v[230:231] op_sel:[0,0,1] op_sel_hi:[1,0,1] neg_lo:[0,0,1] neg_hi:[0,0,1]\n"
"v_pk_fma_f32 v[124:125], v[124:125], v[230:231], v[230:231] op_sel:[0,0,1] op_sel_hi:[1,0,1] neg_lo:[0,0,1] neg_hi:[0,0,1]\n"
"v_pk_fma_f32 v[126:127], v[126:127], v[230:231], v[230:231] op_sel:[0,0,1] op_sel_hi:[1,0,1] neg_lo:[0,0,1] neg_hi:[0,0,1]\n"
"s_cmp_lg_u64 vcc, 0\n"
"s_addc_u32 s55, s55, s55\n"
"v_cvt_pk_bf16_f32 v96, v96, v97\n"
"v_cvt_pk_bf16_f32 v97, v98, v99\n"
"v_cvt_pk_bf16_f32 v98, v100, v101\n"
"v_cvt_pk_bf16_f32 v99, v102, v103\n"
"v_cvt_pk_bf16_f32 v100, v104, v105\n"
"v_cvt_pk_bf16_f32 v101, v106, v107\n"
"v_cvt_pk_bf16_f32 v102, v108, v109\n"
"v_cvt_pk_bf16_f32 v103, v110, v111\n"
"v_cvt_pk_bf16_f32 v104, v112, v113\n"
"v_cvt_pk_bf16_f32 v105, v114, v115\n"
"v_cvt_pk_bf16_f32 v106, v116, v117\n"
"v_cvt_pk_bf16_f32 v107, v118, v119\n"
"v_cvt_pk_bf16_f32 v108, v120, v121\n"
"v_cvt_pk_bf16_f32 v109, v122, v123\n"
"v_cvt_pk_bf16_f32 v110, v124, v125\n"
"v_cvt_pk_bf16_f32 v111, v126, v127\n"
"v_mfma_f32_16x16x32_bf16 v[200:203], a[128:131], a[64:67], v[232:235]\n"
"ds_write_b128 v160, v[96:99] offset:0\n"
"buffer_store_dwordx4 v[76:79], v255, s[48:51], s68 offen offset:3072 sc1\n"
"v_add_f32 v244, v244, v196\n"
"v_mfma_f32_16x16x32_bf16 v[200:203], a[132:135], a[68:71], v[200:203]\n"
"s_add_u32 s68, s68, 4096\n"
"v_exp_f32 v197, v197\n"
"v_mfma_f32_16x16x32_bf16 v[200:203], a[136:139], a[72:75], v[200:203]\n"
"ds_write_b128 v160, v[100:103] offset:64\n"
"v_add_f32 v244, v244, v197\n"
"v_cvt_pk_bf16_f32 v210, v196, v197\n"
"v_mfma_f32_16x16x32_bf16 v[200:203], a[140:143], a[76:79], v[200:203]\n"
"v_exp_f32 v198, v198\n"
"v_mfma_f32_16x16x32_bf16 v[204:207], a[144:147], a[64:67], v[232:235]\n"
"ds_write_b128 v160, v[104:107] offset:128\n"
"v_add_f32 v244, v244, v198\n"
"v_mfma_f32_16x16x32_bf16 v[204:207], a[148:151], a[68:71], v[204:207]\n"
"v_exp_f32 v199, v199\n"
"v_mfma_f32_16x16x32_bf16 v[204:207], a[152:155], a[72:75], v[204:207]\n"
"ds_write_b128 v160, v[108:111] offset:192\n"
"v_add_f32 v244, v244, v199\n"
"v_cvt_pk_bf16_f32 v211, v198, v199\n"
"v_mfma_f32_16x16x32_bf16 v[204:207], a[156:159], a[76:79], v[204:207]\n"
"v_exp_f32 v200, v200\n"
"v_mfma_f32_16x16x32_bf16 v[128:131], a[160:163], v[208:211], v[128:131]\n"
"ds_read2_b64 v[96:99], v161 offset0:0 offset1:2\n"
"v_add_f32 v244, v244, v200\n"
"v_mfma_f32_16x16x32_bf16 v[132:135], a[164:167], v[208:211], v[132:135]\n"
"ds_read2_b64 v[100:103], v161 offset0:136 offset1:138\n"
"v_exp_f32 v201, v201\n"
"v_mfma_f32_16x16x32_bf16 v[136:139], a[168:171], v[208:211], v[136:139]\n"
"ds_read2_b64 v[104:107], v164 offset0:0 offset1:2\n"
"v_add_f32 v244, v244, v201\n"
"v_cvt_pk_bf16_f32 v212, v200, v201\n"
"v_mfma_f32_16x16x32_bf16 v[140:143], a[172:175], v[208:211], v[140:143]\n"
"ds_read2_b64 v[108:111], v164 offset0:136 offset1:138\n"
"v_exp_f32 v202, v202\n"
"v_mfma_f32_16x16x32_bf16 v[144:147], a[176:179], v[208:211], v[144:147]\n"
"v_add_f32 v244, v244, v202\n"
"v_mfma_f32_16x16x32_bf16 v[148:151], a[180:183], v[208:211], v[148:151]\n"
"v_exp_f32 v203, v203\n"
"v_mfma_f32_16x16x32_bf16 v[152:155], a[184:187], v[208:211], v[152:155]\n"
"v_add_f32 v244, v244, v203\n"
"v_cvt_pk_bf16_f32 v213, v202, v203\n"
"v_mfma_f32_16x16x32_bf16 v[156:159], a[188:191], v[208:211], v[156:159]\n"
"v_exp_f32 v204, v204\n"
"s_nop 0\n"
"v_add_f32 v244, v244, v204\n"
"v_exp_f32 v205, v205\n"
"s_nop 0\n"
"v_add_f32 v244, v244, v205\n"
"v_cvt_pk_bf16_f32 v214, v204, v205\n"
"v_exp_f32 v206, v206\n"
"s_nop 0\n"
"v_add_f32 v244, v244, v206\n"
"v_exp_f32 v207, v207\n"
"s_nop 0\n"
"v_add_f32 v244, v244, v207\n"
"v_cvt_pk_bf16_f32 v215, v206, v207\n"
"s_waitcnt lgkmcnt(0)\n"
"v_mfma_f32_16x16x4_f32 v[232:235], v250, v244, 0\n"
"v_mfma_f32_16x16x32_bf16 v[128:131], a[192:195], v[212:215], v[128:131]\n"
"buffer_store_dwordx4 v[96:99], v255, s[48:51], s68 offen sc1\n"
"v_mfma_f32_16x16x32_bf16 v[132:135], a[196:199], v[212:215], v[132:135]\n"
"v_mfma_f32_16x16x32_bf16 v[136:139], a[200:203], v[212:215], v[136:139]\n"
"buffer_store_dwordx4 v[100:103], v255, s[48:51], s68 offen offset:1024 sc1\n"
"v_mfma_f32_16x16x32_bf16 v[140:143], a[204:207], v[212:215], v[140:143]\n"
"v_mfma_f32_16x16x32_bf16 v[144:147], a[208:211], v[212:215], v[144:147]\n"
"buffer_store_dwordx4 v[104:107], v255, s[48:51], s68 offen offset:2048 sc1\n"
"v_mfma_f32_16x16x32_bf16 v[148:151], a[212:215], v[212:215], v[148:151]\n"
"v_mfma_f32_16x16x32_bf16 v[152:155], a[216:219], v[212:215], v[152:155]\n"
"buffer_store_dwordx4 v[108:111], v255, s[48:51], s68 offen offset:3072 sc1\n"
"v_mfma_f32_16x16x32_bf16 v[156:159], a[220:223], v[212:215], v[156:159]\n"
"s_add_u32 s68, s68, 4096\n"
"v_rcp_f32 v234, v232\n"
"v_med3_f32 v233, v232, v251, s73\n"
"v_cmp_neq_f32 vcc, v233, v232\n"
"v_mul_f32 v235, s99, v234\n"
"v_pk_fma_f32 v[128:129], v[128:129], v[234:235], v[234:235] op_sel:[0,0,1] op_sel_hi:[1,0,1] neg_lo:[0,0,1] neg_hi:[0,0,1]\n"
"v_pk_fma_f32 v[130:131], v[130:131], v[234:235], v[234:235] op_sel:[0,0,1] op_sel_hi:[1,0,1] neg_lo:[0,0,1] neg_hi:[0,0,1]\n"
"v_pk_fma_f32 v[132:133], v[132:133], v[234:235], v[234:235] op_sel:[0,0,1] op_sel_hi:[1,0,1] neg_lo:[0,0,1] neg_hi:[0,0,1]\n"
"v_pk_fma_f32 v[134:135], v[134:135], v[234:235], v[234:235] op_sel:[0,0,1] op_sel_hi:[1,0,1] neg_lo:[0,0,1] neg_hi:[0,0,1]\n"
"v_pk_fma_f32 v[136:137], v[136:137], v[234:235], v[234:235] op_sel:[0,0,1] op_sel_hi:[1,0,1] neg_lo:[0,0,1] neg_hi:[0,0,1]\n"
"v_pk_fma_f32 v[138:139], v[138:139], v[234:235], v[234:235] op_sel:[0,0,1] op_sel_hi:[1,0,1] neg_lo:[0,0,1] neg_hi:[0,0,1]\n"
"v_pk_fma_f32 v[140:141], v[140:141], v[234:235], v[234:235] op_sel:[0,0,1] op_sel_hi:[1,0,1] neg_lo:[0,0,1] neg_hi:[0,0,1]\n"
"v_pk_fma_f32 v[142:143], v[142:143], v[234:235], v[234:235] op_sel:[0,0,1] op_sel_hi:[1,0,1] neg_lo:[0,0,1] neg_hi:[0,0,1]\n"
"v_pk_fma_f32 v[144:145], v[144:145], v[234:235], v[234:235] op_sel:[0,0,1] op_sel_hi:[1,0,1] neg_lo:[0,0,1] neg_hi:[0,0,1]\n"
"v_pk_fma_f32 v[146:147], v[146:147], v[234:235], v[234:235] op_sel:[0,0,1] op_sel_hi:[1,0,1] neg_lo:[0,0,1] neg_hi:[0,0,1]\n"
"v_pk_fma_f32 v[148:149], v[148:149], v[234:235], v[234:235] op_sel:[0,0,1] op_sel_hi:[1,0,1] neg_lo:[0,0,1] neg_hi:[0,0,1]\n"
"v_pk_fma_f32 v[150:151], v[150:151], v[234:235], v[234:235] op_sel:[0,0,1] op_sel_hi:[1,0,1] neg_lo:[0,0,1] neg_hi:[0,0,1]\n"
"v_pk_fma_f32 v[152:153], v[152:153], v[234:235], v[234:235] op_sel:[0,0,1] op_sel_hi:[1,0,1] neg_lo:[0,0,1] neg_hi:[0,0,1]\n"
"v_pk_fma_f32 v[154:155], v[154:155], v[234:235], v[234:235] op_sel:[0,0,1] op_sel_hi:[1,0,1] neg_lo:[0,0,1] neg_hi:[0,0,1]\n"
"v_pk_fma_f32 v[156:157], v[156:157], v[234:235], v[234:235] op_sel:[0,0,1] op_sel_hi:[1,0,1] neg_lo:[0,0,1] neg_hi:[0,0,1]\n"
"v_pk_fma_f32 v[158:159], v[158:159], v[234:235], v[234:235] op_sel:[0,0,1] op_sel_hi:[1,0,1] neg_lo:[0,0,1] neg_hi:[0,0,1]\n"
"s_cmp_lg_u64 vcc, 0\n"
"s_addc_u32 s55, s55, s55\n"
"v_cvt_pk_bf16_f32 v128, v128, v129\n"
"v_cvt_pk_bf16_f32 v129, v130, v131\n"
"v_cvt_pk_bf16_f32 v130, v132, v133\n"
"v_cvt_pk_bf16_f32 v131, v134, v135\n"
"ds_write_b128 v160, v[128:131] offset:0\n"
"v_cvt_pk_bf16_f32 v132, v136, v137\n"
"v_cvt_pk_bf16_f32 v133, v138, v139\n"
"v_cvt_pk_bf16_f32 v134, v140, v141\n"
"v_cvt_pk_bf16_f32 v135, v142, v143\n"
"ds_write_b128 v160, v[132:135] offset:64\n"
"v_cvt_pk_bf16_f32 v136, v144, v145\n"
"v_cvt_pk_bf16_f32 v137, v146, v147\n"
"v_cvt_pk_bf16_f32 v138, v148, v149\n"
"v_cvt_pk_bf16_f32 v139, v150, v151\n"
"ds_write_b128 v160, v[136:139] offset:128\n"
"v_cvt_pk_bf16_f32 v140, v152, v153\n"
"v_cvt_pk_bf16_f32 v141, v154, v155\n"
"v_cvt_pk_bf16_f32 v142, v156, v157\n"
"v_cvt_pk_bf16_f32 v143, v158, v159\n"
"ds_write_b128 v160, v[140:143] offset:192\n"
"ds_read2_b64 v[128:131], v161 offset0:0 offset1:2\n"
"ds_read2_b64 v[132:135], v161 offset0:136 offset1:138\n"
"ds_read2_b64 v[136:139], v164 offset0:0 offset1:2\n"
"ds_read2_b64 v[140:143], v164 offset0:136 offset1:138\n"
"s_waitcnt lgkmcnt(0)\n"
"buffer_store_dwordx4 v[128:131], v255, s[48:51], s68 offen sc1\n"
"buffer_store_dwordx4 v[132:135], v255, s[48:51], s68 offen offset:1024 sc1\n"
"buffer_store_dwordx4 v[136:139], v255, s[48:51], s68 offen offset:2048 sc1\n"
"buffer_store_dwordx4 v[140:143], v255, s[48:51], s68 offen offset:3072 sc1\n"
"s_add_u32 s68, s68, 4096\n"
"s_branch Lend%=\n"
"LSEAMB%=:\n"
"v_exp_f32 v196, v196\n"
"v_mfma_f32_16x16x32_bf16 v[200:203], a[96:99], a[16:19], v[220:223]\n"
"ds_read_b128 a[128:131], v246 offset:24576\n"
"v_add_f32 v240, v240, v196\n"
"v_mfma_f32_16x16x32_bf16 v[200:203], a[100:103], a[20:23], v[200:203]\n"
"v_exp_f32 v197, v197\n"
"v_mfma_f32_16x16x32_bf16 v[200:203], a[104:107], a[24:27], v[200:203]\n"
"v_add_f32 v240, v240, v197\n"
"v_cvt_pk_bf16_f32 v210, v196, v197\n"
"v_mfma_f32_16x16x32_bf16 v[200:203], a[108:111], a[28:31], v[200:203]\n"
"v_exp_f32 v198, v198\n"
"v_mfma_f32_16x16x32_bf16 v[204:207], a[112:115], a[16:19], v[220:223]\n"
"ds_read_b128 a[132:135], v246 offset:25088\n"
"v_add_f32 v240, v240, v198\n"
"v_mfma_f32_16x16x32_bf16 v[204:207], a[116:119], a[20:23], v[204:207]\n"
"s_waitcnt lgkmcnt(2)\n"
"v_exp_f32 v199, v199\n"
"v_mfma_f32_16x16x32_bf16 v[204:207], a[120:123], a[24:27], v[204:207]\n"
"s_waitcnt vmcnt(38)\n"
"v_add_f32 v240, v240, v199\n"
"v_cvt_pk_bf16_f32 v211, v198, v199\n"
"v_mfma_f32_16x16x32_bf16 v[204:207], a[124:127], a[28:31], v[204:207]\n"
"s_barrier\n"
"v_exp_f32 v200, v200\n"
"v_mfma_f32_16x16x32_bf16 v[0:3], a[160:163], v[208:211], v[0:3]\n"
"s_add_u32 s56, s56, 8192\n"
"ds_read_b128 a[136:139], v246 offset:25600\n"
"v_add_f32 v241, v241, v200\n"
"v_mfma_f32_16x16x32_bf16 v[4:7], a[164:167], v[208:211], v[4:7]\n"
"v_exp_f32 v201, v201\n"
"v_mfma_f32_16x16x32_bf16 v[8:11], a[168:171], v[208:211], v[8:11]\n"
"s_mov_b32 m0, 16384\n"
"ds_read_b128 a[140:143], v246 offset:26112\n"
"v_add_f32 v241, v241, v201\n"
"v_cvt_pk_bf16_f32 v212, v200, v201\n"
"v_mfma_f32_16x16x32_bf16 v[12:15], a[172:175], v[208:211], v[12:15]\n"
"s_add_u32 s59, s56, 0\n"
"buffer_load_dwordx4 v252, s[36:39], s59 offen lds\n"
"v_exp_f32 v202, v202\n"
"v_mfma_f32_16x16x32_bf16 v[16:19], a[176:179], v[208:211], v[16:19]\n"
"ds_read_b128 a[144:147], v246 offset:28672\n"
"v_add_f32 v241, v241, v202\n"
"v_mfma_f32_16x16x32_bf16 v[20:23], a[180:183], v[208:211], v[20:23]\n"
"buffer_load_dwordx4 v253, s[36:39], s59 offen offset:2048 lds\n"
"v_exp_f32 v203, v203\n"
"v_mfma_f32_16x16x32_bf16 v[24:27], a[184:187], v[208:211], v[24:27]\n"
"s_add_u32 m0, s58, 0\n"
"v_add_f32 v241, v241, v203\n"
"v_cvt_pk_bf16_f32 v213, v202, v203\n"
"v_mfma_f32_16x16x32_bf16 v[28:31], a[188:191], v[208:211], v[28:31]\n"
"s_add_u32 s59, s57, 0\n"
"buffer_load_dwordx4 v252, s[44:47], s59 offen lds\n"
"v_exp_f32 v204, v204\n"
"v_mfma_f32_16x16x4_f32 v[216:219], v250, v240, 0\n"
"s_waitcnt vmcnt(37)\n"
"ds_read_b128 a[0:3], v254 offset:0\n"
"ds_read_b128 a[4:7], v254 offset:512\n"
"ds_read_b128 a[8:11], v254 offset:1024\n"
"ds_read_b128 a[12:15], v254 offset:1536\n"
"ds_read_b128 a[16:19], v254 offset:4096\n"
"ds_read_b128 a[20:23], v254 offset:4608\n"
"ds_read_b128 a[24:27], v254 offset:5120\n"
"ds_read_b128 a[28:31], v254 offset:5632\n"
"ds_read_b128 a[240:243], v249 offset:24576\n"
"ds_read_b128 a[244:247], v249 offset:25088\n"
"ds_read_b128 a[248:251], v249 offset:25600\n"
"ds_read_b128 a[252:255], v249 offset:26112\n"
"v_add_u32 v164, 2176, v161\n"
"v_rcp_f32 v218, v216\n"
"v_med3_f32 v217, v216, v251, s63\n"
"v_cmp_neq_f32 vcc, v217, v216\n"
"v_mul_f32 v219, s98, v218\n"
"v_pk_fma_f32 v[0:1], v[0:1], v[218:219], v[218:219] op_sel:[0,0,1] op_sel_hi:[1,0,1] neg_lo:[0,0,1] neg_hi:[0,0,1]\n"
"v_pk_fma_f32 v[2:3], v[2:3], v[218:219], v[218:219] op_sel:[0,0,1] op_sel_hi:[1,0,1] neg_lo:[0,0,1] neg_hi:[0,0,1]\n"
"v_pk_fma_f32 v[4:5], v[4:5], v[218:219], v[218:219] op_sel:[0,0,1] op_sel_hi:[1,0,1] neg_lo:[0,0,1] neg_hi:[0,0,1]\n"
"v_pk_fma_f32 v[6:7], v[6:7], v[218:219], v[218:219] op_sel:[0,0,1] op_sel_hi:[1,0,1] neg_lo:[0,0,1] neg_hi:[0,0,1]\n"
"v_pk_fma_f32 v[8:9], v[8:9], v[218:219], v[218:219] op_sel:[0,0,1] op_sel_hi:[1,0,1] neg_lo:[0,0,1] neg_hi:[0,0,1]\n"
"v_pk_fma_f32 v[10:11], v[10:11], v[218:219], v[218:219] op_sel:[0,0,1] op_sel_hi:[1,0,1] neg_lo:[0,0,1] neg_hi:[0,0,1]\n"
"v_pk_fma_f32 v[12:13], v[12:13], v[218:219], v[218:219] op_sel:[0,0,1] op_sel_hi:[1,0,1] neg_lo:[0,0,1] neg_hi:[0,0,1]\n"
"v_pk_fma_f32 v[14:15], v[14:15], v[218:219], v[218:219] op_sel:[0,0,1] op_sel_hi:[1,0,1] neg_lo:[0,0,1] neg_hi:[0,0,1]\n"
"v_pk_fma_f32 v[16:17], v[16:17], v[218:219], v[218:219] op_sel:[0,0,1] op_sel_hi:[1,0,1] neg_lo:[0,0,1] neg_hi:[0,0,1]\n"
"v_pk_fma_f32 v[18:19], v[18:19], v[218:219], v[218:219] op_sel:[0,0,1] op_sel_hi:[1,0,1] neg_lo:[0,0,1] neg_hi:[0,0,1]\n"
"v_pk_fma_f32 v[20:21], v[20:21], v[218:219], v[218:219] op_sel:[0,0,1] op_sel_hi:[1,0,1] neg_lo:[0,0,1] neg_hi:[0,0,1]\n"
"v_pk_fma_f32 v[22:23], v[22:23], v[218:219], v[218:219] op_sel:[0,0,1] op_sel_hi:[1,0,1] neg_lo:[0,0,1] neg_hi:[0,0,1]\n"
"v_pk_fma_f32 v[24:25], v[24:25], v[218:219], v[218:219] op_sel:[0,0,1] op_sel_hi:[1,0,1] neg_lo:[0,0,1] neg_hi:[0,0,1]\n"
"v_pk_fma_f32 v[26:27], v[26:27], v[218:219], v[218:219] op_sel:[0,0,1] op_sel_hi:[1,0,1] neg_lo:[0,0,1] neg_hi:[0,0,1]\n"
"v_pk_fma_f32 v[28:29], v[28:29], v[218:219], v[218:219] op_sel:[0,0,1] op_sel_hi:[1,0,1] neg_lo:[0,0,1] neg_hi:[0,0,1]\n"
"v_pk_fma_f32 v[30:31], v[30:31], v[218:219], v[218:219] op_sel:[0,0,1] op_sel_hi:[1,0,1] neg_lo:[0,0,1] neg_hi:[0,0,1]\n"
"s_cmp_lg_u64 vcc, 0\n"
"s_addc_u32 s55, s55, s55\n"
"v_cvt_pk_bf16_f32 v0, v0, v1\n"
"v_cvt_pk_bf16_f32 v1, v2, v3\n"
"v_cvt_pk_bf16_f32 v2, v4, v5\n"
"v_cvt_pk_bf16_f32 v3, v6, v7\n"
"ds_write_b128 v160, v[0:3] offset:0\n"
"v_cvt_pk_bf16_f32 v4, v8, v9\n"
"v_cvt_pk_bf16_f32 v5, v10, v11\n"
"v_cvt_pk_bf16_f32 v6, v12, v13\n"
"v_cvt_pk_bf16_f32 v7, v14, v15\n"
"ds_write_b128 v160, v[4:7] offset:64\n"
"v_cvt_pk_bf16_f32 v8, v16, v17\n"
"v_cvt_pk_bf16_f32 v9, v18, v19\n"
"v_cvt_pk_bf16_f32 v10, v20, v21\n"
"v_cvt_pk_bf16_f32 v11, v22, v23\n"
"ds_write_b128 v160, v[8:11] offset:128\n"
"v_cvt_pk_bf16_f32 v12, v24, v25\n"
"v_cvt_pk_bf16_f32 v13, v26, v27\n"
"v_cvt_pk_bf16_f32 v14, v28, v29\n"
"v_cvt_pk_bf16_f32 v15, v30, v31\n"
"ds_write_b128 v160, v[12:15] offset:192\n"
"v_mfma_f32_16x16x32_bf16 v[192:195], a[96:99], a[32:35], v[224:227]\n"
"ds_read_b128 a[148:151], v246 offset:29184\n"
"v_add_f32 v241, v241, v204\n"
"v_mfma_f32_16x16x32_bf16 v[192:195], a[100:103], a[36:39], v[192:195]\n"
"ds_read2_b64 v[0:3], v161 offset0:0 offset1:2\n"
"buffer_load_dwordx4 v253, s[44:47], s59 offen offset:2048 lds\n"
"v_exp_f32 v205, v205\n"
"v_mfma_f32_16x16x32_bf16 v[192:195], a[104:107], a[40:43], v[192:195]\n"
"ds_read2_b64 v[4:7], v161 offset0:136 offset1:138\n"
"v_add_f32 v241, v241, v205\n"
"v_cvt_pk_bf16_f32 v214, v204, v205\n"
"v_mfma_f32_16x16x32_bf16 v[192:195], a[108:111], a[44:47], v[192:195]\n"
"ds_read2_b64 v[8:11], v164 offset0:0 offset1:2\n"
"v_exp_f32 v206, v206\n"
"v_mfma_f32_16x16x32_bf16 v[196:199], a[112:115], a[32:35], v[224:227]\n"
"s_add_u32 m0, s58, 1024\n"
"ds_read2_b64 v[12:15], v164 offset0:136 offset1:138\n"
"ds_read_b128 a[152:155], v246 offset:29696\n"
"v_add_f32 v241, v241, v206\n"
"v_mfma_f32_16x16x32_bf16 v[196:199], a[116:119], a[36:39], v[196:199]\n"
"s_add_u32 s59, s57, 128\n"
"buffer_load_dwordx4 v252, s[44:47], s59 offen lds\n"
"v_exp_f32 v207, v207\n"
"v_mfma_f32_16x16x32_bf16 v[196:199], a[120:123], a[40:43], v[196:199]\n"
"v_add_f32 v241, v241, v207\n"
"v_cvt_pk_bf16_f32 v215, v206, v207\n"
"v_mfma_f32_16x16x32_bf16 v[196:199], a[124:127], a[44:47], v[196:199]\n"
"buffer_load_dwordx4 v253, s[44:47], s59 offen offset:2048 lds\n"
"v_exp_f32 v192, v192\n"
"v_mfma_f32_16x16x32_bf16 v[32:35], a[160:163], v[212:215], v[32:35]\n"
"s_add_u32 m0, s58, 4096\n"
"ds_read_b128 a[156:159], v246 offset:30208\n"
"v_add_f32 v242, v242, v192\n"
"v_mfma_f32_16x16x32_bf16 v[36:39], a[164:167], v[212:215], v[36:39]\n"
"s_add_u32 s59, s57, 4096\n"
"buffer_load_dwordx4 v252, s[44:47], s59 offen lds\n"
"v_exp_f32 v193, v193\n"
"v_mfma_f32_16x16x32_bf16 v[40:43], a[168:171], v[212:215], v[40:43]\n"
"v_add_f32 v242, v242, v193\n"
"v_cvt_pk_bf16_f32 v208, v192, v193\n"
"v_mfma_f32_16x16x32_bf16 v[44:47], a[172:175], v[212:215], v[44:47]\n"
"v_exp_f32 v194, v194\n"
"v_mfma_f32_16x16x32_bf16 v[48:51], a[176:179], v[212:215], v[48:51]\n"
"ds_read_b64_tr_b16 a[192:193], v247 offset:57344\n"
"v_add_f32 v242, v242, v194\n"
"v_mfma_f32_16x16x32_bf16 v[52:55], a[180:183], v[212:215], v[52:55]\n"
"buffer_load_dwordx4 v253, s[44:47], s59 offen offset:2048 lds\n"
"v_exp_f32 v195, v195\n"
"v_mfma_f32_16x16x32_bf16 v[56:59], a[184:187], v[212:215], v[56:59]\n"
"s_add_u32 m0, s58, 5120\n"
"ds_read_b64_tr_b16 a[194:195], v247 offset:61440\n"
"v_add_f32 v242, v242, v195\n"
"v_cvt_pk_bf16_f32 v209, v194, v195\n"
"v_mfma_f32_16x16x32_bf16 v[60:63], a[188:191], v[212:215], v[60:63]\n"
"s_add_u32 s59, s57, 4224\n"
"buffer_load_dwordx4 v252, s[44:47], s59 offen lds\n"
"v_exp_f32 v196, v196\n"
"v_mfma_f32_16x16x4_f32 v[220:223], v250, v241, 0\n"
"s_waitcnt lgkmcnt(4)\n"
"buffer_store_dwordx4 v[0:3], v255, s[48:51], s68 offen sc1\n"
"buffer_store_dwordx4 v[4:7], v255, s[48:51], s68 offen offset:1024 sc1\n"
"buffer_store_dwordx4 v[8:11], v255, s[48:51], s68 offen offset:2048 sc1\n"
"buffer_store_dwordx4 v[12:15], v255, s[48:51], s68 offen offset:3072 sc1\n"
"s_add_u32 s68, s68, 4096\n"
"s_nop 3\n"
"v_rcp_f32 v222, v220\n"
"v_med3_f32 v221, v220, v251, s63\n"
"v_cmp_neq_f32 vcc, v221, v220\n"
"v_mul_f32 v223, s98, v222\n"
"v_pk_fma_f32 v[32:33], v[32:33], v[222:223], v[222:223] op_sel:[0,0,1] op_sel_hi:[1,0,1] neg_lo:[0,0,1] neg_hi:[0,0,1]\n"
"v_pk_fma_f32 v[34:35], v[34:35], v[222:223], v[222:223] op_sel:[0,0,1] op_sel_hi:[1,0,1] neg_lo:[0,0,1] neg_hi:[0,0,1]\n"
"v_pk_fma_f32 v[36:37], v[36:37], v[222:223], v[222:223] op_sel:[0,0,1] op_sel_hi:[1,0,1] neg_lo:[0,0,1] neg_hi:[0,0,1]\n"
"v_pk_fma_f32 v[38:39], v[38:39], v[222:223], v[222:223] op_sel:[0,0,1] op_sel_hi:[1,0,1] neg_lo:[0,0,1] neg_hi:[0,0,1]\n"
"v_pk_fma_f32 v[40:41], v[40:41], v[222:223], v[222:223] op_sel:[0,0,1] op_sel_hi:[1,0,1] neg_lo:[0,0,1] neg_hi:[0,0,1]\n"
"v_pk_fma_f32 v[42:43], v[42:43], v[222:223], v[222:223] op_sel:[0,0,1] op_sel_hi:[1,0,1] neg_lo:[0,0,1] neg_hi:[0,0,1]\n"
"v_pk_fma_f32 v[44:45], v[44:45], v[222:223], v[222:223] op_sel:[0,0,1] op_sel_hi:[1,0,1] neg_lo:[0,0,1] neg_hi:[0,0,1]\n"
"v_pk_fma_f32 v[46:47], v[46:47], v[222:223], v[222:223] op_sel:[0,0,1] op_sel_hi:[1,0,1] neg_lo:[0,0,1] neg_hi:[0,0,1]\n"
"v_pk_fma_f32 v[48:49], v[48:49], v[222:223], v[222:223] op_sel:[0,0,1] op_sel_hi:[1,0,1] neg_lo:[0,0,1] neg_hi:[0,0,1]\n"
"v_pk_fma_f32 v[50:51], v[50:51], v[222:223], v[222:223] op_sel:[0,0,1] op_sel_hi:[1,0,1] neg_lo:[0,0,1] neg_hi:[0,0,1]\n"
"v_pk_fma_f32 v[52:53], v[52:53], v[222:223], v[222:223] op_sel:[0,0,1] op_sel_hi:[1,0,1] neg_lo:[0,0,1] neg_hi:[0,0,1]\n"
"v_pk_fma_f32 v[54:55], v[54:55], v[222:223], v[222:223] op_sel:[0,0,1] op_sel_hi:[1,0,1] neg_lo:[0,0,1] neg_hi:[0,0,1]\n"
"v_pk_fma_f32 v[56:57], v[56:57], v[222:223], v[222:223] op_sel:[0,0,1] op_sel_hi:[1,0,1] neg_lo:[0,0,1] neg_hi:[0,0,1]\n"
"v_pk_fma_f32 v[58:59], v[58:59], v[222:223], v[222:223] op_sel:[0,0,1] op_sel_hi:[1,0,1] neg_lo:[0,0,1] neg_hi:[0,0,1]\n"
"v_pk_fma_f32 v[60:61], v[60:61], v[222:223], v[222:223] op_sel:[0,0,1] op_sel_hi:[1,0,1] neg_lo:[0,0,1] neg_hi:[0,0,1]\n"
"v_pk_fma_f32 v[62:63], v[62:63], v[222:223], v[222:223] op_sel:[0,0,1] op_sel_hi:[1,0,1] neg_lo:[0,0,1] neg_hi:[0,0,1]\n"
"s_cmp_lg_u64 vcc, 0\n"
"s_addc_u32 s55, s55, s55\n"
"v_mfma_f32_32x32x16_bf16 v[16:31], a[224:227], a[8:11], 0\n"
"v_cvt_pk_bf16_f32 v32, v32, v33\n"
"v_cvt_pk_bf16_f32 v33, v34, v35\n"
"v_cvt_pk_bf16_f32 v34, v36, v37\n"
"v_cvt_pk_bf16_f32 v35, v38, v39\n"
"v_mfma_f32_32x32x16_bf16 v[16:31], a[228:231], a[8:11], v[16:31]\n"
"ds_write_b128 v160, v[32:35] offset:0\n"
"v_mfma_f32_32x32x16_bf16 v[16:31], a[232:235], a[12:15], v[16:31]\n"
"v_cvt_pk_bf16_f32 v36, v40, v41\n"
"v_cvt_pk_bf16_f32 v37, v42, v43\n"
"v_cvt_pk_bf16_f32 v38, v44, v45\n"
"v_cvt_pk_bf16_f32 v39, v46, v47\n"
"v_mfma_f32_32x32x16_bf16 v[16:31], a[236:239], a[12:15], v[16:31]\n"
"ds_write_b128 v160, v[36:39] offset:64\n"
"v_mfma_f32_32x32x16_bf16 v[0:15], a[224:227], a[0:3], 0\n"
"v_cvt_pk_bf16_f32 v40, v48, v49\n"
"v_cvt_pk_bf16_f32 v41, v50, v51\n"
"v_cvt_pk_bf16_f32 v42, v52, v53\n"
"v_cvt_pk_bf16_f32 v43, v54, v55\n"
"v_mfma_f32_32x32x16_bf16 v[0:15], a[228:231], a[0:3], v[0:15]\n"
"ds_write_b128 v160, v[40:43] offset:128\n"
"v_mfma_f32_32x32x16_bf16 v[0:15], a[232:235], a[4:7], v[0:15]\n"
"v_cvt_pk_bf16_f32 v44, v56, v57\n"
"v_cvt_pk_bf16_f32 v45, v58, v59\n"
"v_cvt_pk_bf16_f32 v46, v60, v61\n"
"v_cvt_pk_bf16_f32 v47, v62, v63\n"
"v_mfma_f32_32x32x16_bf16 v[0:15], a[236:239], a[4:7], v[0:15]\n"
"ds_write_b128 v160, v[44:47] offset:192\n"
"v_mfma_f32_16x16x32_bf16 v[200:203], a[96:99], a[48:51], v[228:231]\n"
"ds_read_b64_tr_b16 a[196:197], v248 offset:57344\n"
"v_add_f32 v242, v242, v196\n"
"v_mfma_f32_16x16x32_bf16 v[200:203], a[100:103], a[52:55], v[200:203]\n"
"buffer_load_dwordx4 v253, s[44:47], s59 offen offset:2048 lds\n"
"v_exp_f32 v197, v197\n"
"v_mfma_f32_16x16x32_bf16 v[200:203], a[104:107], a[56:59], v[200:203]\n"
"s_mov_b32 m0, 17408\n"
"v_add_f32 v242, v242, v197\n"
"v_cvt_pk_bf16_f32 v210, v196, v197\n"
"v_mfma_f32_16x16x32_bf16 v[200:203], a[108:111], a[60:63], v[200:203]\n"
"s_add_u32 s59, s56, 128\n"
"buffer_load_dwordx4 v252, s[36:39], s59 offen lds\n"
"v_exp_f32 v198, v198\n"
"v_mfma_f32_16x16x32_bf16 v[204:207], a[112:115], a[48:51], v[228:231]\n"
"ds_read_b64_tr_b16 a[198:199], v248 offset:61440\n"
"v_add_f32 v242, v242, v198\n"
"v_mfma_f32_16x16x32_bf16 v[204:207], a[116:119], a[52:55], v[204:207]\n"
"v_exp_f32 v199, v199\n"
"v_mfma_f32_16x16x32_bf16 v[204:207], a[120:123], a[56:59], v[204:207]\n"
"v_add_f32 v242, v242, v199\n"
"v_cvt_pk_bf16_f32 v211, v198, v199\n"
"v_mfma_f32_16x16x32_bf16 v[204:207], a[124:127], a[60:63], v[204:207]\n"
"buffer_load_dwordx4 v253, s[36:39], s59 offen offset:2048 lds\n"
"v_exp_f32 v200, v200\n"
"v_mfma_f32_16x16x32_bf16 v[64:67], a[160:163], v[208:211], v[64:67]\n"
"s_mov_b32 m0, 20480\n"
"ds_read_b64_tr_b16 a[200:201], v247 offset:57856\n"
"v_add_f32 v243, v243, v200\n"
"v_mfma_f32_16x16x32_bf16 v[68:71], a[164:167], v[208:211], v[68:71]\n"
"s_add_u32 s59, s56, 4096\n"
"buffer_load_dwordx4 v252, s[36:39], s59 offen lds\n"
"v_exp_f32 v201, v201\n"
"v_mfma_f32_16x16x32_bf16 v[72:75], a[168:171], v[208:211], v[72:75]\n"
"v_add_f32 v243, v243, v201\n"
"v_cvt_pk_bf16_f32 v212, v200, v201\n"
"v_mfma_f32_16x16x32_bf16 v[76:79], a[172:175], v[208:211], v[76:79]\n"
"buffer_load_dwordx4 v253, s[36:39], s59 offen offset:2048 lds\n"
"v_exp_f32 v202, v202\n"
"v_mfma_f32_16x16x32_bf16 v[80:83], a[176:179], v[208:211], v[80:83]\n"
"ds_read_b64_tr_b16 a[202:203], v247 offset:61952\n"
"v_add_f32 v243, v243, v202\n"
"v_mfma_f32_16x16x32_bf16 v[84:87], a[180:183], v[208:211], v[84:87]\n"
"v_exp_f32 v203, v203\n"
"v_mfma_f32_16x16x32_bf16 v[88:91], a[184:187], v[208:211], v[88:91]\n"
"s_mov_b32 m0, 21504\n"
"v_add_f32 v243, v243, v203\n"
"v_cvt_pk_bf16_f32 v213, v202, v203\n"
"v_mfma_f32_16x16x32_bf16 v[92:95], a[188:191], v[208:211], v[92:95]\n"
"s_add_u32 s59, s56, 4224\n"
"buffer_load_dwordx4 v252, s[36:39], s59 offen lds\n"
"v_exp_f32 v204, v204\n"
"v_mfma_f32_16x16x4_f32 v[224:227], v250, v242, 0\n"
"s_waitcnt vmcnt(29)\n"
"ds_read_b128 a[32:35], v254 offset:8192\n"
"ds_read_b128 a[36:39], v254 offset:8704\n"
"ds_read_b128 a[40:43], v254 offset:9216\n"
"ds_read_b128 a[44:47], v254 offset:9728\n"
"v_cvt_pk_bf16_f32 v0, v0, v1\n"
"v_cvt_pk_bf16_f32 v2, v2, v3\n"
"v_cvt_pk_bf16_f32 v4, v4, v5\n"
"v_cvt_pk_bf16_f32 v6, v6, v7\n"
"v_accvgpr_write_b32 a0, v0\n"
"v_accvgpr_write_b32 a1, v2\n"
"v_accvgpr_write_b32 a2, v4\n"
"v_accvgpr_write_b32 a3, v6\n"
"v_rcp_f32 v226, v224\n"
"v_med3_f32 v225, v224, v251, s63\n"
"v_cmp_neq_f32 vcc, v225, v224\n"
"v_mul_f32 v227, s98, v226\n"
"v_pk_fma_f32 v[64:65], v[64:65], v[226:227], v[226:227] op_sel:[0,0,1] op_sel_hi:[1,0,1] neg_lo:[0,0,1] neg_hi:[0,0,1]\n"
"v_pk_fma_f32 v[66:67], v[66:67], v[226:227], v[226:227] op_sel:[0,0,1] op_sel_hi:[1,0,1] neg_lo:[0,0,1] neg_hi:[0,0,1]\n"
"v_pk_fma_f32 v[68:69], v[68:69], v[226:227], v[226:227] op_sel:[0,0,1] op_sel_hi:[1,0,1] neg_lo:[0,0,1] neg_hi:[0,0,1]\n"
"v_pk_fma_f32 v[70:71], v[70:71], v[226:227], v[226:227] op_sel:[0,0,1] op_sel_hi:[1,0,1] neg_lo:[0,0,1] neg_hi:[0,0,1]\n"
"v_pk_fma_f32 v[72:73], v[72:73], v[226:227], v[226:227] op_sel:[0,0,1] op_sel_hi:[1,0,1] neg_lo:[0,0,1] neg_hi:[0,0,1]\n"
"v_pk_fma_f32 v[74:75], v[74:75], v[226:227], v[226:227] op_sel:[0,0,1] op_sel_hi:[1,0,1] neg_lo:[0,0,1] neg_hi:[0,0,1]\n"
"v_pk_fma_f32 v[76:77], v[76:77], v[226:227], v[226:227] op_sel:[0,0,1] op_sel_hi:[1,0,1] neg_lo:[0,0,1] neg_hi:[0,0,1]\n"
"v_pk_fma_f32 v[78:79], v[78:79], v[226:227], v[226:227] op_sel:[0,0,1] op_sel_hi:[1,0,1] neg_lo:[0,0,1] neg_hi:[0,0,1]\n"
"v_pk_fma_f32 v[80:81], v[80:81], v[226:227], v[226:227] op_sel:[0,0,1] op_sel_hi:[1,0,1] neg_lo:[0,0,1] neg_hi:[0,0,1]\n"
"v_pk_fma_f32 v[82:83], v[82:83], v[226:227], v[226:227] op_sel:[0,0,1] op_sel_hi:[1,0,1] neg_lo:[0,0,1] neg_hi:[0,0,1]\n"
"v_pk_fma_f32 v[84:85], v[84:85], v[226:227], v[226:227] op_sel:[0,0,1] op_sel_hi:[1,0,1] neg_lo:[0,0,1] neg_hi:[0,0,1]\n"
"v_pk_fma_f32 v[86:87], v[86:87], v[226:227], v[226:227] op_sel:[0,0,1] op_sel_hi:[1,0,1] neg_lo:[0,0,1] neg_hi:[0,0,1]\n"
"v_pk_fma_f32 v[88:89], v[88:89], v[226:227], v[226:227] op_sel:[0,0,1] op_sel_hi:[1,0,1] neg_lo:[0,0,1] neg_hi:[0,0,1]\n"
"v_pk_fma_f32 v[90:91], v[90:91], v[226:227], v[226:227] op_sel:[0,0,1] op_sel_hi:[1,0,1] neg_lo:[0,0,1] neg_hi:[0,0,1]\n"
"v_pk_fma_f32 v[92:93], v[92:93], v[226:227], v[226:227] op_sel:[0,0,1] op_sel_hi:[1,0,1] neg_lo:[0,0,1] neg_hi:[0,0,1]\n"
"v_pk_fma_f32 v[94:95], v[94:95], v[226:227], v[226:227] op_sel:[0,0,1] op_sel_hi:[1,0,1] neg_lo:[0,0,1] neg_hi:[0,0,1]\n"
"s_cmp_lg_u64 vcc, 0\n"
"s_addc_u32 s55, s55, s55\n"
"v_mfma_f32_32x32x16_bf16 v[48:63], a[224:227], a[24:27], 0\n"
"v_cvt_pk_bf16_f32 v64, v64, v65\n"
"v_cvt_pk_bf16_f32 v65, v66, v67\n"
"v_cvt_pk_bf16_f32 v66, v68, v69\n"
"v_cvt_pk_bf16_f32 v67, v70, v71\n"
"v_cvt_pk_bf16_f32 v8, v8, v9\n"
"v_cvt_pk_bf16_f32 v10, v10, v11\n"
"v_mfma_f32_32x32x16_bf16 v[48:63], a[228:231], a[24:27], v[48:63]\n"
"ds_write_b128 v160, v[64:67] offset:4352\n"
"v_cvt_pk_bf16_f32 v12, v12, v13\n"
"v_cvt_pk_bf16_f32 v14, v14, v15\n"
"v_accvgpr_write_b32 a4, v8\n"
"v_accvgpr_write_b32 a5, v10\n"
"v_mfma_f32_32x32x16_bf16 v[48:63], a[232:235], a[28:31], v[48:63]\n"
"v_cvt_pk_bf16_f32 v68, v72, v73\n"
"v_cvt_pk_bf16_f32 v69, v74, v75\n"
"v_cvt_pk_bf16_f32 v70, v76, v77\n"
"v_cvt_pk_bf16_f32 v71, v78, v79\n"
"v_accvgpr_write_b32 a6, v12\n"
"v_accvgpr_write_b32 a7, v14\n"
"v_mfma_f32_32x32x16_bf16 v[48:63], a[236:239], a[28:31], v[48:63]\n"
"ds_write_b128 v160, v[68:71] offset:4416\n"
"v_cvt_pk_bf16_f32 v16, v16, v17\n"
"v_cvt_pk_bf16_f32 v18, v18, v19\n"
"v_cvt_pk_bf16_f32 v20, v20, v21\n"
"v_cvt_pk_bf16_f32 v22, v22, v23\n"
"v_mfma_f32_32x32x16_bf16 v[32:47], a[224:227], a[16:19], 0\n"
"v_cvt_pk_bf16_f32 v72, v80, v81\n"
"v_cvt_pk_bf16_f32 v73, v82, v83\n"
"v_cvt_pk_bf16_f32 v74, v84, v85\n"
"v_cvt_pk_bf16_f32 v75, v86, v87\n"
"v_accvgpr_write_b32 a8, v16\n"
"v_accvgpr_write_b32 a9, v18\n"
"v_mfma_f32_32x32x16_bf16 v[32:47], a[228:231], a[16:19], v[32:47]\n"
"ds_write_b128 v160, v[72:75] offset:4480\n"
"v_accvgpr_write_b32 a10, v20\n"
"v_accvgpr_write_b32 a11, v22\n"
"v_cvt_pk_bf16_f32 v24, v24, v25\n"
"v_cvt_pk_bf16_f32 v26, v26, v27\n"
"v_mfma_f32_32x32x16_bf16 v[32:47], a[232:235], a[20:23], v[32:47]\n"
"v_cvt_pk_bf16_f32 v76, v88, v89\n"
"v_cvt_pk_bf16_f32 v77, v90, v91\n"
"v_cvt_pk_bf16_f32 v78, v92, v93\n"
"v_cvt_pk_bf16_f32 v79, v94, v95\n"
"v_cvt_pk_bf16_f32 v28, v28, v29\n"
"v_cvt_pk_bf16_f32 v30, v30, v31\n"
"v_mfma_f32_32x32x16_bf16 v[32:47], a[236:239], a[20:23], v[32:47]\n"
"ds_write_b128 v160, v[76:79] offset:4544\n"
"v_accvgpr_write_b32 a12, v24\n"
"v_accvgpr_write_b32 a13, v26\n"
"v_accvgpr_write_b32 a14, v28\n"
"v_accvgpr_write_b32 a15, v30\n"
"v_mfma_f32_16x16x32_bf16 v[216:219], a[240:243], a[0:3], 0\n"
"v_mfma_f32_16x16x32_bf16 v[216:219], a[244:247], a[4:7], v[216:219]\n"
"v_mfma_f32_16x16x32_bf16 v[216:219], a[248:251], a[8:11], v[216:219]\n"
"v_mfma_f32_16x16x32_bf16 v[216:219], a[252:255], a[12:15], v[216:219]\n"
"s_waitcnt lgkmcnt(2)\n"
"v_mfma_f32_16x16x32_bf16 v[0:3], a[192:195], a[192:195], 0\n"
"v_mfma_f32_16x16x32_bf16 v[0:3], a[196:199], a[196:199], v[0:3]\n"
"v_mfma_f32_16x16x32_bf16 v[192:195], a[96:99], a[64:67], v[232:235]\n"
"ds_read_b64_tr_b16 a[204:205], v248 offset:57856\n"
"v_add_f32 v243, v243, v204\n"
"v_mfma_f32_16x16x32_bf16 v[192:195], a[100:103], a[68:71], v[192:195]\n"
"buffer_load_dwordx4 v253, s[36:39], s59 offen offset:2048 lds\n"
"v_exp_f32 v205, v205\n"
"v_mfma_f32_16x16x32_bf16 v[192:195], a[104:107], a[72:75], v[192:195]\n"
"s_mov_b32 m0, 49152\n"
"ds_read_b64_tr_b16 a[206:207], v248 offset:61952\n"
"v_add_f32 v243, v243, v205\n"
"v_cvt_pk_bf16_f32 v214, v204, v205\n"
"v_mfma_f32_16x16x32_bf16 v[192:195], a[108:111], a[76:79], v[192:195]\n"
"s_add_u32 s59, s56, 0\n"
"buffer_load_dwordx4 v252, s[40:43], s59 offen lds\n"
"v_exp_f32 v206, v206\n"
"v_mfma_f32_16x16x32_bf16 v[196:199], a[112:115], a[64:67], v[232:235]\n"
"ds_read_b64_tr_b16 a[208:209], v247 offset:58368\n"
"v_add_f32 v243, v243, v206\n"
"v_mfma_f32_16x16x32_bf16 v[196:199], a[116:119], a[68:71], v[196:199]\n"
"buffer_load_dwordx4 v252, s[40:43], s59 offen offset:2048 lds\n"
"v_exp_f32 v207, v207\n"
"v_mfma_f32_16x16x32_bf16 v[196:199], a[120:123], a[72:75], v[196:199]\n"
"v_add_f32 v243, v243, v207\n"
"v_cvt_pk_bf16_f32 v215, v206, v207\n"
"v_mfma_f32_16x16x32_bf16 v[196:199], a[124:127], a[76:79], v[196:199]\n"
"v_exp_f32 v192, v192\n"
"v_mfma_f32_16x16x32_bf16 v[96:99], a[160:163], v[212:215], v[96:99]\n"
"s_mov_b32 m0, 50176\n"
"ds_read_b64_tr_b16 a[210:211], v247 offset:62464\n"
"v_add_f32 v244, v244, v192\n"
"v_mfma_f32_16x16x32_bf16 v[100:103], a[164:167], v[212:215], v[100:103]\n"
"s_add_u32 s59, s56, 128\n"
"buffer_load_dwordx4 v252, s[40:43], s59 offen lds\n"
"v_exp_f32 v193, v193\n"
"v_mfma_f32_16x16x32_bf16 v[104:107], a[168:171], v[212:215], v[104:107]\n"
"v_add_f32 v244, v244, v193\n"
"v_cvt_pk_bf16_f32 v208, v192, v193\n"
"v_mfma_f32_16x16x32_bf16 v[108:111], a[172:175], v[212:215], v[108:111]\n"
"buffer_load_dwordx4 v252, s[40:43], s59 offen offset:2048 lds\n"
"v_exp_f32 v194, v194\n"
"v_mfma_f32_16x16x32_bf16 v[112:115], a[176:179], v[212:215], v[112:115]\n"
"s_mov_b32 m0, 53248\n"
"ds_read_b64_tr_b16 a[212:213], v248 offset:58368\n"
"v_add_f32 v244, v244, v194\n"
"v_mfma_f32_16x16x32_bf16 v[116:119], a[180:183], v[212:215], v[116:119]\n"
"s_add_u32 s59, s56, 4096\n"
"buffer_load_dwordx4 v252, s[40:43], s59 offen lds\n"
"v_exp_f32 v195, v195\n"
"v_mfma_f32_16x16x32_bf16 v[120:123], a[184:187], v[212:215], v[120:123]\n"
"v_add_f32 v244, v244, v195\n"
"v_cvt_pk_bf16_f32 v209, v194, v195\n"
"v_mfma_f32_16x16x32_bf16 v[124:127], a[188:191], v[212:215], v[124:127]\n"
"v_exp_f32 v196, v196\n"
"v_mfma_f32_16x16x4_f32 v[228:231], v250, v243, 0\n"
"s_waitcnt vmcnt(15)\n"
"ds_read_b128 a[48:51], v254 offset:12288\n"
"ds_read_b128 a[52:55], v254 offset:12800\n"
"ds_read_b128 a[56:59], v254 offset:13312\n"
"ds_read_b128 a[60:63], v254 offset:13824\n"
"v_cvt_pk_bf16_f32 v32, v32, v33\n"
"v_cvt_pk_bf16_f32 v34, v34, v35\n"
"v_cvt_pk_bf16_f32 v36, v36, v37\n"
"v_cvt_pk_bf16_f32 v38, v38, v39\n"
"v_accvgpr_write_b32 a16, v32\n"
"v_accvgpr_write_b32 a17, v34\n"
"v_accvgpr_write_b32 a18, v36\n"
"v_accvgpr_write_b32 a19, v38\n"
"v_readlane_b32 s99, v0, 0\n"
"v_readlane_b32 s72, v1, 21\n"
"v_readlane_b32 s73, v2, 42\n"
"v_readlane_b32 s60, v3, 63\n"
"v_rcp_f32 v230, v228\n"
"v_med3_f32 v229, v228, v251, s63\n"
"v_cmp_neq_f32 vcc, v229, v228\n"
"v_mul_f32 v231, s98, v230\n"
"v_pk_fma_f32 v[96:97], v[96:97], v[230:231], v[230:231] op_sel:[0,0,1] op_sel_hi:[1,0,1] neg_lo:[0,0,1] neg_hi:[0,0,1]\n"
"v_pk_fma_f32 v[98:99], v[98:99], v[230:231], v[230:231] op_sel:[0,0,1] op_sel_hi:[1,0,1] neg_lo:[0,0,1] neg_hi:[0,0,1]\n"
"v_pk_fma_f32 v[100:101], v[100:101], v[230:231], v[230:231] op_sel:[0,0,1] op_sel_hi:[1,0,1] neg_lo:[0,0,1] neg_hi:[0,0,1]\n"
"v_pk_fma_f32 v[102:103], v[102:103], v[230:231], v[230:231] op_sel:[0,0,1] op_sel_hi:[1,0,1] neg_lo:[0,0,1] neg_hi:[0,0,1]\n"
"v_pk_fma_f32 v[104:105], v[104:105], v[230:231], v[230:231] op_sel:[0,0,1] op_sel_hi:[1,0,1] neg_lo:[0,0,1] neg_hi:[0,0,1]\n"
"v_pk_fma_f32 v[106:107], v[106:107], v[230:231], v[230:231] op_sel:[0,0,1] op_sel_hi:[1,0,1] neg_lo:[0,0,1] neg_hi:[0,0,1]\n"
"v_pk_fma_f32 v[108:109], v[108:109], v[230:231], v[230:231] op_sel:[0,0,1] op_sel_hi:[1,0,1] neg_lo:[0,0,1] neg_hi:[0,0,1]\n"
"v_pk_fma_f32 v[110:111], v[110:111], v[230:231], v[230:231] op_sel:[0,0,1] op_sel_hi:[1,0,1] neg_lo:[0,0,1] neg_hi:[0,0,1]\n"
"v_pk_fma_f32 v[112:113], v[112:113], v[230:231], v[230:231] op_sel:[0,0,1] op_sel_hi:[1,0,1] neg_lo:[0,0,1] neg_hi:[0,0,1]\n"
"v_pk_fma_f32 v[114:115], v[114:115], v[230:231], v[230:231] op_sel:[0,0,1] op_sel_hi:[1,0,1] neg_lo:[0,0,1] neg_hi:[0,0,1]\n"
"v_pk_fma_f32 v[116:117], v[116:117], v[230:231], v[230:231] op_sel:[0,0,1] op_sel_hi:[1,0,1] neg_lo:[0,0,1] neg_hi:[0,0,1]\n"
"v_pk_fma_f32 v[118:119], v[118:119], v[230:231], v[230:231] op_sel:[0,0,1] op_sel_hi:[1,0,1] neg_lo:[0,0,1] neg_hi:[0,0,1]\n"
"v_pk_fma_f32 v[120:121], v[120:121], v[230:231], v[230:231] op_sel:[0,0,1] op_sel_hi:[1,0,1] neg_lo:[0,0,1] neg_hi:[0,0,1]\n"
"v_pk_fma_f32 v[122:123], v[122:123], v[230:231], v[230:231] op_sel:[0,0,1] op_sel_hi:[1,0,1] neg_lo:[0,0,1] neg_hi:[0,0,1]\n"
"v_pk_fma_f32 v[124:125], v[124:125], v[230:231], v[230:231] op_sel:[0,0,1] op_sel_hi:[1,0,1] neg_lo:[0,0,1] neg_hi:[0,0,1]\n"
"v_pk_fma_f32 v[126:127], v[126:127], v[230:231], v[230:231] op_sel:[0,0,1] op_sel_hi:[1,0,1] neg_lo:[0,0,1] neg_hi:[0,0,1]\n"
"s_cmp_lg_u64 vcc, 0\n"
"s_addc_u32 s55, s55, s55\n"
"v_mfma_f32_32x32x16_bf16 v[80:95], a[224:227], a[40:43], 0\n"
"s_min_u32 s99, s99, s72\n"
"v_cvt_pk_bf16_f32 v96, v96, v97\n"
"v_cvt_pk_bf16_f32 v97, v98, v99\n"
"v_cvt_pk_bf16_f32 v98, v100, v101\n"
"v_cvt_pk_bf16_f32 v99, v102, v103\n"
"v_cvt_pk_bf16_f32 v40, v40, v41\n"
"v_cvt_pk_bf16_f32 v42, v42, v43\n"
"v_mfma_f32_32x32x16_bf16 v[80:95], a[228:231], a[40:43], v[80:95]\n"
"s_min_u32 s73, s73, s60\n"
"ds_write_b128 v160, v[96:99] offset:8704\n"
"v_cvt_pk_bf16_f32 v44, v44, v45\n"
"v_cvt_pk_bf16_f32 v46, v46, v47\n"
"v_accvgpr_write_b32 a20, v40\n"
"v_accvgpr_write_b32 a21, v42\n"
"v_mfma_f32_32x32x16_bf16 v[80:95], a[232:235], a[44:47], v[80:95]\n"
"s_min_u32 s99, s99, s73\n"
"v_cvt_pk_bf16_f32 v100, v104, v105\n"
"v_cvt_pk_bf16_f32 v101, v106, v107\n"
"v_cvt_pk_bf16_f32 v102, v108, v109\n"
"v_cvt_pk_bf16_f32 v103, v110, v111\n"
"v_accvgpr_write_b32 a22, v44\n"
"v_accvgpr_write_b32 a23, v46\n"
"v_mfma_f32_32x32x16_bf16 v[80:95], a[236:239], a[44:47], v[80:95]\n"
"s_lshr_b32 s99, s99, 23\n"
"ds_write_b128 v160, v[100:103] offset:8768\n"
"v_cvt_pk_bf16_f32 v48, v48, v49\n"
"v_cvt_pk_bf16_f32 v50, v50, v51\n"
"v_cvt_pk_bf16_f32 v52, v52, v53\n"
"v_cvt_pk_bf16_f32 v54, v54, v55\n"
"v_mfma_f32_32x32x16_bf16 v[64:79], a[224:227], a[32:35], 0\n"
"s_add_u32 s72, s99, 45\n"
"v_cvt_pk_bf16_f32 v104, v112, v113\n"
"v_cvt_pk_bf16_f32 v105, v114, v115\n"
"v_cvt_pk_bf16_f32 v106, v116, v117\n"
"v_cvt_pk_bf16_f32 v107, v118, v119\n"
"v_accvgpr_write_b32 a24, v48\n"
"v_accvgpr_write_b32 a25, v50\n"
"v_mfma_f32_32x32x16_bf16 v[64:79], a[228:231], a[32:35], v[64:79]\n"
"s_lshr_b32 s72, s72, 1\n"
"ds_write_b128 v160, v[104:107] offset:8832\n"
"v_accvgpr_write_b32 a26, v52\n"
"v_accvgpr_write_b32 a27, v54\n"
"v_cvt_pk_bf16_f32 v56, v56, v57\n"
"v_cvt_pk_bf16_f32 v58, v58, v59\n"
"v_mfma_f32_32x32x16_bf16 v[64:79], a[232:235], a[36:39], v[64:79]\n"
"s_lshl_b32 s72, s72, 23\n"
"v_cvt_pk_bf16_f32 v108, v120, v121\n"
"v_cvt_pk_bf16_f32 v109, v122, v123\n"
"v_cvt_pk_bf16_f32 v110, v124, v125\n"
"v_cvt_pk_bf16_f32 v111, v126, v127\n"
"v_cvt_pk_bf16_f32 v60, v60, v61\n"
"v_cvt_pk_bf16_f32 v62, v62, v63\n"
"v_mfma_f32_32x32x16_bf16 v[64:79], a[236:239], a[36:39], v[64:79]\n"
"s_or_b32 s72, s72, 4194304\n"
"s_sub_u32 s99, s99, 33\n"
"s_mov_b32 s73, 671088640\n"
"s_cmp_lt_u32 s99, 200\n"
"s_cselect_b32 s99, s72, 0\n"
"s_cselect_b32 s73, 1568669696, s73\n"
"ds_write_b128 v160, v[108:111] offset:8896\n"
"v_accvgpr_write_b32 a28, v56\n"
"v_accvgpr_write_b32 a29, v58\n"
"v_accvgpr_write_b32 a30, v60\n"
"v_accvgpr_write_b32 a31, v62\n"
"v_mfma_f32_16x16x32_bf16 v[220:223], a[240:243], a[16:19], 0\n"
"v_sub_f32 v216, s71, v216\n"
"v_mfma_f32_16x16x32_bf16 v[220:223], a[244:247], a[20:23], v[220:223]\n"
"v_sub_f32 v217, s71, v217\n"
"v_mfma_f32_16x16x32_bf16 v[220:223], a[248:251], a[24:27], v[220:223]\n"
"v_sub_f32 v218, s71, v218\n"
"v_mfma_f32_16x16x32_bf16 v[220:223], a[252:255], a[28:31], v[220:223]\n"
"v_sub_f32 v219, s71, v219\n"
"s_nop 1\n"
"v_mfma_f32_16x16x32_bf16 v[200:203], a[128:131], a[0:3], v[216:219]\n"
"ds_read_b64_tr_b16 a[214:215], v248 offset:62464\n"
"v_add_f32 v244, v244, v196\n"
"v_mfma_f32_16x16x32_bf16 v[200:203], a[132:135], a[4:7], v[200:203]\n"
"buffer_load_dwordx4 v252, s[40:43], s59 offen offset:2048 lds\n"
"v_exp_f32 v197, v197\n"
"v_mfma_f32_16x16x32_bf16 v[200:203], a[136:139], a[8:11], v[200:203]\n"
"s_mov_b32 m0, 54272\n"
"v_add_f32 v244, v244, v197\n"
"v_cvt_pk_bf16_f32 v210, v196, v197\n"
"v_mfma_f32_16x16x32_bf16 v[200:203], a[140:143], a[12:15], v[200:203]\n"
"s_add_u32 s59, s56, 4224\n"
"buffer_load_dwordx4 v252, s[40:43], s59 offen lds\n"
"v_exp_f32 v198, v198\n"
"v_mfma_f32_16x16x32_bf16 v[204:207], a[144:147], a[0:3], v[216:219]\n"
"ds_read_b64_tr_b16 a[216:217], v247 offset:58880\n"
"v_add_f32 v244, v244, v198\n"
"v_mfma_f32_16x16x32_bf16 v[204:207], a[148:151], a[4:7], v[204:207]\n"
"buffer_load_dwordx4 v252, s[40:43], s59 offen offset:2048 lds\n"
"v_exp_f32 v199, v199\n"
"v_mfma_f32_16x16x32_bf16 v[204:207], a[152:155], a[8:11], v[204:207]\n"
"s_mov_b32 s58, 159744\n"
"s_mov_b32 s69, 0\n"
"s_mov_b32 s46, 0\n"
"ds_read_b64_tr_b16 a[218:219], v247 offset:62976\n"
"v_add_f32 v244, v244, v199\n"
"v_cvt_pk_bf16_f32 v211, v198, v199\n"
"v_mfma_f32_16x16x32_bf16 v[204:207], a[156:159], a[12:15], v[204:207]\n"
"v_exp_f32 v200, v200\n"
"v_mfma_f32_16x16x32_bf16 v[128:131], a[160:163], v[208:211], v[128:131]\n"
"ds_read_b64_tr_b16 a[220:221], v248 offset:58880\n"
"v_mov_b32 v28, s99\n"
"v_mov_b32 v240, v200\n"
"v_mfma_f32_16x16x32_bf16 v[132:135], a[164:167], v[208:211], v[132:135]\n"
"v_exp_f32 v201, v201\n"
"v_mfma_f32_16x16x32_bf16 v[136:139], a[168:171], v[208:211], v[136:139]\n"
"v_add_f32 v240, v240, v201\n"
"v_cvt_pk_bf16_f32 v212, v200, v201\n"
"v_mfma_f32_16x16x32_bf16 v[140:143], a[172:175], v[208:211], v[140:143]\n"
"v_exp_f32 v202, v202\n"
"v_mfma_f32_16x16x32_bf16 v[144:147], a[176:179], v[208:211], v[144:147]\n"
"ds_read_b64_tr_b16 a[222:223], v248 offset:62976\n"
"v_mov_b32 v29, s99\n"
"v_add_f32 v240, v240, v202\n"
"v_mfma_f32_16x16x32_bf16 v[148:151], a[180:183], v[208:211], v[148:151]\n"
"v_exp_f32 v203, v203\n"
"v_mfma_f32_16x16x32_bf16 v[152:155], a[184:187], v[208:211], v[152:155]\n"
"v_add_f32 v240, v240, v203\n"
"v_cvt_pk_bf16_f32 v213, v202, v203\n"
"v_mfma_f32_16x16x32_bf16 v[156:159], a[188:191], v[208:211], v[156:159]\n"
"v_exp_f32 v204, v204\n"
"v_mfma_f32_16x16x4_f32 v[232:235], v250, v244, 0\n"
"s_waitcnt vmcnt(14)\n"
"ds_read_b128 a[64:67], v254 offset:16384\n"
"ds_read_b128 a[68:71], v254 offset:16896\n"
"ds_read_b128 a[72:75], v254 offset:17408\n"
"ds_read_b128 a[76:79], v254 offset:17920\n"
"v_cvt_pk_bf16_f32 v64, v64, v65\n"
"v_cvt_pk_bf16_f32 v66, v66, v67\n"
"v_cvt_pk_bf16_f32 v68, v68, v69\n"
"v_cvt_pk_bf16_f32 v70, v70, v71\n"
"v_accvgpr_write_b32 a32, v64\n"
"v_accvgpr_write_b32 a33, v66\n"
"v_accvgpr_write_b32 a34, v68\n"
"v_accvgpr_write_b32 a35, v70\n"
"v_rcp_f32 v234, v232\n"
"v_med3_f32 v233, v232, v251, s63\n"
"v_cmp_neq_f32 vcc, v233, v232\n"
"v_mul_f32 v235, s98, v234\n"
"v_pk_fma_f32 v[128:129], v[128:129], v[234:235], v[234:235] op_sel:[0,0,1] op_sel_hi:[1,0,1] neg_lo:[0,0,1] neg_hi:[0,0,1]\n"
"v_pk_fma_f32 v[130:131], v[130:131], v[234:235], v[234:235] op_sel:[0,0,1] op_sel_hi:[1,0,1] neg_lo:[0,0,1] neg_hi:[0,0,1]\n"
"v_pk_fma_f32 v[132:133], v[132:133], v[234:235], v[234:235] op_sel:[0,0,1] op_sel_hi:[1,0,1] neg_lo:[0,0,1] neg_hi:[0,0,1]\n"
"v_pk_fma_f32 v[134:135], v[134:135], v[234:235], v[234:235] op_sel:[0,0,1] op_sel_hi:[1,0,1] neg_lo:[0,0,1] neg_hi:[0,0,1]\n"
"v_pk_fma_f32 v[136:137], v[136:137], v[234:235], v[234:235] op_sel:[0,0,1] op_sel_hi:[1,0,1] neg_lo:[0,0,1] neg_hi:[0,0,1]\n"
"v_pk_fma_f32 v[138:139], v[138:139], v[234:235], v[234:235] op_sel:[0,0,1] op_sel_hi:[1,0,1] neg_lo:[0,0,1] neg_hi:[0,0,1]\n"
"v_pk_fma_f32 v[140:141], v[140:141], v[234:235], v[234:235] op_sel:[0,0,1] op_sel_hi:[1,0,1] neg_lo:[0,0,1] neg_hi:[0,0,1]\n"
"v_pk_fma_f32 v[142:143], v[142:143], v[234:235], v[234:235] op_sel:[0,0,1] op_sel_hi:[1,0,1] neg_lo:[0,0,1] neg_hi:[0,0,1]\n"
"v_pk_fma_f32 v[144:145], v[144:145], v[234:235], v[234:235] op_sel:[0,0,1] op_sel_hi:[1,0,1] neg_lo:[0,0,1] neg_hi:[0,0,1]\n"
"v_pk_fma_f32 v[146:147], v[146:147], v[234:235], v[234:235] op_sel:[0,0,1] op_sel_hi:[1,0,1] neg_lo:[0,0,1] neg_hi:[0,0,1]\n"
"v_pk_fma_f32 v[148:149], v[148:149], v[234:235], v[234:235] op_sel:[0,0,1] op_sel_hi:[1,0,1] neg_lo:[0,0,1] neg_hi:[0,0,1]\n"
"v_pk_fma_f32 v[150:151], v[150:151], v[234:235], v[234:235] op_sel:[0,0,1] op_sel_hi:[1,0,1] neg_lo:[0,0,1] neg_hi:[0,0,1]\n"
"v_pk_fma_f32 v[152:153], v[152:153], v[234:235], v[234:235] op_sel:[0,0,1] op_sel_hi:[1,0,1] neg_lo:[0,0,1] neg_hi:[0,0,1]\n"
"v_pk_fma_f32 v[154:155], v[154:155], v[234:235], v[234:235] op_sel:[0,0,1] op_sel_hi:[1,0,1] neg_lo:[0,0,1] neg_hi:[0,0,1]\n"
"v_pk_fma_f32 v[156:157], v[156:157], v[234:235], v[234:235] op_sel:[0,0,1] op_sel_hi:[1,0,1] neg_lo:[0,0,1] neg_hi:[0,0,1]\n"
"v_pk_fma_f32 v[158:159], v[158:159], v[234:235], v[234:235] op_sel:[0,0,1] op_sel_hi:[1,0,1] neg_lo:[0,0,1] neg_hi:[0,0,1]\n"
"s_cmp_lg_u64 vcc, 0\n"
"s_addc_u32 s55, s55, s55\n"
"s_waitcnt lgkmcnt(0)\n"
"v_mfma_f32_32x32x16_bf16 v[112:127], a[224:227], a[56:59], 0\n"
"v_cvt_pk_bf16_f32 v128, v128, v129\n"
"v_cvt_pk_bf16_f32 v129, v130, v131\n"
"v_cvt_pk_bf16_f32 v130, v132, v133\n"
"v_cvt_pk_bf16_f32 v131, v134, v135\n"
"v_cvt_pk_bf16_f32 v72, v72, v73\n"
"v_cvt_pk_bf16_f32 v74, v74, v75\n"
"v_mfma_f32_32x32x16_bf16 v[112:127], a[228:231], a[56:59], v[112:127]\n"
"ds_write_b128 v160, v[128:131] offset:13056\n"
"v_cvt_pk_bf16_f32 v76, v76, v77\n"
"v_cvt_pk_bf16_f32 v78, v78, v79\n"
"v_accvgpr_write_b32 a36, v72\n"
"v_accvgpr_write_b32 a37, v74\n"
"v_mfma_f32_32x32x16_bf16 v[112:127], a[232:235], a[60:63], v[112:127]\n"
"v_cvt_pk_bf16_f32 v132, v136, v137\n"
"v_cvt_pk_bf16_f32 v133, v138, v139\n"
"v_cvt_pk_bf16_f32 v134, v140, v141\n"
"v_cvt_pk_bf16_f32 v135, v142, v143\n"
"v_accvgpr_write_b32 a38, v76\n"
"v_accvgpr_write_b32 a39, v78\n"
"v_mfma_f32_32x32x16_bf16 v[112:127], a[236:239], a[60:63], v[112:127]\n"
"ds_write_b128 v160, v[132:135] offset:13120\n"
"v_cvt_pk_bf16_f32 v80, v80, v81\n"
"v_cvt_pk_bf16_f32 v82, v82, v83\n"
"v_cvt_pk_bf16_f32 v84, v84, v85\n"
"v_cvt_pk_bf16_f32 v86, v86, v87\n"
"v_mfma_f32_32x32x16_bf16 v[96:111], a[224:227], a[48:51], 0\n"
"v_cvt_pk_bf16_f32 v136, v144, v145\n"
"v_cvt_pk_bf16_f32 v137, v146, v147\n"
"v_cvt_pk_bf16_f32 v138, v148, v149\n"
"v_cvt_pk_bf16_f32 v139, v150, v151\n"
"v_accvgpr_write_b32 a40, v80\n"
"v_accvgpr_write_b32 a41, v82\n"
"v_mfma_f32_32x32x16_bf16 v[96:111], a[228:231], a[48:51], v[96:111]\n"
"ds_write_b128 v160, v[136:139] offset:13184\n"
"v_accvgpr_write_b32 a42, v84\n"
"v_accvgpr_write_b32 a43, v86\n"
"v_cvt_pk_bf16_f32 v88, v88, v89\n"
"v_cvt_pk_bf16_f32 v90, v90, v91\n"
"v_mfma_f32_32x32x16_bf16 v[96:111], a[232:235], a[52:55], v[96:111]\n"
"v_cvt_pk_bf16_f32 v140, v152, v153\n"
"v_cvt_pk_bf16_f32 v141, v154, v155\n"
"v_cvt_pk_bf16_f32 v142, v156, v157\n"
"v_cvt_pk_bf16_f32 v143, v158, v159\n"
"v_cvt_pk_bf16_f32 v92, v92, v93\n"
"v_cvt_pk_bf16_f32 v94, v94, v95\n"
"v_mfma_f32_32x32x16_bf16 v[96:111], a[236:239], a[52:55], v[96:111]\n"
"ds_write_b128 v160, v[140:143] offset:13248\n"
"v_accvgpr_write_b32 a44, v88\n"
"v_accvgpr_write_b32 a45, v90\n"
"v_accvgpr_write_b32 a46, v92\n"
"v_accvgpr_write_b32 a47, v94\n"
"v_mfma_f32_16x16x32_bf16 v[224:227], a[240:243], a[32:35], 0\n"
"v_sub_f32 v220, s71, v220\n"
"v_mfma_f32_16x16x32_bf16 v[224:227], a[244:247], a[36:39], v[224:227]\n"
"v_sub_f32 v221, s71, v221\n"
"v_mfma_f32_16x16x32_bf16 v[224:227], a[248:251], a[40:43], v[224:227]\n"
"v_sub_f32 v222, s71, v222\n"
"v_mfma_f32_16x16x32_bf16 v[224:227], a[252:255], a[44:47], v[224:227]\n"
"v_sub_f32 v223, s71, v223\n"
"s_nop 1\n"
"v_mfma_f32_16x16x32_bf16 v[192:195], a[128:131], a[16:19], v[220:223]\n"
"ds_read_b128 a[96:99], v246 offset:0\n"
"v_mov_b32 v30, s99\n"
"v_add_f32 v240, v240, v204\n"
"v_mfma_f32_16x16x32_bf16 v[192:195], a[132:135], a[20:23], v[192:195]\n"
"v_exp_f32 v205, v205\n"
"v_mfma_f32_16x16x32_bf16 v[192:195], a[136:139], a[24:27], v[192:195]\n"
"v_add_f32 v240, v240, v205\n"
"v_cvt_pk_bf16_f32 v214, v204, v205\n"
"v_mfma_f32_16x16x32_bf16 v[192:195], a[140:143], a[28:31], v[192:195]\n"
"v_exp_f32 v206, v206\n"
"v_mfma_f32_16x16x32_bf16 v[196:199], a[144:147], a[16:19], v[220:223]\n"
"ds_read_b128 a[100:103], v246 offset:512\n"
"v_mov_b32 v31, s99\n"
"v_add_f32 v240, v240, v206\n"
"v_mfma_f32_16x16x32_bf16 v[196:199], a[148:151], a[20:23], v[196:199]\n"
"v_exp_f32 v207, v207\n"
"v_mfma_f32_16x16x32_bf16 v[196:199], a[152:155], a[24:27], v[196:199]\n"
"v_add_f32 v240, v240, v207\n"
"v_cvt_pk_bf16_f32 v215, v206, v207\n"
"v_mfma_f32_16x16x32_bf16 v[196:199], a[156:159], a[28:31], v[196:199]\n"
"s_barrier\n"
"v_exp_f32 v192, v192\n"
"v_mfma_f32_16x16x32_bf16 v[0:3], a[192:195], v[212:215], v[28:31]\n"
"s_add_u32 s56, s56, 8192\n"
"ds_read_b128 a[104:107], v246 offset:1024\n"
"v_mov_b32 v60, s99\n"
"v_mov_b32 v241, v192\n"
"v_mfma_f32_16x16x32_bf16 v[4:7], a[196:199], v[212:215], v[28:31]\n"
"v_exp_f32 v193, v193\n"
"v_mfma_f32_16x16x32_bf16 v[8:11], a[200:203], v[212:215], v[28:31]\n"
"s_mov_b32 m0, 24576\n"
"ds_read_b128 a[108:111], v246 offset:1536\n"
"v_add_f32 v241, v241, v193\n"
"v_cvt_pk_bf16_f32 v208, v192, v193\n"
"v_mfma_f32_16x16x32_bf16 v[12:15], a[204:207], v[212:215], v[28:31]\n"
"s_add_u32 s59, s56, 0\n"
"buffer_load_dwordx4 v252, s[36:39], s59 offen lds\n"
"v_exp_f32 v194, v194\n"
"v_mfma_f32_16x16x32_bf16 v[16:19], a[208:211], v[212:215], v[28:31]\n"
"ds_read_b128 a[112:115], v246 offset:4096\n"
"v_mov_b32 v61, s99\n"
"v_add_f32 v241, v241, v194\n"
"v_mfma_f32_16x16x32_bf16 v[20:23], a[212:215], v[212:215], v[28:31]\n"
"buffer_load_dwordx4 v253, s[36:39], s59 offen offset:2048 lds\n"
"v_exp_f32 v195, v195\n"
"v_mfma_f32_16x16x32_bf16 v[24:27], a[216:219], v[212:215], v[28:31]\n"
"v_add_f32 v241, v241, v195\n"
"v_cvt_pk_bf16_f32 v209, v194, v195\n"
"v_mfma_f32_16x16x32_bf16 v[28:31], a[220:223], v[212:215], v[28:31]\n"
"v_exp_f32 v196, v196\n"
"v_mfma_f32_32x32x16_bf16 v[144:159], a[224:227], a[72:75], 0\n"
"v_cvt_pk_bf16_f32 v96, v96, v97\n"
"v_cvt_pk_bf16_f32 v98, v98, v99\n"
"v_cvt_pk_bf16_f32 v100, v100, v101\n"
"v_cvt_pk_bf16_f32 v102, v102, v103\n"
"v_mfma_f32_32x32x16_bf16 v[144:159], a[228:231], a[72:75], v[144:159]\n"
"v_accvgpr_write_b32 a48, v96\n"
"v_accvgpr_write_b32 a49, v98\n"
"v_accvgpr_write_b32 a50, v100\n"
"v_accvgpr_write_b32 a51, v102\n"
"v_cvt_pk_bf16_f32 v104, v104, v105\n"
"v_mfma_f32_32x32x16_bf16 v[144:159], a[232:235], a[76:79], v[144:159]\n"
"v_cvt_pk_bf16_f32 v106, v106, v107\n"
"v_cvt_pk_bf16_f32 v108, v108, v109\n"
"v_cvt_pk_bf16_f32 v110, v110, v111\n"
"v_accvgpr_write_b32 a52, v104\n"
"v_mfma_f32_32x32x16_bf16 v[144:159], a[236:239], a[76:79], v[144:159]\n"
"v_accvgpr_write_b32 a53, v106\n"
"v_accvgpr_write_b32 a54, v108\n"
"v_accvgpr_write_b32 a55, v110\n"
"v_cvt_pk_bf16_f32 v112, v112, v113\n"
"v_cvt_pk_bf16_f32 v114, v114, v115\n"
"v_mfma_f32_32x32x16_bf16 v[128:143], a[224:227], a[64:67], 0\n"
"v_cvt_pk_bf16_f32 v116, v116, v117\n"
"v_cvt_pk_bf16_f32 v118, v118, v119\n"
"v_accvgpr_write_b32 a56, v112\n"
"v_accvgpr_write_b32 a57, v114\n"
"v_mfma_f32_32x32x16_bf16 v[128:143], a[228:231], a[64:67], v[128:143]\n"
"v_accvgpr_write_b32 a58, v116\n"
"v_accvgpr_write_b32 a59, v118\n"
"v_cvt_pk_bf16_f32 v120, v120, v121\n"
"v_cvt_pk_bf16_f32 v122, v122, v123\n"
"v_cvt_pk_bf16_f32 v124, v124, v125\n"
"v_mfma_f32_32x32x16_bf16 v[128:143], a[232:235], a[68:71], v[128:143]\n"
"v_cvt_pk_bf16_f32 v126, v126, v127\n"
"v_accvgpr_write_b32 a60, v120\n"
"v_accvgpr_write_b32 a61, v122\n"
"v_accvgpr_write_b32 a62, v124\n"
"v_mfma_f32_32x32x16_bf16 v[128:143], a[236:239], a[68:71], v[128:143]\n"
"v_accvgpr_write_b32 a63, v126\n"
"v_sub_f32 v224, s71, v224\n"
"v_sub_f32 v225, s71, v225\n"
"v_sub_f32 v226, s71, v226\n"
"v_sub_f32 v227, s71, v227\n"
"v_mfma_f32_16x16x32_bf16 v[228:231], a[240:243], a[48:51], 0\n"
"v_mfma_f32_16x16x32_bf16 v[228:231], a[244:247], a[52:55], v[228:231]\n"
"v_mfma_f32_16x16x32_bf16 v[228:231], a[248:251], a[56:59], v[228:231]\n"
"v_mfma_f32_16x16x32_bf16 v[228:231], a[252:255], a[60:63], v[228:231]\n"
"v_mfma_f32_16x16x32_bf16 v[200:203], a[128:131], a[32:35], v[224:227]\n"
"s_add_u32 m0, s58, 0\n"
"ds_read_b128 a[116:119], v246 offset:4608\n"
"v_mov_b32 v62, s99\n"
"v_add_f32 v241, v241, v196\n"
"v_mfma_f32_16x16x32_bf16 v[200:203], a[132:135], a[36:39], v[200:203]\n"
"s_add_u32 s59, s57, 0\n"
"buffer_load_dwordx4 v252, s[44:47], s59 offen lds\n"
"v_exp_f32 v197, v197\n"
"v_mfma_f32_16x16x32_bf16 v[200:203], a[136:139], a[40:43], v[200:203]\n"
"v_add_f32 v241, v241, v197\n"
"v_cvt_pk_bf16_f32 v210, v196, v197\n"
"v_mfma_f32_16x16x32_bf16 v[200:203], a[140:143], a[44:47], v[200:203]\n"
"buffer_load_dwordx4 v253, s[44:47], s59 offen offset:2048 lds\n"
"v_exp_f32 v198, v198\n"
"v_mfma_f32_16x16x32_bf16 v[204:207], a[144:147], a[32:35], v[224:227]\n"
"ds_read_b128 a[120:123], v246 offset:5120\n"
"v_mov_b32 v63, s99\n"
"v_add_f32 v241, v241, v198\n"
"v_mfma_f32_16x16x32_bf16 v[204:207], a[148:151], a[36:39], v[204:207]\n"
"v_exp_f32 v199, v199\n"
"v_mfma_f32_16x16x32_bf16 v[204:207], a[152:155], a[40:43], v[204:207]\n"
"s_add_u32 m0, s58, 1024\n"
"v_add_f32 v241, v241, v199\n"
"v_cvt_pk_bf16_f32 v211, v198, v199\n"
"v_mfma_f32_16x16x32_bf16 v[204:207], a[156:159], a[44:47], v[204:207]\n"
"s_add_u32 s59, s57, 128\n"
"buffer_load_dwordx4 v252, s[44:47], s59 offen lds\n"
"v_exp_f32 v200, v200\n"
"v_mfma_f32_16x16x32_bf16 v[32:35], a[192:195], v[208:211], v[60:63]\n"
"ds_read_b128 a[124:127], v246 offset:5632\n"
"v_mov_b32 v92, s99\n"
"v_mov_b32 v242, v200\n"
"v_mfma_f32_16x16x32_bf16 v[36:39], a[196:199], v[208:211], v[60:63]\n"
"buffer_load_dwordx4 v253, s[44:47], s59 offen offset:2048 lds\n"
"v_exp_f32 v201, v201\n"
"v_mfma_f32_16x16x32_bf16 v[40:43], a[200:203], v[208:211], v[60:63]\n"
"v_add_f32 v242, v242, v201\n"
"v_cvt_pk_bf16_f32 v212, v200, v201\n"
"v_mfma_f32_16x16x32_bf16 v[44:47], a[204:207], v[208:211], v[60:63]\n"
"v_exp_f32 v202, v202\n"
"v_mfma_f32_16x16x32_bf16 v[48:51], a[208:211], v[208:211], v[60:63]\n"
"s_mov_b32 m0, 25600\n"
"ds_read_b64_tr_b16 a[160:161], v247 offset:32768\n"
"v_mov_b32 v93, s99\n"
"v_add_f32 v242, v242, v202\n"
"v_mfma_f32_16x16x32_bf16 v[52:55], a[212:215], v[208:211], v[60:63]\n"
"s_add_u32 s59, s56, 128\n"
"buffer_load_dwordx4 v252, s[36:39], s59 offen lds\n"
"v_exp_f32 v203, v203\n"
"v_mfma_f32_16x16x32_bf16 v[56:59], a[216:219], v[208:211], v[60:63]\n"
"ds_read_b64_tr_b16 a[162:163], v247 offset:36864\n"
"v_add_f32 v242, v242, v203\n"
"v_cvt_pk_bf16_f32 v213, v202, v203\n"
"v_mfma_f32_16x16x32_bf16 v[60:63], a[220:223], v[208:211], v[60:63]\n"
"buffer_load_dwordx4 v253, s[36:39], s59 offen offset:2048 lds\n"
"v_exp_f32 v204, v204\n"
"v_cvt_pk_bf16_f32 v128, v128, v129\n"
"v_cvt_pk_bf16_f32 v130, v130, v131\n"
"v_cvt_pk_bf16_f32 v132, v132, v133\n"
"v_cvt_pk_bf16_f32 v134, v134, v135\n"
"v_accvgpr_write_b32 a64, v128\n"
"v_accvgpr_write_b32 a65, v130\n"
"v_accvgpr_write_b32 a66, v132\n"
"v_accvgpr_write_b32 a67, v134\n"
"v_cvt_pk_bf16_f32 v136, v136, v137\n"
"v_cvt_pk_bf16_f32 v138, v138, v139\n"
"v_cvt_pk_bf16_f32 v140, v140, v141\n"
"v_cvt_pk_bf16_f32 v142, v142, v143\n"
"v_accvgpr_write_b32 a68, v136\n"
"v_accvgpr_write_b32 a69, v138\n"
"v_accvgpr_write_b32 a70, v140\n"
"v_accvgpr_write_b32 a71, v142\n"
"v_cvt_pk_bf16_f32 v144, v144, v145\n"
"v_cvt_pk_bf16_f32 v146, v146, v147\n"
"v_cvt_pk_bf16_f32 v148, v148, v149\n"
"v_cvt_pk_bf16_f32 v150, v150, v151\n"
"v_accvgpr_write_b32 a72, v144\n"
"v_accvgpr_write_b32 a73, v146\n"
"v_accvgpr_write_b32 a74, v148\n"
"v_accvgpr_write_b32 a75, v150\n"
"v_cvt_pk_bf16_f32 v152, v152, v153\n"
"v_cvt_pk_bf16_f32 v154, v154, v155\n"
"v_cvt_pk_bf16_f32 v156, v156, v157\n"
"v_cvt_pk_bf16_f32 v158, v158, v159\n"
"v_accvgpr_write_b32 a76, v152\n"
"v_accvgpr_write_b32 a77, v154\n"
"v_accvgpr_write_b32 a78, v156\n"
"v_accvgpr_write_b32 a79, v158\n"
"v_mfma_f32_16x16x32_bf16 v[232:235], a[240:243], a[64:67], 0\n"
"v_sub_f32 v228, s71, v228\n"
"v_mfma_f32_16x16x32_bf16 v[232:235], a[244:247], a[68:71], v[232:235]\n"
"v_sub_f32 v229, s71, v229\n"
"v_mfma_f32_16x16x32_bf16 v[232:235], a[248:251], a[72:75], v[232:235]\n"
"v_sub_f32 v230, s71, v230\n"
"v_mfma_f32_16x16x32_bf16 v[232:235], a[252:255], a[76:79], v[232:235]\n"
"v_sub_f32 v231, s71, v231\n"
"s_nop 1\n"
"v_mfma_f32_16x16x32_bf16 v[192:195], a[128:131], a[48:51], v[228:231]\n"
"ds_read2_b64 a[224:227], v161 offset0:0 offset1:2\n"
"ds_read_b64_tr_b16 a[164:165], v248 offset:32768\n"
"v_mov_b32 v94, s99\n"
"v_add_f32 v242, v242, v204\n"
"v_mfma_f32_16x16x32_bf16 v[192:195], a[132:135], a[52:55], v[192:195]\n"
"ds_read2_b64 a[228:231], v161 offset0:136 offset1:138\n"
"v_exp_f32 v205, v205\n"
"v_mfma_f32_16x16x32_bf16 v[192:195], a[136:139], a[56:59], v[192:195]\n"
"s_mov_b32 m0, 28672\n"
"ds_read2_b64 a[232:235], v164 offset0:0 offset1:2\n"
"v_add_f32 v242, v242, v205\n"
"v_cvt_pk_bf16_f32 v214, v204, v205\n"
"v_mfma_f32_16x16x32_bf16 v[192:195], a[140:143], a[60:63], v[192:195]\n"
"s_add_u32 s59, s56, 4096\n"
"ds_read2_b64 a[236:239], v164 offset0:136 offset1:138\n"
"buffer_load_dwordx4 v252, s[36:39], s59 offen lds\n"
"v_exp_f32 v206, v206\n"
"v_mfma_f32_16x16x32_bf16 v[196:199], a[144:147], a[48:51], v[228:231]\n"
"ds_read_b64_tr_b16 a[166:167], v248 offset:36864\n"
"v_mov_b32 v95, s99\n"
"v_add_f32 v242, v242, v206\n"
"v_mfma_f32_16x16x32_bf16 v[196:199], a[148:151], a[52:55], v[196:199]\n"
"buffer_load_dwordx4 v253, s[36:39], s59 offen offset:2048 lds\n"
"v_exp_f32 v207, v207\n"
"v_mfma_f32_16x16x32_bf16 v[196:199], a[152:155], a[56:59], v[196:199]\n"
"v_add_f32 v242, v242, v207\n"
"v_cvt_pk_bf16_f32 v215, v206, v207\n"
"v_mfma_f32_16x16x32_bf16 v[196:199], a[156:159], a[60:63], v[196:199]\n"
"v_exp_f32 v192, v192\n"
"v_mfma_f32_16x16x32_bf16 v[64:67], a[192:195], v[212:215], v[92:95]\n"
"s_mov_b32 m0, 29696\n"
"ds_read_b64_tr_b16 a[168:169], v247 offset:33280\n"
"v_mov_b32 v124, s99\n"
"v_mov_b32 v243, v192\n"
"v_mfma_f32_16x16x32_bf16 v[68:71], a[196:199], v[212:215], v[92:95]\n"
"s_add_u32 s59, s56, 4224\n"
"buffer_load_dwordx4 v252, s[36:39], s59 offen lds\n"
"v_exp_f32 v193, v193\n"
"v_mfma_f32_16x16x32_bf16 v[72:75], a[200:203], v[212:215], v[92:95]\n"
"v_add_f32 v243, v243, v193\n"
"v_cvt_pk_bf16_f32 v208, v192, v193\n"
"v_mfma_f32_16x16x32_bf16 v[76:79], a[204:207], v[212:215], v[92:95]\n"
"v_exp_f32 v194, v194\n"
"v_mfma_f32_16x16x32_bf16 v[80:83], a[208:211], v[212:215], v[92:95]\n"
"ds_read_b64_tr_b16 a[170:171], v247 offset:37376\n"
"v_mov_b32 v125, s99\n"
"v_add_f32 v243, v243, v194\n"
"v_mfma_f32_16x16x32_bf16 v[84:87], a[212:215], v[212:215], v[92:95]\n"
"buffer_load_dwordx4 v253, s[36:39], s59 offen offset:2048 lds\n"
"v_exp_f32 v195, v195\n"
"v_mfma_f32_16x16x32_bf16 v[88:91], a[216:219], v[212:215], v[92:95]\n"
"s_mov_b32 m0, 57344\n"
"v_add_f32 v243, v243, v195\n"
"v_cvt_pk_bf16_f32 v209, v194, v195\n"
"v_mfma_f32_16x16x32_bf16 v[92:95], a[220:223], v[212:215], v[92:95]\n"
"s_add_u32 s59, s56, 0\n"
"buffer_load_dwordx4 v252, s[40:43], s59 offen lds\n"
"v_exp_f32 v196, v196\n"
"v_sub_f32 v232, s71, v232\n"
"v_sub_f32 v233, s71, v233\n"
"v_sub_f32 v234, s71, v234\n"
"v_sub_f32 v235, s71, v235\n"
"s_nop 1\n"
"v_mfma_f32_16x16x32_bf16 v[200:203], a[128:131], a[64:67], v[232:235]\n"
"ds_read_b64_tr_b16 a[172:173], v248 offset:33280\n"
"v_mov_b32 v126, s99\n"
"v_add_f32 v243, v243, v196\n"
"v_mfma_f32_16x16x32_bf16 v[200:203], a[132:135], a[68:71], v[200:203]\n"
"v_exp_f32 v197, v197\n"
"v_mfma_f32_16x16x32_bf16 v[200:203], a[136:139], a[72:75], v[200:203]\n"
"ds_read_b64_tr_b16 a[174:175], v248 offset:37376\n"
"v_add_f32 v243, v243, v197\n"
"v_cvt_pk_bf16_f32 v210, v196, v197\n"
"v_mfma_f32_16x16x32_bf16 v[200:203], a[140:143], a[76:79], v[200:203]\n"
"buffer_load_dwordx4 v252, s[40:43], s59 offen offset:2048 lds\n"
"v_exp_f32 v198, v198\n"
"v_mfma_f32_16x16x32_bf16 v[204:207], a[144:147], a[64:67], v[232:235]\n"
"s_mov_b32 m0, 58368\n"
"ds_read_b64_tr_b16 a[176:177], v247 offset:33792\n"
"v_mov_b32 v127, s99\n"
"v_add_f32 v243, v243, v198\n"
"v_mfma_f32_16x16x32_bf16 v[204:207], a[148:151], a[68:71], v[204:207]\n"
"s_add_u32 s59, s56, 128\n"
"buffer_load_dwordx4 v252, s[40:43], s59 offen lds\n"
"v_exp_f32 v199, v199\n"
"v_mfma_f32_16x16x32_bf16 v[204:207], a[152:155], a[72:75], v[204:207]\n"
"v_add_f32 v243, v243, v199\n"
"v_cvt_pk_bf16_f32 v211, v198, v199\n"
"v_mfma_f32_16x16x32_bf16 v[204:207], a[156:159], a[76:79], v[204:207]\n"
"v_exp_f32 v200, v200\n"
"v_mfma_f32_16x16x32_bf16 v[96:99], a[192:195], v[208:211], v[124:127]\n"
"ds_read_b64_tr_b16 a[178:179], v247 offset:37888\n"
"v_mov_b32 v156, s99\n"
"v_mov_b32 v244, v200\n"
"v_mfma_f32_16x16x32_bf16 v[100:103], a[196:199], v[208:211], v[124:127]\n"
"buffer_load_dwordx4 v252, s[40:43], s59 offen offset:2048 lds\n"
"v_exp_f32 v201, v201\n"
"v_mfma_f32_16x16x32_bf16 v[104:107], a[200:203], v[208:211], v[124:127]\n"
"s_mov_b32 m0, 61440\n"
"v_add_f32 v244, v244, v201\n"
"v_cvt_pk_bf16_f32 v212, v200, v201\n"
"v_mfma_f32_16x16x32_bf16 v[108:111], a[204:207], v[208:211], v[124:127]\n"
"s_add_u32 s59, s56, 4096\n"
"buffer_load_dwordx4 v252, s[40:43], s59 offen lds\n"
"v_exp_f32 v202, v202\n"
"v_mfma_f32_16x16x32_bf16 v[112:115], a[208:211], v[208:211], v[124:127]\n"
"ds_read_b64_tr_b16 a[180:181], v248 offset:33792\n"
"v_mov_b32 v157, s99\n"
"v_add_f32 v244, v244, v202\n"
"v_mfma_f32_16x16x32_bf16 v[116:119], a[212:215], v[208:211], v[124:127]\n"
"v_exp_f32 v203, v203\n"
"v_mfma_f32_16x16x32_bf16 v[120:123], a[216:219], v[208:211], v[124:127]\n"
"v_add_f32 v244, v244, v203\n"
"v_cvt_pk_bf16_f32 v213, v202, v203\n"
"v_mfma_f32_16x16x32_bf16 v[124:127], a[220:223], v[208:211], v[124:127]\n"
"s_waitcnt lgkmcnt(2)\n"
"buffer_load_dwordx4 v252, s[40:43], s59 offen offset:2048 lds\n"
"v_exp_f32 v204, v204\n"
"v_mfma_f32_16x16x32_bf16 v[192:195], a[96:99], a[0:3], v[216:219]\n"
"s_mov_b32 m0, 62464\n"
"ds_read_b64_tr_b16 a[182:183], v248 offset:37888\n"
"v_mov_b32 v158, s99\n"
"v_add_f32 v244, v244, v204\n"
"v_mfma_f32_16x16x32_bf16 v[192:195], a[100:103], a[4:7], v[192:195]\n"
"s_add_u32 s59, s56, 4224\n"
"buffer_load_dwordx4 v252, s[40:43], s59 offen lds\n"
"v_exp_f32 v205, v205\n"
"v_mfma_f32_16x16x32_bf16 v[192:195], a[104:107], a[8:11], v[192:195]\n"
"v_add_f32 v244, v244, v205\n"
"v_cvt_pk_bf16_f32 v214, v204, v205\n"
"v_mfma_f32_16x16x32_bf16 v[192:195], a[108:111], a[12:15], v[192:195]\n"
"v_exp_f32 v206, v206\n"
"v_mfma_f32_16x16x32_bf16 v[196:199], a[112:115], a[0:3], v[216:219]\n"
"ds_read_b64_tr_b16 a[184:185], v247 offset:34304\n"
"v_mov_b32 v159, s99\n"
"v_add_f32 v244, v244, v206\n"
"v_mfma_f32_16x16x32_bf16 v[196:199], a[116:119], a[4:7], v[196:199]\n"
"buffer_load_dwordx4 v252, s[40:43], s59 offen offset:2048 lds\n"
"v_exp_f32 v207, v207\n"
"v_mfma_f32_16x16x32_bf16 v[196:199], a[120:123], a[8:11], v[196:199]\n"
"s_add_u32 s58, s58, s69\n"
"ds_read_b64_tr_b16 a[186:187], v247 offset:38400\n"
"v_add_f32 v244, v244, v207\n"
"v_cvt_pk_bf16_f32 v215, v206, v207\n"
"v_mfma_f32_16x16x32_bf16 v[196:199], a[124:127], a[12:15], v[196:199]\n"
"s_add_u32 s57, s57, 4096\n"
"v_exp_f32 v192, v192\n"
"v_mfma_f32_16x16x32_bf16 v[128:131], a[192:195], v[212:215], v[156:159]\n"
"ds_read_b64_tr_b16 a[188:189], v248 offset:34304\n"
"v_add_f32 v240, v240, v192\n"
"v_mfma_f32_16x16x32_bf16 v[132:135], a[196:199], v[212:215], v[156:159]\n"
"v_exp_f32 v193, v193\n"
"v_mfma_f32_16x16x32_bf16 v[136:139], a[200:203], v[212:215], v[156:159]\n"
"v_add_f32 v240, v240, v193\n"
"v_cvt_pk_bf16_f32 v208, v192, v193\n"
"v_mfma_f32_16x16x32_bf16 v[140:143], a[204:207], v[212:215], v[156:159]\n"
"v_exp_f32 v194, v194\n"
"v_mfma_f32_16x16x32_bf16 v[144:147], a[208:211], v[212:215], v[156:159]\n"
"ds_read_b64_tr_b16 a[190:191], v248 offset:38400\n"
"v_add_f32 v240, v240, v194\n"
"v_mfma_f32_16x16x32_bf16 v[148:151], a[212:215], v[212:215], v[156:159]\n"
"v_exp_f32 v195, v195\n"
"v_mfma_f32_16x16x32_bf16 v[152:155], a[216:219], v[212:215], v[156:159]\n"
"v_add_f32 v240, v240, v195\n"
"v_cvt_pk_bf16_f32 v209, v194, v195\n"
"v_mfma_f32_16x16x32_bf16 v[156:159], a[220:223], v[212:215], v[156:159]\n"
"s_branch LMB2%=\n"
"Lend%=:\n"
"s_waitcnt vmcnt(0) lgkmcnt(0)\n"
"s_mov_b32 %0, s55\n"
    : "=s"(flags)
    : "s"(ka), "s"(w), "s"(b)
    : "v0","v1","v2","v3","v4","v5","v6","v7","v8","v9","v10","v11","v12","v13","v14","v15","v16","v17","v18","v19","v20","v21","v22","v23","v24","v25","v26","v27","v28","v29","v30","v31","v32","v33","v34","v35","v36","v37","v38","v39","v40","v41","v42","v43","v44","v45","v46","v47","v48","v49","v50","v51","v52","v53","v54","v55","v56","v57","v58","v59","v60","v61","v62","v63","v64","v65","v66","v67","v68","v69","v70","v71","v72","v73","v74","v75","v76","v77","v78","v79","v80","v81","v82","v83","v84","v85","v86","v87","v88","v89","v90","v91","v92","v93","v94","v95","v96","v97","v98","v99","v100","v101","v102","v103","v104","v105","v106","v107","v108","v109","v110","v111","v112","v113","v114","v115","v116","v117","v118","v119","v120","v121","v122","v123","v124","v125","v126","v127","v128","v129","v130","v131","v132","v133","v134","v135","v136","v137","v138","v139","v140","v141","v142","v143","v144","v145","v146","v147","v148","v149","v150","v151","v152","v153","v154","v155","v156","v157","v158","v159","v160","v161","v162","v163","v164","v165","v166","v167","v168","v169","v170","v171","v172","v173","v174","v175","v176","v177","v178","v179","v180","v181","v182","v183","v184","v185","v186","v187","v188","v189","v190","v191","v192","v193","v194","v195","v196","v197","v198","v199","v200","v201","v202","v203","v204","v205","v206","v207","v208","v209","v210","v211","v212","v213","v214","v215","v216","v217","v218","v219","v220","v221","v222","v223","v224","v225","v226","v227","v228","v229","v230","v231","v232","v233","v234","v235","v236","v237","v238","v239","v240","v241","v242","v243","v244","v245","v246","v247","v248","v249","v250","v251","v252","v253","v254","v255","a0","a1","a2","a3","a4","a5","a6","a7","a8","a9","a10","a11","a12","a13","a14","a15","a16","a17","a18","a19","a20","a21","a22","a23","a24","a25","a26","a27","a28","a29","a30","a31","a32","a33","a34","a35","a36","a37","a38","a39","a40","a41","a42","a43","a44","a45","a46","a47","a48","a49","a50","a51","a52","a53","a54","a55","a56","a57","a58","a59","a60","a61","a62","a63","a64","a65","a66","a67","a68","a69","a70","a71","a72","a73","a74","a75","a76","a77","a78","a79","a80","a81","a82","a83","a84","a85","a86","a87","a88","a89","a90","a91","a92","a93","a94","a95","a96","a97","a98","a99","a100","a101","a102","a103","a104","a105","a106","a107","a108","a109","a110","a111","a112","a113","a114","a115","a116","a117","a118","a119","a120","a121","a122","a123","a124","a125","a126","a127","a128","a129","a130","a131","a132","a133","a134","a135","a136","a137","a138","a139","a140","a141","a142","a143","a144","a145","a146","a147","a148","a149","a150","a151","a152","a153","a154","a155","a156","a157","a158","a159","a160","a161","a162","a163","a164","a165","a166","a167","a168","a169","a170","a171","a172","a173","a174","a175","a176","a177","a178","a179","a180","a181","a182","a183","a184","a185","a186","a187","a188","a189","a190","a191","a192","a193","a194","a195","a196","a197","a198","a199","a200","a201","a202","a203","a204","a205","a206","a207","a208","a209","a210","a211","a212","a213","a214","a215","a216","a217","a218","a219","a220","a221","a222","a223","a224","a225","a226","a227","a228","a229","a230","a231","a232","a233","a234","a235","a236","a237","a238","a239","a240","a241","a242","a243","a244","a245","a246","a247","a248","a249","a250","a251","a252","a253","a254","a255","s36","s37","s38","s39","s40","s41","s42","s43","s44","s45","s46","s47","s48","s49","s50","s51","s52","s53","s54","s55","s56","s57","s58","s59","s60","s61","s62","s63","s64","s65","s66","s67","s68","s69","s70","s71","s72","s73","s74","s75","s76","s77","s78","s79","s80","s81","s82","s83","s84","s85","s86","s87","s88","s89","s90","s91","s92","s93","s94","s95","s96","s97","s98","s99","memory","scc","vcc");
  if (flags) {
    asm volatile("" : "+s"(ka));
    unsigned short* const* arg = (unsigned short* const*)ka;
    const unsigned short *Q = arg[0], *K = arg[1], *V = arg[2];
    unsigned short* O = arg[3];
    const unsigned pair = (b & 7u) + 8u * (b >> 4), half = (b >> 3) & 1u;
    const size_t base = (size_t)pair * (2 * S * D);
    const int nq = w == 3 ? 5 : 6;
    const int lane = __builtin_amdgcn_mbcnt_hi(-1, __builtin_amdgcn_mbcnt_lo(-1, 0));
#pragma unroll 1
    for (int j = 0; j < 2 * nq; ++j) {
      if (!((flags >> (2 * nq - 1 - j)) & 1u)) continue;
      const int ps = j >= nq, i = j - ps * nq;
      const size_t hb = base + (size_t)ps * (S * D);
      const size_t rb = hb + (size_t)(half * (S / 2) + (6 * w + i) * 16) * D;
      safe_tile(Q + rb, K + hb, V + hb, O + rb, lane);
    }
  }
}

extern "C" void kernel_launch(void* const* d_in, const int* in_sizes, int n_in,
                              void* d_out, int out_size, void* d_ws, size_t ws_size,
                              hipStream_t stream) {
  attn_fwd<<<256, 256, 0, stream>>>((const unsigned short*)d_in[0], (const unsigned short*)d_in[1],
                                    (const unsigned short*)d_in[2], (unsigned short*)d_out);
}
```
